```python
import torch

def _softmax(x, axis=-1):
    return torch.softmax(x, dim=axis)
def _gelu(x):
    return torch.nn.functional.gelu(x, approximate="tanh")
def _silu(x):
    return torch.nn.functional.silu(x)
def _sigmoid(x):
    return torch.sigmoid(x)
def _layernorm(x, axis=-1, eps=1e-5):
    m = x.mean(dim=axis, keepdim=True)
    v = x.var(dim=axis, keepdim=True, unbiased=False)
    return (x - m) / torch.sqrt(v + eps)
def _scatter_add(tgt, idx, src, axis=0):
    return tgt.clone().index_add_(axis, idx, src)
def _segment_sum(src, seg, n):
    out = torch.zeros((n,) + src.shape[1:], dtype=src.dtype)
    return out.index_add_(0, seg, src)
def _pad(x, pw):
    flat = tuple(v for pair in reversed(pw) for v in pair)
    return torch.nn.functional.pad(x, flat)

kernel_name = 'parallel_sum__ssm_conv_h3__dense_activation_gelu__prompt'

def setup_inputs():

    _w = (lambda a: (lambda t: t.to(torch.bfloat16) if t.is_floating_point() else t)(torch.as_tensor(a)))
    def _A_setup():
        B, S, D = 1, 2048, 576
        ks = 4
        return {
            "q": _w((lambda *s: torch.randn(*s, dtype=torch.bfloat16) * 0.05)(B, S, D)),
            "k": _w((lambda *s: torch.randn(*s, dtype=torch.bfloat16) * 0.2)(B, S, D)),
            "v": _w((lambda *s: torch.randn(*s, dtype=torch.bfloat16) * 0.2)(B, S, D)),
            "shift_k": _w((lambda *s: torch.randn(*s, dtype=torch.bfloat16) * 0.2)(D, ks) * 0.5),
            "ssm_k": _w((lambda *s: torch.randn(*s, dtype=torch.bfloat16) * 0.8)(D, S) * (1.0 / S ** 0.5)),
            "ssm_D": _w((lambda *s: torch.randn(*s, dtype=torch.bfloat16) * 0.2)(D)),
        }
    _A_in = _A_setup()

    def _B_setup():
        B, S, D, F = 1, 2048, 576, 4608
        return {
            "x": _w((lambda *s: torch.randn(*s, dtype=torch.bfloat16) * 0.05)(B, S, D)),
            "w1": _w((lambda *s: torch.randn(*s, dtype=torch.bfloat16) * 0.4)(D, F) * (D ** -0.5)),
            "b1": _w((lambda *s: torch.randn(*s, dtype=torch.bfloat16) * 12.8)(F) * 0.01),
            "w2": _w((lambda *s: torch.randn(*s, dtype=torch.bfloat16) * 0.2)(F, D) * (F ** -0.5)),
            "b2": _w((lambda *s: torch.randn(*s, dtype=torch.bfloat16) * 1.6)(D) * 0.01),
        }
    _B_in = _B_setup()

    return {
        "A_q": _A_in["q"],
        "A_k": _A_in["k"],
        "A_v": _A_in["v"],
        "A_shift_k": _A_in["shift_k"],
        "A_ssm_k": _A_in["ssm_k"],
        "A_ssm_D": _A_in["ssm_D"],
        "B_x": _B_in["x"],
        "B_w1": _B_in["w1"],
        "B_b1": _B_in["b1"],
        "B_w2": _B_in["w2"],
        "B_b2": _B_in["b2"],
    }

def reference(A_q, A_k, A_v, A_shift_k, A_ssm_k, A_ssm_D, B_x, B_w1, B_b1, B_w2, B_b2):
    def _A_ref(q, k, v, shift_k, ssm_k, ssm_D):
        B, S, D = q.shape
        ks = shift_k.shape[-1]
        k_pad = _pad(k, ((0, 0), (ks - 1, 0), (0, 0)))
        k_shift = torch.zeros_like(k)
        for t in range(ks):
            k_shift = k_shift + k_pad[:, t:t + S, :] * shift_k[:, ks - 1 - t][None, None, :]
        kv = k_shift * v
        L = 2 * S
        kv_pad = _pad(kv, ((0, 0), (0, S), (0, 0)))
        kf_pad = _pad(ssm_k, ((0, 0), (0, S)))
        kv_f = (lambda x, axes=(-2,-1): torch.fft.rfft2(x.float(), dim=axes))(kv_pad[:, None, :, :], axes=(1, 2))
        kf_f = (lambda x, axes=(-2,-1): torch.fft.rfft2(x.float(), dim=axes))(kf_pad[:, None, :], axes=(1, 2))
        kf_f = (lambda x, *d: x.permute(*d))(kf_f, 1, 2, 0)
        y_f = kv_f * kf_f[None, :, :, :]
        y = (lambda x, s, axes=(-2,-1): torch.fft.irfft2(x, s=s, dim=axes).to(torch.bfloat16))(y_f, (1, L), axes=(1, 2))[:, 0, :S, :]
        y = y + kv * ssm_D[None, None, :]
        return q * y

    def _B_ref(x, w1, b1, w2, b2):
        h = torch.einsum("bsd,df->bsf", x, w1) + b1
        h = _gelu(h)
        return torch.einsum("bsf,fd->bsd", h, w2) + b2

    x = A_q
    xn = _layernorm(x, axis=-1)
    return x + _A_ref(xn, A_k, A_v, A_shift_k, A_ssm_k, A_ssm_D) + _B_ref(xn, B_w1, B_b1, B_w2, B_b2)
```

```cpp
#define Z_PAD 256
#define SLAB_PAD 4352
#include <hip/hip_runtime.h>
#include <hip/hip_bf16.h>

typedef unsigned u32;
typedef unsigned short u16;
typedef unsigned long long u64;
typedef unsigned u32x2 __attribute__((ext_vector_type(2)));
typedef unsigned u32x4 __attribute__((ext_vector_type(4)));
typedef float f32x4 __attribute__((ext_vector_type(4)));
typedef float f32x2 __attribute__((ext_vector_type(2)));
typedef __bf16 bf16x8 __attribute__((ext_vector_type(8)));
typedef __bf16 bf16x2 __attribute__((ext_vector_type(2)));
typedef __bf16 bf16x4 __attribute__((ext_vector_type(4)));
#define LAS __attribute__((address_space(3)))
#define DI __device__ __forceinline__

constexpr int S_ = 2048, D_ = 576, F_ = 4608;
constexpr int NSP = 12, FC = F_ / NSP;
constexpr int NMLP = 16 * NSP;
constexpr int NCONV = 64;
constexpr u32 SLAB = 147456 + SLAB_PAD;
constexpr u32 ZROW = S_ * 2 + Z_PAD;
constexpr size_t PART_BYTES = (size_t)16 * NSP * SLAB;
constexpr size_t WS_PART = 0;
constexpr size_t WS_Z = PART_BYTES;
constexpr size_t WS_STATS = WS_Z + (size_t)D_ * ZROW;
constexpr size_t WS_CTRL = WS_STATS + 16384;
constexpr u32 CTRL_BYTES = 327680, CT_SEEN = 4096, CT_ZF = 36864, CT_PF = 65536;
#define WS_SHIFT 0
constexpr size_t WS_DBG = (64u << 20) - WS_SHIFT % (64u << 20);
#define STAMP(i)

DI float bf2f(u32 h) { return __builtin_bit_cast(float, h << 16); }
DI float bflo(u32 w) { return __builtin_bit_cast(float, w << 16); }
DI float bfhi(u32 w) { return __builtin_bit_cast(float, w & 0xffff0000u); }
DI u32 pk(float a, float b) { f32x2 v = {a, b}; bf16x2 r = __builtin_convertvector(v, bf16x2); return __builtin_bit_cast(u32, r); }
DI float rbf(float a) { return bflo(pk(a, a)); }
DI __amdgpu_buffer_rsrc_t mk(const void* p, u32 n) { return __builtin_amdgcn_make_buffer_rsrc((void*)p, 0, n, 0x00020000); }

#define POLL_SLEEP 1
constexpr int SC1 = 16;
#define PAUX 16
constexpr int PLD = PAUX;

DI void put_flag(__amdgpu_buffer_rsrc_t rc, u32 off, u32 tag, int tid) {
  if (tid < 16) { const u32x2 f = {tag, ~tag}; __builtin_amdgcn_raw_buffer_store_b64(f, rc, off + 8u * (u32)tid, 0, SC1); }
}
DI void wait_lines(__amdgpu_buffer_rsrc_t rc, u32 o, u32 tag) {
  for (int it = 0; it < (1 << 14); ++it) {
    const u32x2 v = __builtin_amdgcn_raw_buffer_load_b64(rc, o, 0, SC1);
    if (__all(v[0] == tag && v[1] == ~tag)) break;
    __builtin_amdgcn_s_sleep(POLL_SLEEP);
  }
  asm volatile("" ::: "memory");
}
DI u32 unit_flag(int rb, int R, int lane) {
  return lane < 7 ? CT_ZF + 128u * (u32)(7 * R + lane) : CT_PF + 128u * (u32)(12 * (rb * 9 + R) + (lane < 7 + NSP ? lane - 7 : 0));
}

DI void wait_lines_shared(bool leader, __amdgpu_buffer_rsrc_t rc, u32 o, u32 tag, volatile LAS u32* word) {
  if (leader) {
    wait_lines(rc, o, tag);
    *word = tag;
    asm volatile("s_waitcnt lgkmcnt(0)" ::: "memory");
  } else {
    for (int it = 0; it < (1 << 20) && *word != tag; ++it) __builtin_amdgcn_s_sleep(4);
  }
  asm volatile("" ::: "memory");
}

DI void combine4(float (&o)[4], const float* acc, u32x2 zz, u32x2 xx, u32x2 ms, u32x2 bb) {
  const float z[4] = {bflo(zz[0]), bfhi(zz[0]), bflo(zz[1]), bfhi(zz[1])};
  const float xf[4] = {bflo(xx[0]), bfhi(xx[0]), bflo(xx[1]), bfhi(xx[1])};
  const float bf[4] = {bflo(bb[0]), bfhi(bb[0]), bflo(bb[1]), bfhi(bb[1])};
  const u32 m0 = ms[0], m1 = ms[1];
  const float mean = __builtin_bit_cast(float, m0), rstd = __builtin_bit_cast(float, m1);
#pragma unroll
  for (int i = 0; i < 4; ++i) o[i] = fmaf((xf[i] - mean) * rstd, z[i], xf[i]) + (acc[i] + bf[i]);
}
DI u32x2 z_tr(LAS char* scr, int c, int l) {
  const bf16x4 zt = __builtin_amdgcn_ds_read_tr16_b64_v4bf16((LAS bf16x4*)(scr + (4 * (l >> 4) + ((l >> 2) & 3)) * 64 + c * 32 + 8 * (l & 3)));
  return __builtin_bit_cast(u32x2, zt);
}

DI float add_lo(float acc, u32 v, u32 sel) { return __builtin_amdgcn_fdot2_f32_bf16(__builtin_bit_cast(bf16x2, v), __builtin_bit_cast(bf16x2, sel), acc, false); }
struct Sel { u32 lo, hi; };
DI Sel make_sel() { Sel s = {0x00003f80u, 0x3f800000u}; asm volatile("" : "+s"(s.lo), "+s"(s.hi)); return s; }
DI void sum_slabs(float (&acc)[8], const u32x4 (&v)[NSP], Sel sel) {
#pragma unroll
  for (int i = 0; i < 8; ++i) acc[i] = 0.f;
#pragma unroll
  for (int s = 0; s < NSP; ++s)
#pragma unroll
    for (int i = 0; i < 4; ++i) { const u32 d = v[s][i]; acc[2 * i] = add_lo(acc[2 * i], d, sel.lo); acc[2 * i + 1] = add_lo(acc[2 * i + 1], d, sel.hi); }
}
template <int NU>
DI void reduce_units(int rb, int tg, int R0, int j, u32 tag, const u16* x, const u16* b2, char* ws, u16* out, LAS char* scr, volatile LAS u32* sig, int l) {
  const int col = l & 15, kg = l >> 4, tok0 = 128 * rb + 32 * tg;
  const __amdgpu_buffer_rsrc_t rc = mk(ws + WS_CTRL, CTRL_BYTES), rp = mk(ws + WS_PART, (u32)PART_BYTES);
  const __amdgpu_buffer_rsrc_t rz = mk(ws + WS_Z, D_ * ZROW), rs = mk(ws + WS_STATS, S_ * 8), ro = mk(out, S_ * D_ * 2);
  const u32 pbase = (u32)(rb * NSP) * SLAB + (u32)(tg * 36864 + (4 * R0 + j) * 1024 + l * 16);
  u32x2 bb[NU], xx[NU][2];
#pragma unroll
  for (int u = 0; u < NU; ++u) {
    const int d0 = 16 * (4 * (R0 + 2 * u) + j) + 4 * kg;
    bb[u] = *(const u32x2*)(b2 + d0);
#pragma unroll
    for (int c = 0; c < 2; ++c) xx[u][c] = *(const u32x2*)(x + (size_t)(tok0 + 16 * c + col) * D_ + d0);
  }
  float acc[NU][8];
  const Sel sel = make_sel();
  {
    wait_lines_shared(j == 0, rc, CT_PF + 128u * (u32)(12 * (rb * 9 + R0 + (l >= NSP && l < 2 * NSP ? 2 : 0)) + (l < 2 * NSP ? l % NSP : 0)), tag, sig);
    u32x4 v[2][NSP];
#pragma unroll
    for (int u = 0; u < 2; ++u)
#pragma unroll
      for (int s = 0; s < NSP; ++s) v[u][s] = __builtin_amdgcn_raw_buffer_load_b128(rp, pbase + (u32)s * SLAB + (u32)(u * 8192), 0, PLD);
    sum_slabs(acc[0], v[0], sel);
    sum_slabs(acc[1], v[1], sel);
  }
  STAMP(8);
  if (j == 0) wait_lines(rc, CT_ZF + 128u * (u32)(l < 7 * NU ? 7 * (R0 + 2 * (l / 7)) + l % 7 : 7 * R0), tag);
  wait_lines_shared(j == 0, rc, CT_PF + 128u * (u32)(12 * (rb * 9 + R0 + 4 + 2 * (l < NSP * (NU - 2) ? l / NSP : 0)) + l % NSP), tag, sig + 1);
  STAMP(9);
  u32x4 v[NU - 2][NSP], zz[NU];
  u32x2 ms[2];
#pragma unroll
  for (int u = 2; u < NU; ++u)
#pragma unroll
    for (int s = 0; s < NSP; ++s) v[u - 2][s] = __builtin_amdgcn_raw_buffer_load_b128(rp, pbase + (u32)s * SLAB + (u32)(u * 8192), 0, PLD);
#pragma unroll
  for (int u = 0; u < NU; ++u)
    zz[u] = __builtin_amdgcn_raw_buffer_load_b128(rz, (u32)((16 * (4 * (R0 + 2 * u) + j) + (l >> 2)) * ZROW + (tok0 + 8 * (l & 3)) * 2), 0, SC1);
#pragma unroll
  for (int c = 0; c < 2; ++c) ms[c] = __builtin_amdgcn_raw_buffer_load_b64(rs, (u32)((tok0 + 16 * c + col) * 8), 0, SC1);
#pragma unroll
  for (int u = 2; u < NU; ++u) sum_slabs(acc[u], v[u - 2], sel);
#pragma unroll
  for (int u = 0; u < NU; ++u) *(LAS u32x4*)(scr + u * 1024 + l * 16) = zz[u];
  asm volatile("s_waitcnt lgkmcnt(0)" ::: "memory");
  STAMP(10);
#pragma unroll
  for (int u = 0; u < NU; ++u)
#pragma unroll
    for (int c = 0; c < 2; ++c) {
      float o[4];
      combine4(o, &acc[u][4 * c], z_tr(scr + u * 1024, c, l), xx[u][c], ms[c], bb[u]);
      const u32x2 o2 = {pk(o[0], o[1]), pk(o[2], o[3])};
      __builtin_amdgcn_raw_buffer_store_b64(o2, ro, (u32)(((tok0 + 16 * c + col) * D_ + 16 * (4 * (R0 + 2 * u) + j) + 4 * kg) * 2), 0, SC1);
    }
}

template <int NR>
DI void reduce_round8(int rb, int tg, int R, u32 tag, const u16* x, const u16* b2, char* ws, u16* out, LAS char* scr, int w, int l) {
  const int col = l & 15, kg = l >> 4, tok0 = 128 * rb + 32 * tg, j = w & 3, c = w >> 2, tok = tok0 + 16 * c + col;
  const __amdgpu_buffer_rsrc_t rc = mk(ws + WS_CTRL, CTRL_BYTES), rp = mk(ws + WS_PART, (u32)PART_BYTES);
  const __amdgpu_buffer_rsrc_t rz = mk(ws + WS_Z, D_ * ZROW), rs = mk(ws + WS_STATS, S_ * 8), ro = mk(out, S_ * D_ * 2);
  u32x2 bb[NR], xx[NR];
#pragma unroll
  for (int r = 0; r < NR; ++r) {
    const int d0 = 64 * (R + r) + 16 * j + 4 * kg;
    bb[r] = *(const u32x2*)(b2 + d0); xx[r] = *(const u32x2*)(x + (size_t)tok * D_ + d0);
  }
  if (w == 0) wait_lines(rc, unit_flag(rb, R + (NR > 1 && l >= 32 ? 1 : 0), l & 31), tag);
  __syncthreads();
  u32x2 v[NR][NSP];
  u32x4 zz[NR];
#pragma unroll
  for (int r = 0; r < NR; ++r) {
#pragma unroll
    for (int s = 0; s < NSP; ++s)
      v[r][s] = __builtin_amdgcn_raw_buffer_load_b64(rp, (u32)(rb * NSP + s) * SLAB + (u32)(tg * 36864 + (4 * (R + r) + j) * 1024 + l * 16 + 8 * c), 0, PLD);
    zz[r] = __builtin_amdgcn_raw_buffer_load_b128(rz, (u32)((64 * (R + r) + 16 * j + (l >> 2)) * ZROW + (tok0 + 8 * (l & 3)) * 2), 0, SC1);
  }
  const u32x2 ms = __builtin_amdgcn_raw_buffer_load_b64(rs, (u32)(tok * 8), 0, SC1);
  float acc[NR][4];
  const Sel sel = make_sel();
#pragma unroll
  for (int r = 0; r < NR; ++r) {
#pragma unroll
    for (int i = 0; i < 4; ++i) acc[r][i] = 0.f;
#pragma unroll
    for (int s = 0; s < NSP; ++s) {
      const u32 d0 = v[r][s][0], d1 = v[r][s][1];
      acc[r][0] = add_lo(acc[r][0], d0, sel.lo); acc[r][1] = add_lo(acc[r][1], d0, sel.hi);
      acc[r][2] = add_lo(acc[r][2], d1, sel.lo); acc[r][3] = add_lo(acc[r][3], d1, sel.hi);
    }
    *(LAS u32x4*)(scr + r * 1024 + l * 16) = zz[r];
  }
  asm volatile("s_waitcnt lgkmcnt(0)" ::: "memory");
#pragma unroll
  for (int r = 0; r < NR; ++r) {
    float o[4];
    combine4(o, acc[r], z_tr(scr + r * 1024, c, l), xx[r], ms, bb[r]);
    const u32x2 o2 = {pk(o[0], o[1]), pk(o[2], o[3])};
    __builtin_amdgcn_raw_buffer_store_b64(o2, ro, (u32)((tok * D_ + 64 * (R + r) + 16 * j + 4 * kg) * 2), 0, SC1);
  }
}

DI void conv_wg(int cw, const u16* x, const u16* kp, const u16* vp, const u16* skp, const u16* gp, const u16* Dp, const u16* b2, const u16* w1, const u16* w2, u16* out,
                LAS char* sm, char* ws) {
  STAMP(0);
  const int tid = threadIdx.x, l = tid & 63;
  constexpr int URS = 5120;
  const __amdgpu_buffer_rsrc_t rc = mk(ws + WS_CTRL, CTRL_BYTES);
  const u32 epoch = __builtin_amdgcn_raw_buffer_load_b32(rc, 0, 0, SC1);
  u32 tag = 0;
  if (cw < 63) {
    const int grp = cw % 7, c0 = 64 * (cw / 7) + 9 * grp, nch = grp == 6 ? 10 : 9;
    {
      u32x4 g0[4];
      {
        const __amdgpu_buffer_rsrc_t rg = mk(gp, D_ * S_ * 2);
        const int x0 = 512 * (l >> 4) - 32 + 24 * (l & 15);
#pragma unroll
        for (int i = 0; i < 4; ++i) g0[i] = __builtin_amdgcn_raw_buffer_load_b128(rg, (u32)((c0 + (tid >> 6)) * 4096 + x0 * 2 + 16 * i), 0, 0);
      }
      const int sh = c0 & 7;
      const int o = tid >> 1, hh = tid & 1, c8 = c0 - sh + 8 * hh, n0 = 8 * o;
      const __amdgpu_buffer_rsrc_t rk = mk(kp, S_ * D_ * 2), rv = mk(vp, S_ * D_ * 2), rs = mk(skp, D_ * 8);
      u32x4 kr[11], vr[8], sk[4];
#pragma unroll
      for (int i = 0; i < 4; ++i) sk[i] = __builtin_amdgcn_raw_buffer_load_b128(rs, (u32)(c8 * 8 + 16 * i), 0, 0);
#pragma unroll
      for (int i = 0; i < 11; ++i) kr[i] = __builtin_amdgcn_raw_buffer_load_b128(rk, (u32)((n0 - 3 + i) * 1152 + c8 * 2), 0, 0);
#pragma unroll
      for (int i = 0; i < 8; ++i) vr[i] = __builtin_amdgcn_raw_buffer_load_b128(rv, (u32)((n0 + i) * 1152 + c8 * 2), 0, 0);
      asm volatile(
    "v_and_b32 v1, 63, v0\n"
    "v_lshrrev_b32 v11, 6, v0\n"
    "s_nop 0\n"
    "v_readfirstlane_b32 s21, v11\n"
    "v_and_b32 v14, 15, v1\n"
    "v_lshrrev_b32 v15, 4, v1\n"
    "s_nop 1\n"
    "s_mul_i32 s22, s21, 9216\n"
    "s_add_u32 s22, s22, 51200\n"
    "v_and_b32 v11, 3, v14\n"
    "v_mul_u32_u24 v11, 576, v11\n"
    "v_and_b32 v12, 12, v14\n"
    "v_lshl_add_u32 v12, v15, 3, v12\n"
    "v_lshl_add_u32 v11, v12, 1, v11\n"
    "v_add_u32 v2, s22, v11\n"
    "v_mul_u32_u24 v11, 2304, v15\n"
    "v_mul_u32_u24 v12, 48, v14\n"
    "v_add3_u32 v7, v11, v12, s22\n"
    "v_mul_u32_u24 v12, 24, v14\n"
    "v_lshl_add_u32 v8, v15, 9, v12\n"
    "v_subrev_u32 v8, 32, v8\n"
    "v_cmp_gt_u32 s[28:29], 12, v14\n"
    "v_cmp_le_i32 s[36:37], 0, v8\n"
    "v_cmp_le_i32 s[38:39], -8, v8\n"
    "s_nop 0\n"
    "v_cndmask_b32 v144, 0, v144, s[36:37]\n"
    "v_cndmask_b32 v145, 0, v145, s[36:37]\n"
    "v_cndmask_b32 v146, 0, v146, s[36:37]\n"
    "v_cndmask_b32 v147, 0, v147, s[36:37]\n"
    "v_cndmask_b32 v148, 0, v148, s[38:39]\n"
    "v_cndmask_b32 v149, 0, v149, s[38:39]\n"
    "v_cndmask_b32 v150, 0, v150, s[38:39]\n"
    "v_cndmask_b32 v151, 0, v151, s[38:39]\n"
    "v_cndmask_b32 v152, 0, v152, s[38:39]\n"
    "v_cndmask_b32 v153, 0, v153, s[38:39]\n"
    "v_cndmask_b32 v154, 0, v154, s[38:39]\n"
    "v_cndmask_b32 v155, 0, v155, s[38:39]\n"
    "v_cndmask_b32 v156, 0, v156, s[38:39]\n"
    "v_cndmask_b32 v157, 0, v157, s[38:39]\n"
    "v_cndmask_b32 v158, 0, v158, s[38:39]\n"
    "v_cndmask_b32 v159, 0, v159, s[38:39]\n"
    "v_alignbit_b32 v160, v145, v144, 16\n"
    "v_alignbit_b32 v161, v146, v145, 16\n"
    "v_alignbit_b32 v162, v147, v146, 16\n"
    "v_alignbit_b32 v163, v148, v147, 16\n"
    "v_alignbit_b32 v164, v149, v148, 16\n"
    "v_alignbit_b32 v165, v150, v149, 16\n"
    "v_alignbit_b32 v166, v151, v150, 16\n"
    "v_alignbit_b32 v167, v152, v151, 16\n"
    "v_alignbit_b32 v168, v153, v152, 16\n"
    "v_alignbit_b32 v169, v154, v153, 16\n"
    "v_alignbit_b32 v170, v155, v154, 16\n"
    "v_alignbit_b32 v171, v156, v155, 16\n"
    "v_alignbit_b32 v172, v146, v145, 16\n"
    "v_alignbit_b32 v173, v147, v146, 16\n"
    "v_alignbit_b32 v174, v148, v147, 16\n"
    "v_alignbit_b32 v175, v149, v148, 16\n"
    "v_alignbit_b32 v176, v150, v149, 16\n"
    "v_alignbit_b32 v177, v151, v150, 16\n"
    "v_alignbit_b32 v178, v152, v151, 16\n"
    "v_alignbit_b32 v179, v153, v152, 16\n"
    "v_alignbit_b32 v180, v154, v153, 16\n"
    "v_alignbit_b32 v181, v155, v154, 16\n"
    "v_alignbit_b32 v182, v156, v155, 16\n"
    "v_alignbit_b32 v183, v157, v156, 16\n"
    "v_mov_b32 v184, v145\n"
    "v_mov_b32 v185, v146\n"
    "v_mov_b32 v186, v147\n"
    "v_mov_b32 v187, v148\n"
    "v_mov_b32 v188, v149\n"
    "v_mov_b32 v189, v150\n"
    "v_mov_b32 v190, v151\n"
    "v_mov_b32 v191, v152\n"
    "v_mov_b32 v192, v153\n"
    "v_mov_b32 v193, v154\n"
    "v_mov_b32 v194, v155\n"
    "v_mov_b32 v195, v156\n"
    "s_mov_b64 s[30:31], exec\n"
    "s_mov_b64 exec, s[28:29]\n"
    "ds_write_b128 v7, v[160:163] offset:0\n"
    "ds_write_b128 v7, v[164:167] offset:16\n"
    "ds_write_b128 v7, v[168:171] offset:32\n"
    "ds_write_b128 v7, v[184:187] offset:576\n"
    "ds_write_b128 v7, v[188:191] offset:592\n"
    "ds_write_b128 v7, v[192:195] offset:608\n"
    "ds_write_b128 v7, v[172:175] offset:1152\n"
    "ds_write_b128 v7, v[176:179] offset:1168\n"
    "ds_write_b128 v7, v[180:183] offset:1184\n"
    "ds_write_b128 v7, v[146:149] offset:1728\n"
    "ds_write_b128 v7, v[150:153] offset:1744\n"
    "ds_write_b128 v7, v[154:157] offset:1760\n"
    "s_mov_b64 exec, s[30:31]\n"
    "s_waitcnt lgkmcnt(0)\n"
          : "+{v[144:147]}"(g0[0]), "+{v[148:151]}"(g0[1]), "+{v[152:155]}"(g0[2]), "+{v[156:159]}"(g0[3])
          : "{v0}"(threadIdx.x)
          : "memory", "vcc", "scc",
        "v1", "v2", "v7", "v8", "v11", "v12", "v14", "v15", "v160", "v161", "v162", "v163", "v164", "v165", "v166", "v167", "v168", "v169", "v170", "v171", "v172", "v173", "v174", "v175", "v176", "v177", "v178", "v179", "v180", "v181", "v182", "v183", "v184", "v185", "v186", "v187", "v188", "v189", "v190", "v191", "v192", "v193", "v194", "v195", "s21", "s22", "s28", "s29", "s30", "s31", "s36", "s37", "s38", "s39"
      );
      for (int i = tid; i < 10 * 64; i += 512) *(LAS u32x4*)(sm + (i >> 6) * URS + (i & 63) * 16) = u32x4{0, 0, 0, 0};
      const int A = (o >> 2) + 16, kg = 3 - (o & 3);
      const int wa = A * 64 + 16 * (kg ^ (((A >> 2) & 1) << 1));
#pragma unroll
      for (int ip = 0; ip < 4; ++ip) {
        const f32x2 t0 = {bflo(sk[ip][0]), bflo(sk[ip][2])}, t1 = {bfhi(sk[ip][0]), bfhi(sk[ip][2])};
        const f32x2 t2 = {bflo(sk[ip][1]), bflo(sk[ip][3])}, t3 = {bfhi(sk[ip][1]), bfhi(sk[ip][3])};
        f32x2 kk[11], u[8];
#pragma unroll
        for (int r = 0; r < 11; ++r) kk[r] = f32x2{bflo(kr[r][ip]), bfhi(kr[r][ip])};
#pragma unroll
        for (int r = 0; r < 8; ++r) {
          const f32x2 a = kk[r] * t3 + kk[r + 1] * t2 + kk[r + 2] * t1 + kk[r + 3] * t0;
          u[r] = a * f32x2{bflo(vr[r][ip]), bfhi(vr[r][ip])};
        }
#pragma unroll
        for (int e = 0; e < 2; ++e) {
          const int ch = 8 * hh + 2 * ip + e - sh;
          const u32x4 o4 = {pk(u[7][e], u[6][e]), pk(u[5][e], u[4][e]), pk(u[3][e], u[2][e]), pk(u[1][e], u[0][e])};
          if (ch >= 0 && ch < nch) *(LAS u32x4*)(sm + ch * URS + wa) = o4;
        }
      }
    }
    STAMP(1);
    tag = (u32)__builtin_amdgcn_readfirstlane(epoch) + 1u;
    put_flag(rc, CT_SEEN + 128 * blockIdx.x, tag, tid);
    __syncthreads();
    STAMP(2);
    {
      const u16* zT = (const u16*)(ws + WS_Z);
      const int nex = 2 * (nch - 8);
      const char* pw2 = (const char*)w2 + (size_t)(1152 * ((blockIdx.x & 7) >> 1) + 144 * (cw & 7)) * 1152;
      const char* pw1 = (const char*)w1 + (size_t)(72 * (cw & 7)) * 9216 + 2304 * ((blockIdx.x & 7) >> 1);
      asm volatile(
    "s_mov_b64 s[8:9], %0\n"
    "s_mov_b64 s[12:13], %1\n"
    "s_mov_b64 s[16:17], %2\n"
    "s_mov_b32 s20, %3\n"
    "s_mov_b32 s40, %4\n"
    "s_mov_b64 s[44:45], %5\n"
    "s_and_b32 s45, s45, 0xffff\n"
    "s_mov_b32 s46, 165888\n"
    "s_mov_b32 s47, 0x20000\n"
    "s_and_b32 s9, s9, 0xffff\n"
    "s_mov_b32 s10, 2359296\n"
    "s_mov_b32 s11, 0x20000\n"
    "s_and_b32 s13, s13, 0xffff\n"
    "s_mov_b32 s14, 1152\n"
    "s_mov_b32 s15, 0x20000\n"
    "s_and_b32 s17, s17, 0xffff\n"
    "s_mov_b32 s18, 2506752\n"
    "s_mov_b32 s19, 0x20000\n"
    "v_and_b32 v1, 63, v0\n"
    "v_lshrrev_b32 v11, 6, v0\n"
    "s_nop 0\n"
    "v_readfirstlane_b32 s21, v11\n"
    "v_and_b32 v14, 15, v1\n"
    "v_lshrrev_b32 v15, 4, v1\n"
    "s_nop 1\n"
    "s_mul_i32 s22, s21, 9216\n"
    "s_add_u32 s22, s22, 51200\n"
    "v_and_b32 v11, 3, v14\n"
    "v_mul_u32_u24 v11, 576, v11\n"
    "v_and_b32 v12, 12, v14\n"
    "v_lshl_add_u32 v12, v15, 3, v12\n"
    "v_lshl_add_u32 v11, v12, 1, v11\n"
    "v_add_u32 v2, s22, v11\n"
    "v_mul_u32_u24 v11, 2304, v15\n"
    "v_mul_u32_u24 v12, 48, v14\n"
    "v_add3_u32 v7, v11, v12, s22\n"
    "v_mul_u32_u24 v12, 24, v14\n"
    "v_lshl_add_u32 v8, v15, 9, v12\n"
    "v_subrev_u32 v8, 32, v8\n"
    "v_cmp_gt_u32 s[28:29], 12, v14\n"
    "v_cmp_le_i32 s[36:37], 0, v8\n"
    "v_cmp_le_i32 s[38:39], -8, v8\n"
    "s_mov_b32 s24, 0xffff0000\n"
    "s_lshr_b32 s41, s21, 1\n"
    "s_and_b32 s41, s41, 1\n"
    "s_add_u32 s41, s41, 8\n"
    "s_add_u32 s22, s20, s21\n"
    "s_lshl_b32 s22, s22, 1\n"
    "v_mov_b32 v13, s22\n"
    "buffer_load_ushort v13, v13, s[12:15], 0 offen\n"
    "s_add_u32 s22, s20, s41\n"
    "s_lshl_b32 s22, s22, 1\n"
    "v_mov_b32 v10, s22\n"
    "buffer_load_ushort v10, v10, s[12:15], 0 offen\n"
    "s_mul_i32 s26, s21, 5120\n"
    "s_add_u32 s25, s20, s21\n"
    "s_lshl_b32 s22, s25, 12\n"
    "s_add_u32 s22, s22, 512\n"
    "v_lshl_add_u32 v6, v8, 1, s22\n"
    "buffer_load_dwordx4 v[144:147], v6, s[8:11], 0 offen\n"
    "v_add_u32 v6, 16, v6\n"
    "buffer_load_dwordx4 v[148:151], v6, s[8:11], 0 offen\n"
    "v_add_u32 v6, 16, v6\n"
    "buffer_load_dwordx4 v[152:155], v6, s[8:11], 0 offen\n"
    "v_add_u32 v6, 16, v6\n"
    "buffer_load_dwordx4 v[156:159], v6, s[8:11], 0 offen\n"
    "v_add_u32 v11, 8, v14\n"
    "v_bfe_u32 v12, v11, 2, 1\n"
    "v_lshlrev_b32 v12, 1, v12\n"
    "v_xor_b32 v12, v12, v15\n"
    "v_lshlrev_b32 v11, 6, v11\n"
    "v_lshl_add_u32 v11, v12, 4, v11\n"
    "v_add_u32 v208, s26, v11\n"
    "v_add_u32 v11, 7, v14\n"
    "v_bfe_u32 v12, v11, 2, 1\n"
    "v_lshlrev_b32 v12, 1, v12\n"
    "v_xor_b32 v12, v12, v15\n"
    "v_lshlrev_b32 v11, 6, v11\n"
    "v_lshl_add_u32 v11, v12, 4, v11\n"
    "v_add_u32 v209, s26, v11\n"
    "v_add_u32 v11, 6, v14\n"
    "v_bfe_u32 v12, v11, 2, 1\n"
    "v_lshlrev_b32 v12, 1, v12\n"
    "v_xor_b32 v12, v12, v15\n"
    "v_lshlrev_b32 v11, 6, v11\n"
    "v_lshl_add_u32 v11, v12, 4, v11\n"
    "v_add_u32 v210, s26, v11\n"
    "v_add_u32 v11, 5, v14\n"
    "v_bfe_u32 v12, v11, 2, 1\n"
    "v_lshlrev_b32 v12, 1, v12\n"
    "v_xor_b32 v12, v12, v15\n"
    "v_lshlrev_b32 v11, 6, v11\n"
    "v_lshl_add_u32 v11, v12, 4, v11\n"
    "v_add_u32 v211, s26, v11\n"
    "v_add_u32 v11, 4, v14\n"
    "v_bfe_u32 v12, v11, 2, 1\n"
    "v_lshlrev_b32 v12, 1, v12\n"
    "v_xor_b32 v12, v12, v15\n"
    "v_lshlrev_b32 v11, 6, v11\n"
    "v_lshl_add_u32 v11, v12, 4, v11\n"
    "v_add_u32 v212, s26, v11\n"
    "v_add_u32 v11, 3, v14\n"
    "v_bfe_u32 v12, v11, 2, 1\n"
    "v_lshlrev_b32 v12, 1, v12\n"
    "v_xor_b32 v12, v12, v15\n"
    "v_lshlrev_b32 v11, 6, v11\n"
    "v_lshl_add_u32 v11, v12, 4, v11\n"
    "v_add_u32 v213, s26, v11\n"
    "v_add_u32 v11, 2, v14\n"
    "v_bfe_u32 v12, v11, 2, 1\n"
    "v_lshlrev_b32 v12, 1, v12\n"
    "v_xor_b32 v12, v12, v15\n"
    "v_lshlrev_b32 v11, 6, v11\n"
    "v_lshl_add_u32 v11, v12, 4, v11\n"
    "v_add_u32 v214, s26, v11\n"
    "v_add_u32 v11, 1, v14\n"
    "v_bfe_u32 v12, v11, 2, 1\n"
    "v_lshlrev_b32 v12, 1, v12\n"
    "v_xor_b32 v12, v12, v15\n"
    "v_lshlrev_b32 v11, 6, v11\n"
    "v_lshl_add_u32 v11, v12, 4, v11\n"
    "v_add_u32 v215, s26, v11\n"
    "ds_read_b128 v[112:115], v208 offset:512\n"
    "ds_read_b128 v[116:119], v208 offset:1536\n"
    "ds_read_b128 v[120:123], v208 offset:2560\n"
    "ds_read_b128 v[124:127], v208 offset:3584\n"
    "ds_read_b64 v[48:49], v2 offset:0\n"
    "ds_read_b64 v[50:51], v2 offset:8\n"
    "ds_read_b64 v[52:53], v2 offset:32\n"
    "ds_read_b64 v[54:55], v2 offset:40\n"
    "ds_read_b64 v[56:57], v2 offset:2304\n"
    "ds_read_b64 v[58:59], v2 offset:2312\n"
    "ds_read_b64 v[60:61], v2 offset:2336\n"
    "ds_read_b64 v[62:63], v2 offset:2344\n"
    "ds_read_b64 v[64:65], v2 offset:4608\n"
    "ds_read_b64 v[66:67], v2 offset:4616\n"
    "ds_read_b64 v[68:69], v2 offset:4640\n"
    "ds_read_b64 v[70:71], v2 offset:4648\n"
    "ds_read_b64 v[72:73], v2 offset:6912\n"
    "ds_read_b64 v[74:75], v2 offset:6920\n"
    "ds_read_b64 v[76:77], v2 offset:6944\n"
    "ds_read_b64 v[78:79], v2 offset:6952\n"
    "s_waitcnt lgkmcnt(14)\n"
    "v_mfma_f32_16x16x32_bf16 v[16:19], v[48:51], v[112:115], 0\n"
    "ds_read_b128 v[128:131], v209 offset:512\n"
    "v_mfma_f32_16x16x32_bf16 v[20:23], v[48:51], v[116:119], 0\n"
    "ds_read_b128 v[132:135], v209 offset:1536\n"
    "v_mfma_f32_16x16x32_bf16 v[24:27], v[48:51], v[120:123], 0\n"
    "ds_read_b128 v[136:139], v209 offset:2560\n"
    "v_mfma_f32_16x16x32_bf16 v[28:31], v[48:51], v[124:127], 0\n"
    "ds_read_b128 v[140:143], v209 offset:3584\n"
    "s_waitcnt lgkmcnt(15)\n"
    "v_mfma_f32_16x16x32_bf16 v[32:35], v[52:55], v[112:115], 0\n"
    "ds_read_b64 v[80:81], v2 offset:64\n"
    "v_mfma_f32_16x16x32_bf16 v[36:39], v[52:55], v[116:119], 0\n"
    "ds_read_b64 v[82:83], v2 offset:72\n"
    "v_mfma_f32_16x16x32_bf16 v[40:43], v[52:55], v[120:123], 0\n"
    "ds_read_b64 v[84:85], v2 offset:96\n"
    "v_mfma_f32_16x16x32_bf16 v[44:47], v[52:55], v[124:127], 0\n"
    "ds_read_b64 v[86:87], v2 offset:104\n"
    "s_waitcnt lgkmcnt(15)\n"
    "v_mfma_f32_16x16x32_bf16 v[20:23], v[56:59], v[112:115], v[20:23]\n"
    "ds_read_b64 v[88:89], v2 offset:2368\n"
    "ds_read_b64 v[90:91], v2 offset:2376\n"
    "v_mfma_f32_16x16x32_bf16 v[24:27], v[56:59], v[116:119], v[24:27]\n"
    "ds_read_b64 v[92:93], v2 offset:2400\n"
    "v_mfma_f32_16x16x32_bf16 v[28:31], v[56:59], v[120:123], v[28:31]\n"
    "ds_read_b64 v[94:95], v2 offset:2408\n"
    "v_mfma_f32_16x16x32_bf16 v[36:39], v[60:63], v[112:115], v[36:39]\n"
    "ds_read_b64 v[96:97], v2 offset:4672\n"
    "v_mfma_f32_16x16x32_bf16 v[40:43], v[60:63], v[116:119], v[40:43]\n"
    "ds_read_b64 v[98:99], v2 offset:4680\n"
    "v_mfma_f32_16x16x32_bf16 v[44:47], v[60:63], v[120:123], v[44:47]\n"
    "ds_read_b64 v[100:101], v2 offset:4704\n"
    "s_waitcnt lgkmcnt(15)\n"
    "v_mfma_f32_16x16x32_bf16 v[24:27], v[64:67], v[112:115], v[24:27]\n"
    "ds_read_b64 v[102:103], v2 offset:4712\n"
    "v_mfma_f32_16x16x32_bf16 v[28:31], v[64:67], v[116:119], v[28:31]\n"
    "ds_read_b64 v[104:105], v2 offset:6976\n"
    "v_mfma_f32_16x16x32_bf16 v[40:43], v[68:71], v[112:115], v[40:43]\n"
    "ds_read_b64 v[106:107], v2 offset:6984\n"
    "v_mfma_f32_16x16x32_bf16 v[44:47], v[68:71], v[116:119], v[44:47]\n"
    "ds_read_b64 v[108:109], v2 offset:7008\n"
    "ds_read_b64 v[110:111], v2 offset:7016\n"
    "v_mfma_f32_16x16x32_bf16 v[28:31], v[72:75], v[112:115], v[28:31]\n"
    "v_mfma_f32_16x16x32_bf16 v[44:47], v[76:79], v[112:115], v[44:47]\n"
    "s_waitcnt lgkmcnt(14)\n"
    "v_mfma_f32_16x16x32_bf16 v[16:19], v[80:83], v[128:131], v[16:19]\n"
    "ds_read_b128 v[112:115], v210 offset:512\n"
    "v_mfma_f32_16x16x32_bf16 v[20:23], v[80:83], v[132:135], v[20:23]\n"
    "ds_read_b128 v[116:119], v210 offset:1536\n"
    "v_mfma_f32_16x16x32_bf16 v[24:27], v[80:83], v[136:139], v[24:27]\n"
    "ds_read_b128 v[120:123], v210 offset:2560\n"
    "v_mfma_f32_16x16x32_bf16 v[28:31], v[80:83], v[140:143], v[28:31]\n"
    "ds_read_b128 v[124:127], v210 offset:3584\n"
    "s_waitcnt lgkmcnt(15)\n"
    "v_mfma_f32_16x16x32_bf16 v[32:35], v[84:87], v[128:131], v[32:35]\n"
    "ds_read_b64 v[48:49], v2 offset:128\n"
    "v_mfma_f32_16x16x32_bf16 v[36:39], v[84:87], v[132:135], v[36:39]\n"
    "ds_read_b64 v[50:51], v2 offset:136\n"
    "v_mfma_f32_16x16x32_bf16 v[40:43], v[84:87], v[136:139], v[40:43]\n"
    "ds_read_b64 v[52:53], v2 offset:160\n"
    "v_mfma_f32_16x16x32_bf16 v[44:47], v[84:87], v[140:143], v[44:47]\n"
    "ds_read_b64 v[54:55], v2 offset:168\n"
    "s_waitcnt lgkmcnt(15)\n"
    "v_mfma_f32_16x16x32_bf16 v[20:23], v[88:91], v[128:131], v[20:23]\n"
    "ds_read_b64 v[56:57], v2 offset:2432\n"
    "ds_read_b64 v[58:59], v2 offset:2440\n"
    "v_mfma_f32_16x16x32_bf16 v[24:27], v[88:91], v[132:135], v[24:27]\n"
    "ds_read_b64 v[60:61], v2 offset:2464\n"
    "v_mfma_f32_16x16x32_bf16 v[28:31], v[88:91], v[136:139], v[28:31]\n"
    "ds_read_b64 v[62:63], v2 offset:2472\n"
    "v_mfma_f32_16x16x32_bf16 v[36:39], v[92:95], v[128:131], v[36:39]\n"
    "ds_read_b64 v[64:65], v2 offset:4736\n"
    "v_mfma_f32_16x16x32_bf16 v[40:43], v[92:95], v[132:135], v[40:43]\n"
    "ds_read_b64 v[66:67], v2 offset:4744\n"
    "v_mfma_f32_16x16x32_bf16 v[44:47], v[92:95], v[136:139], v[44:47]\n"
    "ds_read_b64 v[68:69], v2 offset:4768\n"
    "s_waitcnt lgkmcnt(15)\n"
    "v_mfma_f32_16x16x32_bf16 v[24:27], v[96:99], v[128:131], v[24:27]\n"
    "ds_read_b64 v[70:71], v2 offset:4776\n"
    "v_mfma_f32_16x16x32_bf16 v[28:31], v[96:99], v[132:135], v[28:31]\n"
    "ds_read_b64 v[72:73], v2 offset:7040\n"
    "v_mfma_f32_16x16x32_bf16 v[40:43], v[100:103], v[128:131], v[40:43]\n"
    "ds_read_b64 v[74:75], v2 offset:7048\n"
    "v_mfma_f32_16x16x32_bf16 v[44:47], v[100:103], v[132:135], v[44:47]\n"
    "ds_read_b64 v[76:77], v2 offset:7072\n"
    "ds_read_b64 v[78:79], v2 offset:7080\n"
    "v_mfma_f32_16x16x32_bf16 v[28:31], v[104:107], v[128:131], v[28:31]\n"
    "v_mfma_f32_16x16x32_bf16 v[44:47], v[108:111], v[128:131], v[44:47]\n"
    "s_waitcnt lgkmcnt(14)\n"
    "v_mfma_f32_16x16x32_bf16 v[16:19], v[48:51], v[112:115], v[16:19]\n"
    "ds_read_b128 v[128:131], v211 offset:512\n"
    "v_mfma_f32_16x16x32_bf16 v[20:23], v[48:51], v[116:119], v[20:23]\n"
    "ds_read_b128 v[132:135], v211 offset:1536\n"
    "v_mfma_f32_16x16x32_bf16 v[24:27], v[48:51], v[120:123], v[24:27]\n"
    "ds_read_b128 v[136:139], v211 offset:2560\n"
    "v_mfma_f32_16x16x32_bf16 v[28:31], v[48:51], v[124:127], v[28:31]\n"
    "ds_read_b128 v[140:143], v211 offset:3584\n"
    "s_waitcnt lgkmcnt(15)\n"
    "v_mfma_f32_16x16x32_bf16 v[32:35], v[52:55], v[112:115], v[32:35]\n"
    "ds_read_b64 v[80:81], v2 offset:192\n"
    "v_mfma_f32_16x16x32_bf16 v[36:39], v[52:55], v[116:119], v[36:39]\n"
    "ds_read_b64 v[82:83], v2 offset:200\n"
    "v_mfma_f32_16x16x32_bf16 v[40:43], v[52:55], v[120:123], v[40:43]\n"
    "ds_read_b64 v[84:85], v2 offset:224\n"
    "v_mfma_f32_16x16x32_bf16 v[44:47], v[52:55], v[124:127], v[44:47]\n"
    "ds_read_b64 v[86:87], v2 offset:232\n"
    "s_waitcnt lgkmcnt(15)\n"
    "v_mfma_f32_16x16x32_bf16 v[20:23], v[56:59], v[112:115], v[20:23]\n"
    "ds_read_b64 v[88:89], v2 offset:2496\n"
    "ds_read_b64 v[90:91], v2 offset:2504\n"
    "v_mfma_f32_16x16x32_bf16 v[24:27], v[56:59], v[116:119], v[24:27]\n"
    "ds_read_b64 v[92:93], v2 offset:2528\n"
    "v_mfma_f32_16x16x32_bf16 v[28:31], v[56:59], v[120:123], v[28:31]\n"
    "ds_read_b64 v[94:95], v2 offset:2536\n"
    "v_mfma_f32_16x16x32_bf16 v[36:39], v[60:63], v[112:115], v[36:39]\n"
    "ds_read_b64 v[96:97], v2 offset:4800\n"
    "v_mfma_f32_16x16x32_bf16 v[40:43], v[60:63], v[116:119], v[40:43]\n"
    "ds_read_b64 v[98:99], v2 offset:4808\n"
    "v_mfma_f32_16x16x32_bf16 v[44:47], v[60:63], v[120:123], v[44:47]\n"
    "ds_read_b64 v[100:101], v2 offset:4832\n"
    "s_waitcnt lgkmcnt(15)\n"
    "v_mfma_f32_16x16x32_bf16 v[24:27], v[64:67], v[112:115], v[24:27]\n"
    "ds_read_b64 v[102:103], v2 offset:4840\n"
    "v_mfma_f32_16x16x32_bf16 v[28:31], v[64:67], v[116:119], v[28:31]\n"
    "ds_read_b64 v[104:105], v2 offset:7104\n"
    "v_mfma_f32_16x16x32_bf16 v[40:43], v[68:71], v[112:115], v[40:43]\n"
    "ds_read_b64 v[106:107], v2 offset:7112\n"
    "v_mfma_f32_16x16x32_bf16 v[44:47], v[68:71], v[116:119], v[44:47]\n"
    "ds_read_b64 v[108:109], v2 offset:7136\n"
    "ds_read_b64 v[110:111], v2 offset:7144\n"
    "v_mfma_f32_16x16x32_bf16 v[28:31], v[72:75], v[112:115], v[28:31]\n"
    "v_mfma_f32_16x16x32_bf16 v[44:47], v[76:79], v[112:115], v[44:47]\n"
    "s_waitcnt lgkmcnt(14)\n"
    "v_mfma_f32_16x16x32_bf16 v[16:19], v[80:83], v[128:131], v[16:19]\n"
    "ds_read_b128 v[112:115], v212 offset:512\n"
    "v_mfma_f32_16x16x32_bf16 v[20:23], v[80:83], v[132:135], v[20:23]\n"
    "ds_read_b128 v[116:119], v212 offset:1536\n"
    "v_mfma_f32_16x16x32_bf16 v[24:27], v[80:83], v[136:139], v[24:27]\n"
    "ds_read_b128 v[120:123], v212 offset:2560\n"
    "v_mfma_f32_16x16x32_bf16 v[28:31], v[80:83], v[140:143], v[28:31]\n"
    "ds_read_b128 v[124:127], v212 offset:3584\n"
    "s_waitcnt lgkmcnt(15)\n"
    "v_mfma_f32_16x16x32_bf16 v[32:35], v[84:87], v[128:131], v[32:35]\n"
    "ds_read_b64 v[48:49], v2 offset:256\n"
    "v_mfma_f32_16x16x32_bf16 v[36:39], v[84:87], v[132:135], v[36:39]\n"
    "ds_read_b64 v[50:51], v2 offset:264\n"
    "v_mfma_f32_16x16x32_bf16 v[40:43], v[84:87], v[136:139], v[40:43]\n"
    "ds_read_b64 v[52:53], v2 offset:288\n"
    "v_mfma_f32_16x16x32_bf16 v[44:47], v[84:87], v[140:143], v[44:47]\n"
    "ds_read_b64 v[54:55], v2 offset:296\n"
    "s_waitcnt lgkmcnt(15)\n"
    "v_mfma_f32_16x16x32_bf16 v[20:23], v[88:91], v[128:131], v[20:23]\n"
    "ds_read_b64 v[56:57], v2 offset:2560\n"
    "ds_read_b64 v[58:59], v2 offset:2568\n"
    "v_mfma_f32_16x16x32_bf16 v[24:27], v[88:91], v[132:135], v[24:27]\n"
    "ds_read_b64 v[60:61], v2 offset:2592\n"
    "v_mfma_f32_16x16x32_bf16 v[28:31], v[88:91], v[136:139], v[28:31]\n"
    "ds_read_b64 v[62:63], v2 offset:2600\n"
    "v_mfma_f32_16x16x32_bf16 v[36:39], v[92:95], v[128:131], v[36:39]\n"
    "ds_read_b64 v[64:65], v2 offset:4864\n"
    "v_mfma_f32_16x16x32_bf16 v[40:43], v[92:95], v[132:135], v[40:43]\n"
    "ds_read_b64 v[66:67], v2 offset:4872\n"
    "v_mfma_f32_16x16x32_bf16 v[44:47], v[92:95], v[136:139], v[44:47]\n"
    "ds_read_b64 v[68:69], v2 offset:4896\n"
    "s_waitcnt lgkmcnt(15)\n"
    "v_mfma_f32_16x16x32_bf16 v[24:27], v[96:99], v[128:131], v[24:27]\n"
    "ds_read_b64 v[70:71], v2 offset:4904\n"
    "v_mfma_f32_16x16x32_bf16 v[28:31], v[96:99], v[132:135], v[28:31]\n"
    "ds_read_b64 v[72:73], v2 offset:7168\n"
    "v_mfma_f32_16x16x32_bf16 v[40:43], v[100:103], v[128:131], v[40:43]\n"
    "ds_read_b64 v[74:75], v2 offset:7176\n"
    "v_mfma_f32_16x16x32_bf16 v[44:47], v[100:103], v[132:135], v[44:47]\n"
    "ds_read_b64 v[76:77], v2 offset:7200\n"
    "ds_read_b64 v[78:79], v2 offset:7208\n"
    "v_mfma_f32_16x16x32_bf16 v[28:31], v[104:107], v[128:131], v[28:31]\n"
    "v_mfma_f32_16x16x32_bf16 v[44:47], v[108:111], v[128:131], v[44:47]\n"
    "s_waitcnt lgkmcnt(14)\n"
    "v_mfma_f32_16x16x32_bf16 v[16:19], v[48:51], v[112:115], v[16:19]\n"
    "ds_read_b128 v[128:131], v213 offset:512\n"
    "v_mfma_f32_16x16x32_bf16 v[20:23], v[48:51], v[116:119], v[20:23]\n"
    "ds_read_b128 v[132:135], v213 offset:1536\n"
    "v_mfma_f32_16x16x32_bf16 v[24:27], v[48:51], v[120:123], v[24:27]\n"
    "ds_read_b128 v[136:139], v213 offset:2560\n"
    "v_mfma_f32_16x16x32_bf16 v[28:31], v[48:51], v[124:127], v[28:31]\n"
    "ds_read_b128 v[140:143], v213 offset:3584\n"
    "s_waitcnt lgkmcnt(15)\n"
    "v_mfma_f32_16x16x32_bf16 v[32:35], v[52:55], v[112:115], v[32:35]\n"
    "ds_read_b64 v[80:81], v2 offset:320\n"
    "v_mfma_f32_16x16x32_bf16 v[36:39], v[52:55], v[116:119], v[36:39]\n"
    "ds_read_b64 v[82:83], v2 offset:328\n"
    "v_mfma_f32_16x16x32_bf16 v[40:43], v[52:55], v[120:123], v[40:43]\n"
    "ds_read_b64 v[84:85], v2 offset:352\n"
    "v_mfma_f32_16x16x32_bf16 v[44:47], v[52:55], v[124:127], v[44:47]\n"
    "ds_read_b64 v[86:87], v2 offset:360\n"
    "s_waitcnt lgkmcnt(15)\n"
    "v_mfma_f32_16x16x32_bf16 v[20:23], v[56:59], v[112:115], v[20:23]\n"
    "ds_read_b64 v[88:89], v2 offset:2624\n"
    "ds_read_b64 v[90:91], v2 offset:2632\n"
    "v_mfma_f32_16x16x32_bf16 v[24:27], v[56:59], v[116:119], v[24:27]\n"
    "ds_read_b64 v[92:93], v2 offset:2656\n"
    "v_mfma_f32_16x16x32_bf16 v[28:31], v[56:59], v[120:123], v[28:31]\n"
    "ds_read_b64 v[94:95], v2 offset:2664\n"
    "v_mfma_f32_16x16x32_bf16 v[36:39], v[60:63], v[112:115], v[36:39]\n"
    "ds_read_b64 v[96:97], v2 offset:4928\n"
    "v_mfma_f32_16x16x32_bf16 v[40:43], v[60:63], v[116:119], v[40:43]\n"
    "ds_read_b64 v[98:99], v2 offset:4936\n"
    "v_mfma_f32_16x16x32_bf16 v[44:47], v[60:63], v[120:123], v[44:47]\n"
    "ds_read_b64 v[100:101], v2 offset:4960\n"
    "s_waitcnt lgkmcnt(15)\n"
    "v_mfma_f32_16x16x32_bf16 v[24:27], v[64:67], v[112:115], v[24:27]\n"
    "ds_read_b64 v[102:103], v2 offset:4968\n"
    "v_mfma_f32_16x16x32_bf16 v[28:31], v[64:67], v[116:119], v[28:31]\n"
    "ds_read_b64 v[104:105], v2 offset:7232\n"
    "v_mfma_f32_16x16x32_bf16 v[40:43], v[68:71], v[112:115], v[40:43]\n"
    "ds_read_b64 v[106:107], v2 offset:7240\n"
    "v_mfma_f32_16x16x32_bf16 v[44:47], v[68:71], v[116:119], v[44:47]\n"
    "ds_read_b64 v[108:109], v2 offset:7264\n"
    "ds_read_b64 v[110:111], v2 offset:7272\n"
    "v_mfma_f32_16x16x32_bf16 v[28:31], v[72:75], v[112:115], v[28:31]\n"
    "v_mfma_f32_16x16x32_bf16 v[44:47], v[76:79], v[112:115], v[44:47]\n"
    "s_waitcnt lgkmcnt(14)\n"
    "v_mfma_f32_16x16x32_bf16 v[16:19], v[80:83], v[128:131], v[16:19]\n"
    "ds_read_b128 v[112:115], v214 offset:512\n"
    "v_mfma_f32_16x16x32_bf16 v[20:23], v[80:83], v[132:135], v[20:23]\n"
    "ds_read_b128 v[116:119], v214 offset:1536\n"
    "v_mfma_f32_16x16x32_bf16 v[24:27], v[80:83], v[136:139], v[24:27]\n"
    "ds_read_b128 v[120:123], v214 offset:2560\n"
    "v_mfma_f32_16x16x32_bf16 v[28:31], v[80:83], v[140:143], v[28:31]\n"
    "ds_read_b128 v[124:127], v214 offset:3584\n"
    "s_waitcnt lgkmcnt(15)\n"
    "v_mfma_f32_16x16x32_bf16 v[32:35], v[84:87], v[128:131], v[32:35]\n"
    "ds_read_b64 v[48:49], v2 offset:384\n"
    "v_mfma_f32_16x16x32_bf16 v[36:39], v[84:87], v[132:135], v[36:39]\n"
    "ds_read_b64 v[50:51], v2 offset:392\n"
    "v_mfma_f32_16x16x32_bf16 v[40:43], v[84:87], v[136:139], v[40:43]\n"
    "ds_read_b64 v[52:53], v2 offset:416\n"
    "v_mfma_f32_16x16x32_bf16 v[44:47], v[84:87], v[140:143], v[44:47]\n"
    "ds_read_b64 v[54:55], v2 offset:424\n"
    "s_waitcnt lgkmcnt(15)\n"
    "v_mfma_f32_16x16x32_bf16 v[20:23], v[88:91], v[128:131], v[20:23]\n"
    "ds_read_b64 v[56:57], v2 offset:2688\n"
    "ds_read_b64 v[58:59], v2 offset:2696\n"
    "v_mfma_f32_16x16x32_bf16 v[24:27], v[88:91], v[132:135], v[24:27]\n"
    "ds_read_b64 v[60:61], v2 offset:2720\n"
    "v_mfma_f32_16x16x32_bf16 v[28:31], v[88:91], v[136:139], v[28:31]\n"
    "ds_read_b64 v[62:63], v2 offset:2728\n"
    "v_mfma_f32_16x16x32_bf16 v[36:39], v[92:95], v[128:131], v[36:39]\n"
    "ds_read_b64 v[64:65], v2 offset:4992\n"
    "v_mfma_f32_16x16x32_bf16 v[40:43], v[92:95], v[132:135], v[40:43]\n"
    "ds_read_b64 v[66:67], v2 offset:5000\n"
    "v_mfma_f32_16x16x32_bf16 v[44:47], v[92:95], v[136:139], v[44:47]\n"
    "ds_read_b64 v[68:69], v2 offset:5024\n"
    "s_waitcnt lgkmcnt(15)\n"
    "v_mfma_f32_16x16x32_bf16 v[24:27], v[96:99], v[128:131], v[24:27]\n"
    "ds_read_b64 v[70:71], v2 offset:5032\n"
    "v_mfma_f32_16x16x32_bf16 v[28:31], v[96:99], v[132:135], v[28:31]\n"
    "ds_read_b64 v[72:73], v2 offset:7296\n"
    "v_mfma_f32_16x16x32_bf16 v[40:43], v[100:103], v[128:131], v[40:43]\n"
    "ds_read_b64 v[74:75], v2 offset:7304\n"
    "v_mfma_f32_16x16x32_bf16 v[44:47], v[100:103], v[132:135], v[44:47]\n"
    "ds_read_b64 v[76:77], v2 offset:7328\n"
    "ds_read_b64 v[78:79], v2 offset:7336\n"
    "v_mfma_f32_16x16x32_bf16 v[28:31], v[104:107], v[128:131], v[28:31]\n"
    "v_mfma_f32_16x16x32_bf16 v[44:47], v[108:111], v[128:131], v[44:47]\n"
    "s_waitcnt lgkmcnt(14)\n"
    "v_mfma_f32_16x16x32_bf16 v[16:19], v[48:51], v[112:115], v[16:19]\n"
    "ds_read_b128 v[128:131], v215 offset:512\n"
    "v_mfma_f32_16x16x32_bf16 v[20:23], v[48:51], v[116:119], v[20:23]\n"
    "ds_read_b128 v[132:135], v215 offset:1536\n"
    "v_mfma_f32_16x16x32_bf16 v[24:27], v[48:51], v[120:123], v[24:27]\n"
    "ds_read_b128 v[136:139], v215 offset:2560\n"
    "v_mfma_f32_16x16x32_bf16 v[28:31], v[48:51], v[124:127], v[28:31]\n"
    "ds_read_b128 v[140:143], v215 offset:3584\n"
    "s_waitcnt lgkmcnt(15)\n"
    "v_mfma_f32_16x16x32_bf16 v[32:35], v[52:55], v[112:115], v[32:35]\n"
    "ds_read_b64 v[80:81], v2 offset:448\n"
    "v_mfma_f32_16x16x32_bf16 v[36:39], v[52:55], v[116:119], v[36:39]\n"
    "ds_read_b64 v[82:83], v2 offset:456\n"
    "v_mfma_f32_16x16x32_bf16 v[40:43], v[52:55], v[120:123], v[40:43]\n"
    "ds_read_b64 v[84:85], v2 offset:480\n"
    "v_mfma_f32_16x16x32_bf16 v[44:47], v[52:55], v[124:127], v[44:47]\n"
    "ds_read_b64 v[86:87], v2 offset:488\n"
    "s_waitcnt lgkmcnt(15)\n"
    "v_mfma_f32_16x16x32_bf16 v[20:23], v[56:59], v[112:115], v[20:23]\n"
    "ds_read_b64 v[88:89], v2 offset:2752\n"
    "ds_read_b64 v[90:91], v2 offset:2760\n"
    "v_mfma_f32_16x16x32_bf16 v[24:27], v[56:59], v[116:119], v[24:27]\n"
    "ds_read_b64 v[92:93], v2 offset:2784\n"
    "v_mfma_f32_16x16x32_bf16 v[28:31], v[56:59], v[120:123], v[28:31]\n"
    "ds_read_b64 v[94:95], v2 offset:2792\n"
    "v_mfma_f32_16x16x32_bf16 v[36:39], v[60:63], v[112:115], v[36:39]\n"
    "ds_read_b64 v[96:97], v2 offset:5056\n"
    "v_mfma_f32_16x16x32_bf16 v[40:43], v[60:63], v[116:119], v[40:43]\n"
    "ds_read_b64 v[98:99], v2 offset:5064\n"
    "v_mfma_f32_16x16x32_bf16 v[44:47], v[60:63], v[120:123], v[44:47]\n"
    "ds_read_b64 v[100:101], v2 offset:5088\n"
    "s_waitcnt lgkmcnt(15)\n"
    "v_mfma_f32_16x16x32_bf16 v[24:27], v[64:67], v[112:115], v[24:27]\n"
    "ds_read_b64 v[102:103], v2 offset:5096\n"
    "v_mfma_f32_16x16x32_bf16 v[28:31], v[64:67], v[116:119], v[28:31]\n"
    "ds_read_b64 v[104:105], v2 offset:7360\n"
    "v_mfma_f32_16x16x32_bf16 v[40:43], v[68:71], v[112:115], v[40:43]\n"
    "ds_read_b64 v[106:107], v2 offset:7368\n"
    "v_mfma_f32_16x16x32_bf16 v[44:47], v[68:71], v[116:119], v[44:47]\n"
    "ds_read_b64 v[108:109], v2 offset:7392\n"
    "ds_read_b64 v[110:111], v2 offset:7400\n"
    "v_mfma_f32_16x16x32_bf16 v[28:31], v[72:75], v[112:115], v[28:31]\n"
    "v_mfma_f32_16x16x32_bf16 v[44:47], v[76:79], v[112:115], v[44:47]\n"
    "s_waitcnt lgkmcnt(14)\n"
    "v_mfma_f32_16x16x32_bf16 v[16:19], v[80:83], v[128:131], v[16:19]\n"
    "v_mfma_f32_16x16x32_bf16 v[20:23], v[80:83], v[132:135], v[20:23]\n"
    "v_mfma_f32_16x16x32_bf16 v[24:27], v[80:83], v[136:139], v[24:27]\n"
    "v_mfma_f32_16x16x32_bf16 v[28:31], v[80:83], v[140:143], v[28:31]\n"
    "s_waitcnt lgkmcnt(12)\n"
    "v_mfma_f32_16x16x32_bf16 v[32:35], v[84:87], v[128:131], v[32:35]\n"
    "v_mfma_f32_16x16x32_bf16 v[36:39], v[84:87], v[132:135], v[36:39]\n"
    "v_mfma_f32_16x16x32_bf16 v[40:43], v[84:87], v[136:139], v[40:43]\n"
    "v_mfma_f32_16x16x32_bf16 v[44:47], v[84:87], v[140:143], v[44:47]\n"
    "s_waitcnt lgkmcnt(10)\n"
    "v_mfma_f32_16x16x32_bf16 v[20:23], v[88:91], v[128:131], v[20:23]\n"
    "v_mfma_f32_16x16x32_bf16 v[24:27], v[88:91], v[132:135], v[24:27]\n"
    "v_mfma_f32_16x16x32_bf16 v[28:31], v[88:91], v[136:139], v[28:31]\n"
    "s_waitcnt lgkmcnt(8)\n"
    "v_mfma_f32_16x16x32_bf16 v[36:39], v[92:95], v[128:131], v[36:39]\n"
    "v_mfma_f32_16x16x32_bf16 v[40:43], v[92:95], v[132:135], v[40:43]\n"
    "v_mfma_f32_16x16x32_bf16 v[44:47], v[92:95], v[136:139], v[44:47]\n"
    "s_waitcnt lgkmcnt(6)\n"
    "v_mfma_f32_16x16x32_bf16 v[24:27], v[96:99], v[128:131], v[24:27]\n"
    "v_mfma_f32_16x16x32_bf16 v[28:31], v[96:99], v[132:135], v[28:31]\n"
    "s_waitcnt lgkmcnt(4)\n"
    "v_mfma_f32_16x16x32_bf16 v[40:43], v[100:103], v[128:131], v[40:43]\n"
    "v_mfma_f32_16x16x32_bf16 v[44:47], v[100:103], v[132:135], v[44:47]\n"
    "s_waitcnt lgkmcnt(2)\n"
    "v_mfma_f32_16x16x32_bf16 v[28:31], v[104:107], v[128:131], v[28:31]\n"
    "s_waitcnt lgkmcnt(0)\n"
    "v_mfma_f32_16x16x32_bf16 v[44:47], v[108:111], v[128:131], v[44:47]\n"
    "s_mul_i32 s22, s21, 21504\n"
    "v_lshl_add_u32 v3, v1, 4, s22\n"
    "s_lshl_b32 s22, s21, 10\n"
    "s_add_u32 m0, s22, 124928\n"
    "s_waitcnt vmcnt(3)\n"
    "v_alignbit_b32 v160, v145, v144, 16\n"
    "v_alignbit_b32 v161, v146, v145, 16\n"
    "v_alignbit_b32 v162, v147, v146, 16\n"
    "s_waitcnt vmcnt(2)\n"
    "v_alignbit_b32 v163, v148, v147, 16\n"
    "v_alignbit_b32 v164, v149, v148, 16\n"
    "v_alignbit_b32 v165, v150, v149, 16\n"
    "v_alignbit_b32 v166, v151, v150, 16\n"
    "s_waitcnt vmcnt(1)\n"
    "v_alignbit_b32 v167, v152, v151, 16\n"
    "v_alignbit_b32 v168, v153, v152, 16\n"
    "v_alignbit_b32 v169, v154, v153, 16\n"
    "v_alignbit_b32 v170, v155, v154, 16\n"
    "s_waitcnt vmcnt(0)\n"
    "v_alignbit_b32 v171, v156, v155, 16\n"
    "v_alignbit_b32 v172, v146, v145, 16\n"
    "v_alignbit_b32 v173, v147, v146, 16\n"
    "v_alignbit_b32 v174, v148, v147, 16\n"
    "v_alignbit_b32 v175, v149, v148, 16\n"
    "v_alignbit_b32 v176, v150, v149, 16\n"
    "v_alignbit_b32 v177, v151, v150, 16\n"
    "v_alignbit_b32 v178, v152, v151, 16\n"
    "v_alignbit_b32 v179, v153, v152, 16\n"
    "v_alignbit_b32 v180, v154, v153, 16\n"
    "v_alignbit_b32 v181, v155, v154, 16\n"
    "v_alignbit_b32 v182, v156, v155, 16\n"
    "v_alignbit_b32 v183, v157, v156, 16\n"
    "v_mov_b32 v184, v145\n"
    "v_mov_b32 v185, v146\n"
    "v_mov_b32 v186, v147\n"
    "v_mov_b32 v187, v148\n"
    "v_mov_b32 v188, v149\n"
    "v_mov_b32 v189, v150\n"
    "v_mov_b32 v190, v151\n"
    "v_mov_b32 v191, v152\n"
    "v_mov_b32 v192, v153\n"
    "v_mov_b32 v193, v154\n"
    "v_mov_b32 v194, v155\n"
    "v_mov_b32 v195, v156\n"
    "s_mov_b64 s[30:31], exec\n"
    "s_mov_b64 exec, s[28:29]\n"
    "ds_write_b128 v7, v[160:163] offset:0\n"
    "ds_write_b128 v7, v[164:167] offset:16\n"
    "ds_write_b128 v7, v[168:171] offset:32\n"
    "ds_write_b128 v7, v[184:187] offset:576\n"
    "ds_write_b128 v7, v[188:191] offset:592\n"
    "ds_write_b128 v7, v[192:195] offset:608\n"
    "ds_write_b128 v7, v[172:175] offset:1152\n"
    "ds_write_b128 v7, v[176:179] offset:1168\n"
    "ds_write_b128 v7, v[180:183] offset:1184\n"
    "ds_write_b128 v7, v[146:149] offset:1728\n"
    "ds_write_b128 v7, v[150:153] offset:1744\n"
    "ds_write_b128 v7, v[154:157] offset:1760\n"
    "s_mov_b64 exec, s[30:31]\n"
    "s_add_u32 s25, s20, s41\n"
    "s_lshl_b32 s22, s25, 12\n"
    "v_lshl_add_u32 v6, v8, 1, s22\n"
    "buffer_load_dwordx4 v[144:147], v6, s[8:11], 0 offen\n"
    "v_add_u32 v6, 16, v6\n"
    "buffer_load_dwordx4 v[148:151], v6, s[8:11], 0 offen\n"
    "v_add_u32 v6, 16, v6\n"
    "buffer_load_dwordx4 v[152:155], v6, s[8:11], 0 offen\n"
    "v_add_u32 v6, 16, v6\n"
    "buffer_load_dwordx4 v[156:159], v6, s[8:11], 0 offen\n"
    "s_waitcnt lgkmcnt(0)\n"
    "ds_read_b128 v[112:115], v208 offset:0\n"
    "ds_read_b128 v[116:119], v208 offset:1024\n"
    "ds_read_b128 v[120:123], v208 offset:2048\n"
    "ds_read_b128 v[124:127], v208 offset:3072\n"
    "ds_read_b64 v[48:49], v2 offset:0\n"
    "ds_read_b64 v[50:51], v2 offset:8\n"
    "ds_read_b64 v[52:53], v2 offset:32\n"
    "ds_read_b64 v[54:55], v2 offset:40\n"
    "ds_read_b64 v[56:57], v2 offset:2304\n"
    "ds_read_b64 v[58:59], v2 offset:2312\n"
    "ds_read_b64 v[60:61], v2 offset:2336\n"
    "ds_read_b64 v[62:63], v2 offset:2344\n"
    "ds_read_b64 v[64:65], v2 offset:4608\n"
    "ds_read_b64 v[66:67], v2 offset:4616\n"
    "ds_read_b64 v[68:69], v2 offset:4640\n"
    "ds_read_b64 v[70:71], v2 offset:4648\n"
    "ds_read_b64 v[72:73], v2 offset:6912\n"
    "ds_read_b64 v[74:75], v2 offset:6920\n"
    "ds_read_b64 v[76:77], v2 offset:6944\n"
    "ds_read_b64 v[78:79], v2 offset:6952\n"
    "s_waitcnt lgkmcnt(14)\n"
    "v_mfma_f32_16x16x32_bf16 v[16:19], v[48:51], v[112:115], v[16:19]\n"
    "ds_read_b128 v[128:131], v209 offset:0\n"
    "v_mfma_f32_16x16x32_bf16 v[20:23], v[48:51], v[116:119], v[20:23]\n"
    "ds_read_b128 v[132:135], v209 offset:1024\n"
    "v_mfma_f32_16x16x32_bf16 v[24:27], v[48:51], v[120:123], v[24:27]\n"
    "ds_read_b128 v[136:139], v209 offset:2048\n"
    "v_mfma_f32_16x16x32_bf16 v[28:31], v[48:51], v[124:127], v[28:31]\n"
    "ds_read_b128 v[140:143], v209 offset:3072\n"
    "s_waitcnt lgkmcnt(15)\n"
    "v_mfma_f32_16x16x32_bf16 v[32:35], v[52:55], v[112:115], v[32:35]\n"
    "ds_read_b64 v[80:81], v2 offset:64\n"
    "v_mfma_f32_16x16x32_bf16 v[36:39], v[52:55], v[116:119], v[36:39]\n"
    "ds_read_b64 v[82:83], v2 offset:72\n"
    "v_mfma_f32_16x16x32_bf16 v[40:43], v[52:55], v[120:123], v[40:43]\n"
    "ds_read_b64 v[84:85], v2 offset:96\n"
    "v_mfma_f32_16x16x32_bf16 v[44:47], v[52:55], v[124:127], v[44:47]\n"
    "s_mov_b32 s42, 0\n"
    "buffer_load_dwordx4 v3, s[44:47], s42 offen lds\n"
    "ds_read_b64 v[86:87], v2 offset:104\n"
    "s_waitcnt lgkmcnt(15)\n"
    "v_mfma_f32_16x16x32_bf16 v[20:23], v[56:59], v[112:115], v[20:23]\n"
    "ds_read_b64 v[88:89], v2 offset:2368\n"
    "ds_read_b64 v[90:91], v2 offset:2376\n"
    "v_mfma_f32_16x16x32_bf16 v[24:27], v[56:59], v[116:119], v[24:27]\n"
    "ds_read_b64 v[92:93], v2 offset:2400\n"
    "v_mfma_f32_16x16x32_bf16 v[28:31], v[56:59], v[120:123], v[28:31]\n"
    "ds_read_b64 v[94:95], v2 offset:2408\n"
    "v_mfma_f32_16x16x32_bf16 v[36:39], v[60:63], v[112:115], v[36:39]\n"
    "ds_read_b64 v[96:97], v2 offset:4672\n"
    "v_mfma_f32_16x16x32_bf16 v[40:43], v[60:63], v[116:119], v[40:43]\n"
    "ds_read_b64 v[98:99], v2 offset:4680\n"
    "v_mfma_f32_16x16x32_bf16 v[44:47], v[60:63], v[120:123], v[44:47]\n"
    "ds_read_b64 v[100:101], v2 offset:4704\n"
    "s_waitcnt lgkmcnt(15)\n"
    "v_mfma_f32_16x16x32_bf16 v[24:27], v[64:67], v[112:115], v[24:27]\n"
    "ds_read_b64 v[102:103], v2 offset:4712\n"
    "v_mfma_f32_16x16x32_bf16 v[28:31], v[64:67], v[116:119], v[28:31]\n"
    "s_mov_b32 s42, 1024\n"
    "buffer_load_dwordx4 v3, s[44:47], s42 offen lds\n"
    "ds_read_b64 v[104:105], v2 offset:6976\n"
    "v_mfma_f32_16x16x32_bf16 v[40:43], v[68:71], v[112:115], v[40:43]\n"
    "ds_read_b64 v[106:107], v2 offset:6984\n"
    "v_mfma_f32_16x16x32_bf16 v[44:47], v[68:71], v[116:119], v[44:47]\n"
    "ds_read_b64 v[108:109], v2 offset:7008\n"
    "ds_read_b64 v[110:111], v2 offset:7016\n"
    "v_mfma_f32_16x16x32_bf16 v[28:31], v[72:75], v[112:115], v[28:31]\n"
    "v_mfma_f32_16x16x32_bf16 v[44:47], v[76:79], v[112:115], v[44:47]\n"
    "s_waitcnt lgkmcnt(14)\n"
    "v_mfma_f32_16x16x32_bf16 v[16:19], v[80:83], v[128:131], v[16:19]\n"
    "ds_read_b128 v[112:115], v210 offset:0\n"
    "v_mfma_f32_16x16x32_bf16 v[20:23], v[80:83], v[132:135], v[20:23]\n"
    "ds_read_b128 v[116:119], v210 offset:1024\n"
    "v_mfma_f32_16x16x32_bf16 v[24:27], v[80:83], v[136:139], v[24:27]\n"
    "s_mov_b32 s42, 2048\n"
    "buffer_load_dwordx4 v3, s[44:47], s42 offen lds\n"
    "ds_read_b128 v[120:123], v210 offset:2048\n"
    "v_mfma_f32_16x16x32_bf16 v[28:31], v[80:83], v[140:143], v[28:31]\n"
    "ds_read_b128 v[124:127], v210 offset:3072\n"
    "s_waitcnt lgkmcnt(15)\n"
    "v_mfma_f32_16x16x32_bf16 v[32:35], v[84:87], v[128:131], v[32:35]\n"
    "ds_read_b64 v[48:49], v2 offset:128\n"
    "v_mfma_f32_16x16x32_bf16 v[36:39], v[84:87], v[132:135], v[36:39]\n"
    "ds_read_b64 v[50:51], v2 offset:136\n"
    "v_mfma_f32_16x16x32_bf16 v[40:43], v[84:87], v[136:139], v[40:43]\n"
    "ds_read_b64 v[52:53], v2 offset:160\n"
    "v_mfma_f32_16x16x32_bf16 v[44:47], v[84:87], v[140:143], v[44:47]\n"
    "ds_read_b64 v[54:55], v2 offset:168\n"
    "s_waitcnt lgkmcnt(15)\n"
    "v_mfma_f32_16x16x32_bf16 v[20:23], v[88:91], v[128:131], v[20:23]\n"
    "ds_read_b64 v[56:57], v2 offset:2432\n"
    "ds_read_b64 v[58:59], v2 offset:2440\n"
    "v_mfma_f32_16x16x32_bf16 v[24:27], v[88:91], v[132:135], v[24:27]\n"
    "ds_read_b64 v[60:61], v2 offset:2464\n"
    "v_mfma_f32_16x16x32_bf16 v[28:31], v[88:91], v[136:139], v[28:31]\n"
    "s_mov_b32 s42, 3072\n"
    "buffer_load_dwordx4 v3, s[44:47], s42 offen lds\n"
    "ds_read_b64 v[62:63], v2 offset:2472\n"
    "v_mfma_f32_16x16x32_bf16 v[36:39], v[92:95], v[128:131], v[36:39]\n"
    "ds_read_b64 v[64:65], v2 offset:4736\n"
    "v_mfma_f32_16x16x32_bf16 v[40:43], v[92:95], v[132:135], v[40:43]\n"
    "ds_read_b64 v[66:67], v2 offset:4744\n"
    "v_mfma_f32_16x16x32_bf16 v[44:47], v[92:95], v[136:139], v[44:47]\n"
    "ds_read_b64 v[68:69], v2 offset:4768\n"
    "s_waitcnt lgkmcnt(15)\n"
    "v_mfma_f32_16x16x32_bf16 v[24:27], v[96:99], v[128:131], v[24:27]\n"
    "ds_read_b64 v[70:71], v2 offset:4776\n"
    "v_mfma_f32_16x16x32_bf16 v[28:31], v[96:99], v[132:135], v[28:31]\n"
    "ds_read_b64 v[72:73], v2 offset:7040\n"
    "v_mfma_f32_16x16x32_bf16 v[40:43], v[100:103], v[128:131], v[40:43]\n"
    "ds_read_b64 v[74:75], v2 offset:7048\n"
    "v_mfma_f32_16x16x32_bf16 v[44:47], v[100:103], v[132:135], v[44:47]\n"
    "ds_read_b64 v[76:77], v2 offset:7072\n"
    "ds_read_b64 v[78:79], v2 offset:7080\n"
    "v_mfma_f32_16x16x32_bf16 v[28:31], v[104:107], v[128:131], v[28:31]\n"
    "s_mov_b32 s42, 4096\n"
    "buffer_load_dwordx4 v3, s[44:47], s42 offen lds\n"
    "v_mfma_f32_16x16x32_bf16 v[44:47], v[108:111], v[128:131], v[44:47]\n"
    "s_waitcnt lgkmcnt(14)\n"
    "v_mfma_f32_16x16x32_bf16 v[16:19], v[48:51], v[112:115], v[16:19]\n"
    "ds_read_b128 v[128:131], v211 offset:0\n"
    "v_mfma_f32_16x16x32_bf16 v[20:23], v[48:51], v[116:119], v[20:23]\n"
    "ds_read_b128 v[132:135], v211 offset:1024\n"
    "v_mfma_f32_16x16x32_bf16 v[24:27], v[48:51], v[120:123], v[24:27]\n"
    "ds_read_b128 v[136:139], v211 offset:2048\n"
    "v_mfma_f32_16x16x32_bf16 v[28:31], v[48:51], v[124:127], v[28:31]\n"
    "ds_read_b128 v[140:143], v211 offset:3072\n"
    "s_waitcnt lgkmcnt(15)\n"
    "v_mfma_f32_16x16x32_bf16 v[32:35], v[52:55], v[112:115], v[32:35]\n"
    "ds_read_b64 v[80:81], v2 offset:192\n"
    "v_mfma_f32_16x16x32_bf16 v[36:39], v[52:55], v[116:119], v[36:39]\n"
    "s_mov_b32 s42, 5120\n"
    "buffer_load_dwordx4 v3, s[44:47], s42 offen lds\n"
    "ds_read_b64 v[82:83], v2 offset:200\n"
    "v_mfma_f32_16x16x32_bf16 v[40:43], v[52:55], v[120:123], v[40:43]\n"
    "ds_read_b64 v[84:85], v2 offset:224\n"
    "v_mfma_f32_16x16x32_bf16 v[44:47], v[52:55], v[124:127], v[44:47]\n"
    "ds_read_b64 v[86:87], v2 offset:232\n"
    "s_waitcnt lgkmcnt(15)\n"
    "v_mfma_f32_16x16x32_bf16 v[20:23], v[56:59], v[112:115], v[20:23]\n"
    "ds_read_b64 v[88:89], v2 offset:2496\n"
    "ds_read_b64 v[90:91], v2 offset:2504\n"
    "v_mfma_f32_16x16x32_bf16 v[24:27], v[56:59], v[116:119], v[24:27]\n"
    "ds_read_b64 v[92:93], v2 offset:2528\n"
    "v_mfma_f32_16x16x32_bf16 v[28:31], v[56:59], v[120:123], v[28:31]\n"
    "ds_read_b64 v[94:95], v2 offset:2536\n"
    "v_mfma_f32_16x16x32_bf16 v[36:39], v[60:63], v[112:115], v[36:39]\n"
    "ds_read_b64 v[96:97], v2 offset:4800\n"
    "v_mfma_f32_16x16x32_bf16 v[40:43], v[60:63], v[116:119], v[40:43]\n"
    "ds_read_b64 v[98:99], v2 offset:4808\n"
    "v_mfma_f32_16x16x32_bf16 v[44:47], v[60:63], v[120:123], v[44:47]\n"
    "s_mov_b32 s42, 6144\n"
    "buffer_load_dwordx4 v3, s[44:47], s42 offen lds\n"
    "ds_read_b64 v[100:101], v2 offset:4832\n"
    "s_waitcnt lgkmcnt(15)\n"
    "v_mfma_f32_16x16x32_bf16 v[24:27], v[64:67], v[112:115], v[24:27]\n"
    "ds_read_b64 v[102:103], v2 offset:4840\n"
    "v_mfma_f32_16x16x32_bf16 v[28:31], v[64:67], v[116:119], v[28:31]\n"
    "ds_read_b64 v[104:105], v2 offset:7104\n"
    "v_mfma_f32_16x16x32_bf16 v[40:43], v[68:71], v[112:115], v[40:43]\n"
    "ds_read_b64 v[106:107], v2 offset:7112\n"
    "v_mfma_f32_16x16x32_bf16 v[44:47], v[68:71], v[116:119], v[44:47]\n"
    "ds_read_b64 v[108:109], v2 offset:7136\n"
    "ds_read_b64 v[110:111], v2 offset:7144\n"
    "v_mfma_f32_16x16x32_bf16 v[28:31], v[72:75], v[112:115], v[28:31]\n"
    "v_mfma_f32_16x16x32_bf16 v[44:47], v[76:79], v[112:115], v[44:47]\n"
    "s_waitcnt lgkmcnt(14)\n"
    "v_mfma_f32_16x16x32_bf16 v[16:19], v[80:83], v[128:131], v[16:19]\n"
    "s_mov_b32 s42, 7168\n"
    "buffer_load_dwordx4 v3, s[44:47], s42 offen lds\n"
    "ds_read_b128 v[112:115], v212 offset:0\n"
    "v_mfma_f32_16x16x32_bf16 v[20:23], v[80:83], v[132:135], v[20:23]\n"
    "ds_read_b128 v[116:119], v212 offset:1024\n"
    "v_mfma_f32_16x16x32_bf16 v[24:27], v[80:83], v[136:139], v[24:27]\n"
    "ds_read_b128 v[120:123], v212 offset:2048\n"
    "v_mfma_f32_16x16x32_bf16 v[28:31], v[80:83], v[140:143], v[28:31]\n"
    "ds_read_b128 v[124:127], v212 offset:3072\n"
    "s_waitcnt lgkmcnt(15)\n"
    "v_mfma_f32_16x16x32_bf16 v[32:35], v[84:87], v[128:131], v[32:35]\n"
    "ds_read_b64 v[48:49], v2 offset:256\n"
    "v_mfma_f32_16x16x32_bf16 v[36:39], v[84:87], v[132:135], v[36:39]\n"
    "ds_read_b64 v[50:51], v2 offset:264\n"
    "v_mfma_f32_16x16x32_bf16 v[40:43], v[84:87], v[136:139], v[40:43]\n"
    "ds_read_b64 v[52:53], v2 offset:288\n"
    "v_mfma_f32_16x16x32_bf16 v[44:47], v[84:87], v[140:143], v[44:47]\n"
    "ds_read_b64 v[54:55], v2 offset:296\n"
    "s_waitcnt lgkmcnt(15)\n"
    "v_mfma_f32_16x16x32_bf16 v[20:23], v[88:91], v[128:131], v[20:23]\n"
    "s_mov_b32 s42, 8192\n"
    "buffer_load_dwordx4 v3, s[44:47], s42 offen lds\n"
    "ds_read_b64 v[56:57], v2 offset:2560\n"
    "ds_read_b64 v[58:59], v2 offset:2568\n"
    "v_mfma_f32_16x16x32_bf16 v[24:27], v[88:91], v[132:135], v[24:27]\n"
    "ds_read_b64 v[60:61], v2 offset:2592\n"
    "v_mfma_f32_16x16x32_bf16 v[28:31], v[88:91], v[136:139], v[28:31]\n"
    "ds_read_b64 v[62:63], v2 offset:2600\n"
    "v_mfma_f32_16x16x32_bf16 v[36:39], v[92:95], v[128:131], v[36:39]\n"
    "ds_read_b64 v[64:65], v2 offset:4864\n"
    "v_mfma_f32_16x16x32_bf16 v[40:43], v[92:95], v[132:135], v[40:43]\n"
    "ds_read_b64 v[66:67], v2 offset:4872\n"
    "v_mfma_f32_16x16x32_bf16 v[44:47], v[92:95], v[136:139], v[44:47]\n"
    "ds_read_b64 v[68:69], v2 offset:4896\n"
    "s_waitcnt lgkmcnt(15)\n"
    "v_mfma_f32_16x16x32_bf16 v[24:27], v[96:99], v[128:131], v[24:27]\n"
    "ds_read_b64 v[70:71], v2 offset:4904\n"
    "v_mfma_f32_16x16x32_bf16 v[28:31], v[96:99], v[132:135], v[28:31]\n"
    "ds_read_b64 v[72:73], v2 offset:7168\n"
    "v_mfma_f32_16x16x32_bf16 v[40:43], v[100:103], v[128:131], v[40:43]\n"
    "s_mov_b32 s42, 9216\n"
    "buffer_load_dwordx4 v3, s[44:47], s42 offen lds\n"
    "ds_read_b64 v[74:75], v2 offset:7176\n"
    "v_mfma_f32_16x16x32_bf16 v[44:47], v[100:103], v[132:135], v[44:47]\n"
    "ds_read_b64 v[76:77], v2 offset:7200\n"
    "ds_read_b64 v[78:79], v2 offset:7208\n"
    "v_mfma_f32_16x16x32_bf16 v[28:31], v[104:107], v[128:131], v[28:31]\n"
    "v_mfma_f32_16x16x32_bf16 v[44:47], v[108:111], v[128:131], v[44:47]\n"
    "s_waitcnt lgkmcnt(14)\n"
    "v_mfma_f32_16x16x32_bf16 v[16:19], v[48:51], v[112:115], v[16:19]\n"
    "ds_read_b128 v[128:131], v213 offset:0\n"
    "v_mfma_f32_16x16x32_bf16 v[20:23], v[48:51], v[116:119], v[20:23]\n"
    "ds_read_b128 v[132:135], v213 offset:1024\n"
    "v_mfma_f32_16x16x32_bf16 v[24:27], v[48:51], v[120:123], v[24:27]\n"
    "ds_read_b128 v[136:139], v213 offset:2048\n"
    "v_mfma_f32_16x16x32_bf16 v[28:31], v[48:51], v[124:127], v[28:31]\n"
    "s_mov_b32 s42, 10240\n"
    "buffer_load_dwordx4 v3, s[44:47], s42 offen lds\n"
    "ds_read_b128 v[140:143], v213 offset:3072\n"
    "s_waitcnt lgkmcnt(15)\n"
    "v_mfma_f32_16x16x32_bf16 v[32:35], v[52:55], v[112:115], v[32:35]\n"
    "ds_read_b64 v[80:81], v2 offset:320\n"
    "v_mfma_f32_16x16x32_bf16 v[36:39], v[52:55], v[116:119], v[36:39]\n"
    "ds_read_b64 v[82:83], v2 offset:328\n"
    "v_mfma_f32_16x16x32_bf16 v[40:43], v[52:55], v[120:123], v[40:43]\n"
    "ds_read_b64 v[84:85], v2 offset:352\n"
    "v_mfma_f32_16x16x32_bf16 v[44:47], v[52:55], v[124:127], v[44:47]\n"
    "ds_read_b64 v[86:87], v2 offset:360\n"
    "s_waitcnt lgkmcnt(15)\n"
    "v_mfma_f32_16x16x32_bf16 v[20:23], v[56:59], v[112:115], v[20:23]\n"
    "ds_read_b64 v[88:89], v2 offset:2624\n"
    "ds_read_b64 v[90:91], v2 offset:2632\n"
    "v_mfma_f32_16x16x32_bf16 v[24:27], v[56:59], v[116:119], v[24:27]\n"
    "ds_read_b64 v[92:93], v2 offset:2656\n"
    "v_mfma_f32_16x16x32_bf16 v[28:31], v[56:59], v[120:123], v[28:31]\n"
    "ds_read_b64 v[94:95], v2 offset:2664\n"
    "v_mfma_f32_16x16x32_bf16 v[36:39], v[60:63], v[112:115], v[36:39]\n"
    "s_mov_b32 s42, 11264\n"
    "buffer_load_dwordx4 v3, s[44:47], s42 offen lds\n"
    "ds_read_b64 v[96:97], v2 offset:4928\n"
    "v_mfma_f32_16x16x32_bf16 v[40:43], v[60:63], v[116:119], v[40:43]\n"
    "ds_read_b64 v[98:99], v2 offset:4936\n"
    "v_mfma_f32_16x16x32_bf16 v[44:47], v[60:63], v[120:123], v[44:47]\n"
    "ds_read_b64 v[100:101], v2 offset:4960\n"
    "s_waitcnt lgkmcnt(15)\n"
    "v_mfma_f32_16x16x32_bf16 v[24:27], v[64:67], v[112:115], v[24:27]\n"
    "ds_read_b64 v[102:103], v2 offset:4968\n"
    "v_mfma_f32_16x16x32_bf16 v[28:31], v[64:67], v[116:119], v[28:31]\n"
    "ds_read_b64 v[104:105], v2 offset:7232\n"
    "v_mfma_f32_16x16x32_bf16 v[40:43], v[68:71], v[112:115], v[40:43]\n"
    "ds_read_b64 v[106:107], v2 offset:7240\n"
    "v_mfma_f32_16x16x32_bf16 v[44:47], v[68:71], v[116:119], v[44:47]\n"
    "ds_read_b64 v[108:109], v2 offset:7264\n"
    "ds_read_b64 v[110:111], v2 offset:7272\n"
    "v_mfma_f32_16x16x32_bf16 v[28:31], v[72:75], v[112:115], v[28:31]\n"
    "v_mfma_f32_16x16x32_bf16 v[44:47], v[76:79], v[112:115], v[44:47]\n"
    "s_mov_b32 s42, 12288\n"
    "buffer_load_dwordx4 v3, s[44:47], s42 offen lds\n"
    "s_waitcnt lgkmcnt(14)\n"
    "v_mfma_f32_16x16x32_bf16 v[16:19], v[80:83], v[128:131], v[16:19]\n"
    "ds_read_b128 v[112:115], v214 offset:0\n"
    "v_mfma_f32_16x16x32_bf16 v[20:23], v[80:83], v[132:135], v[20:23]\n"
    "ds_read_b128 v[116:119], v214 offset:1024\n"
    "v_mfma_f32_16x16x32_bf16 v[24:27], v[80:83], v[136:139], v[24:27]\n"
    "ds_read_b128 v[120:123], v214 offset:2048\n"
    "v_mfma_f32_16x16x32_bf16 v[28:31], v[80:83], v[140:143], v[28:31]\n"
    "ds_read_b128 v[124:127], v214 offset:3072\n"
    "s_waitcnt lgkmcnt(15)\n"
    "v_mfma_f32_16x16x32_bf16 v[32:35], v[84:87], v[128:131], v[32:35]\n"
    "ds_read_b64 v[48:49], v2 offset:384\n"
    "v_mfma_f32_16x16x32_bf16 v[36:39], v[84:87], v[132:135], v[36:39]\n"
    "ds_read_b64 v[50:51], v2 offset:392\n"
    "v_mfma_f32_16x16x32_bf16 v[40:43], v[84:87], v[136:139], v[40:43]\n"
    "s_mov_b32 s42, 13312\n"
    "buffer_load_dwordx4 v3, s[44:47], s42 offen lds\n"
    "ds_read_b64 v[52:53], v2 offset:416\n"
    "v_mfma_f32_16x16x32_bf16 v[44:47], v[84:87], v[140:143], v[44:47]\n"
    "ds_read_b64 v[54:55], v2 offset:424\n"
    "s_waitcnt lgkmcnt(15)\n"
    "v_mfma_f32_16x16x32_bf16 v[20:23], v[88:91], v[128:131], v[20:23]\n"
    "ds_read_b64 v[56:57], v2 offset:2688\n"
    "ds_read_b64 v[58:59], v2 offset:2696\n"
    "v_mfma_f32_16x16x32_bf16 v[24:27], v[88:91], v[132:135], v[24:27]\n"
    "ds_read_b64 v[60:61], v2 offset:2720\n"
    "v_mfma_f32_16x16x32_bf16 v[28:31], v[88:91], v[136:139], v[28:31]\n"
    "ds_read_b64 v[62:63], v2 offset:2728\n"
    "v_mfma_f32_16x16x32_bf16 v[36:39], v[92:95], v[128:131], v[36:39]\n"
    "ds_read_b64 v[64:65], v2 offset:4992\n"
    "v_mfma_f32_16x16x32_bf16 v[40:43], v[92:95], v[132:135], v[40:43]\n"
    "ds_read_b64 v[66:67], v2 offset:5000\n"
    "v_mfma_f32_16x16x32_bf16 v[44:47], v[92:95], v[136:139], v[44:47]\n"
    "ds_read_b64 v[68:69], v2 offset:5024\n"
    "s_waitcnt lgkmcnt(15)\n"
    "v_mfma_f32_16x16x32_bf16 v[24:27], v[96:99], v[128:131], v[24:27]\n"
    "s_mov_b32 s42, 14336\n"
    "buffer_load_dwordx4 v3, s[44:47], s42 offen lds\n"
    "ds_read_b64 v[70:71], v2 offset:5032\n"
    "v_mfma_f32_16x16x32_bf16 v[28:31], v[96:99], v[132:135], v[28:31]\n"
    "ds_read_b64 v[72:73], v2 offset:7296\n"
    "v_mfma_f32_16x16x32_bf16 v[40:43], v[100:103], v[128:131], v[40:43]\n"
    "ds_read_b64 v[74:75], v2 offset:7304\n"
    "v_mfma_f32_16x16x32_bf16 v[44:47], v[100:103], v[132:135], v[44:47]\n"
    "ds_read_b64 v[76:77], v2 offset:7328\n"
    "ds_read_b64 v[78:79], v2 offset:7336\n"
    "v_mfma_f32_16x16x32_bf16 v[28:31], v[104:107], v[128:131], v[28:31]\n"
    "v_mfma_f32_16x16x32_bf16 v[44:47], v[108:111], v[128:131], v[44:47]\n"
    "s_waitcnt lgkmcnt(14)\n"
    "v_mfma_f32_16x16x32_bf16 v[16:19], v[48:51], v[112:115], v[16:19]\n"
    "ds_read_b128 v[128:131], v215 offset:0\n"
    "v_mfma_f32_16x16x32_bf16 v[20:23], v[48:51], v[116:119], v[20:23]\n"
    "s_mov_b32 s42, 15360\n"
    "buffer_load_dwordx4 v3, s[44:47], s42 offen lds\n"
    "ds_read_b128 v[132:135], v215 offset:1024\n"
    "v_mfma_f32_16x16x32_bf16 v[24:27], v[48:51], v[120:123], v[24:27]\n"
    "ds_read_b128 v[136:139], v215 offset:2048\n"
    "v_mfma_f32_16x16x32_bf16 v[28:31], v[48:51], v[124:127], v[28:31]\n"
    "ds_read_b128 v[140:143], v215 offset:3072\n"
    "s_waitcnt lgkmcnt(15)\n"
    "v_mfma_f32_16x16x32_bf16 v[32:35], v[52:55], v[112:115], v[32:35]\n"
    "ds_read_b64 v[80:81], v2 offset:448\n"
    "v_mfma_f32_16x16x32_bf16 v[36:39], v[52:55], v[116:119], v[36:39]\n"
    "ds_read_b64 v[82:83], v2 offset:456\n"
    "v_mfma_f32_16x16x32_bf16 v[40:43], v[52:55], v[120:123], v[40:43]\n"
    "ds_read_b64 v[84:85], v2 offset:480\n"
    "v_mfma_f32_16x16x32_bf16 v[44:47], v[52:55], v[124:127], v[44:47]\n"
    "ds_read_b64 v[86:87], v2 offset:488\n"
    "s_waitcnt lgkmcnt(15)\n"
    "v_mfma_f32_16x16x32_bf16 v[20:23], v[56:59], v[112:115], v[20:23]\n"
    "ds_read_b64 v[88:89], v2 offset:2752\n"
    "ds_read_b64 v[90:91], v2 offset:2760\n"
    "v_mfma_f32_16x16x32_bf16 v[24:27], v[56:59], v[116:119], v[24:27]\n"
    "s_mov_b32 s42, 16384\n"
    "buffer_load_dwordx4 v3, s[44:47], s42 offen lds\n"
    "ds_read_b64 v[92:93], v2 offset:2784\n"
    "v_mfma_f32_16x16x32_bf16 v[28:31], v[56:59], v[120:123], v[28:31]\n"
    "ds_read_b64 v[94:95], v2 offset:2792\n"
    "v_mfma_f32_16x16x32_bf16 v[36:39], v[60:63], v[112:115], v[36:39]\n"
    "ds_read_b64 v[96:97], v2 offset:5056\n"
    "v_mfma_f32_16x16x32_bf16 v[40:43], v[60:63], v[116:119], v[40:43]\n"
    "ds_read_b64 v[98:99], v2 offset:5064\n"
    "v_mfma_f32_16x16x32_bf16 v[44:47], v[60:63], v[120:123], v[44:47]\n"
    "ds_read_b64 v[100:101], v2 offset:5088\n"
    "s_waitcnt lgkmcnt(15)\n"
    "v_mfma_f32_16x16x32_bf16 v[24:27], v[64:67], v[112:115], v[24:27]\n"
    "ds_read_b64 v[102:103], v2 offset:5096\n"
    "v_mfma_f32_16x16x32_bf16 v[28:31], v[64:67], v[116:119], v[28:31]\n"
    "ds_read_b64 v[104:105], v2 offset:7360\n"
    "v_mfma_f32_16x16x32_bf16 v[40:43], v[68:71], v[112:115], v[40:43]\n"
    "ds_read_b64 v[106:107], v2 offset:7368\n"
    "v_mfma_f32_16x16x32_bf16 v[44:47], v[68:71], v[116:119], v[44:47]\n"
    "s_mov_b32 s42, 17408\n"
    "buffer_load_dwordx4 v3, s[44:47], s42 offen lds\n"
    "ds_read_b64 v[108:109], v2 offset:7392\n"
    "ds_read_b64 v[110:111], v2 offset:7400\n"
    "v_mfma_f32_16x16x32_bf16 v[28:31], v[72:75], v[112:115], v[28:31]\n"
    "v_mfma_f32_16x16x32_bf16 v[44:47], v[76:79], v[112:115], v[44:47]\n"
    "s_waitcnt lgkmcnt(14)\n"
    "v_mfma_f32_16x16x32_bf16 v[16:19], v[80:83], v[128:131], v[16:19]\n"
    "v_mfma_f32_16x16x32_bf16 v[20:23], v[80:83], v[132:135], v[20:23]\n"
    "v_mfma_f32_16x16x32_bf16 v[24:27], v[80:83], v[136:139], v[24:27]\n"
    "v_mfma_f32_16x16x32_bf16 v[28:31], v[80:83], v[140:143], v[28:31]\n"
    "s_waitcnt lgkmcnt(12)\n"
    "v_mfma_f32_16x16x32_bf16 v[32:35], v[84:87], v[128:131], v[32:35]\n"
    "s_mov_b32 s42, 18432\n"
    "buffer_load_dwordx4 v3, s[44:47], s42 offen lds\n"
    "v_mfma_f32_16x16x32_bf16 v[36:39], v[84:87], v[132:135], v[36:39]\n"
    "v_mfma_f32_16x16x32_bf16 v[40:43], v[84:87], v[136:139], v[40:43]\n"
    "v_mfma_f32_16x16x32_bf16 v[44:47], v[84:87], v[140:143], v[44:47]\n"
    "s_waitcnt lgkmcnt(10)\n"
    "v_mfma_f32_16x16x32_bf16 v[20:23], v[88:91], v[128:131], v[20:23]\n"
    "v_mfma_f32_16x16x32_bf16 v[24:27], v[88:91], v[132:135], v[24:27]\n"
    "v_mfma_f32_16x16x32_bf16 v[28:31], v[88:91], v[136:139], v[28:31]\n"
    "s_waitcnt lgkmcnt(8)\n"
    "v_mfma_f32_16x16x32_bf16 v[36:39], v[92:95], v[128:131], v[36:39]\n"
    "v_mfma_f32_16x16x32_bf16 v[40:43], v[92:95], v[132:135], v[40:43]\n"
    "s_mov_b32 s42, 19456\n"
    "buffer_load_dwordx4 v3, s[44:47], s42 offen lds\n"
    "v_mfma_f32_16x16x32_bf16 v[44:47], v[92:95], v[136:139], v[44:47]\n"
    "s_waitcnt lgkmcnt(6)\n"
    "v_mfma_f32_16x16x32_bf16 v[24:27], v[96:99], v[128:131], v[24:27]\n"
    "v_mfma_f32_16x16x32_bf16 v[28:31], v[96:99], v[132:135], v[28:31]\n"
    "s_waitcnt lgkmcnt(4)\n"
    "v_mfma_f32_16x16x32_bf16 v[40:43], v[100:103], v[128:131], v[40:43]\n"
    "v_mfma_f32_16x16x32_bf16 v[44:47], v[100:103], v[132:135], v[44:47]\n"
    "s_waitcnt lgkmcnt(2)\n"
    "v_mfma_f32_16x16x32_bf16 v[28:31], v[104:107], v[128:131], v[28:31]\n"
    "s_waitcnt lgkmcnt(0)\n"
    "v_mfma_f32_16x16x32_bf16 v[44:47], v[108:111], v[128:131], v[44:47]\n"
    "s_mov_b32 s42, 20480\n"
    "buffer_load_dwordx4 v3, s[44:47], s42 offen lds\n"
    "s_add_u32 s23, s20, s21\n"
    "v_sub_u32 v11, 7, v15\n"
    "v_lshrrev_b32 v12, 1, v11\n"
    "v_bfe_u32 v204, v14, 2, 1\n"
    "v_lshlrev_b32 v204, 1, v204\n"
    "v_xor_b32 v12, v12, v204\n"
    "v_and_b32 v11, 1, v11\n"
    "v_lshlrev_b32 v11, 3, v11\n"
    "v_lshl_add_u32 v11, v12, 4, v11\n"
    "v_lshl_add_u32 v11, v14, 6, v11\n"
    "v_add_u32 v204, s26, v11\n"
    "v_sub_u32 v11, 3, v15\n"
    "v_lshrrev_b32 v12, 1, v11\n"
    "v_bfe_u32 v205, v14, 2, 1\n"
    "v_lshlrev_b32 v205, 1, v205\n"
    "v_xor_b32 v12, v12, v205\n"
    "v_and_b32 v11, 1, v11\n"
    "v_lshlrev_b32 v11, 3, v11\n"
    "v_lshl_add_u32 v11, v12, 4, v11\n"
    "v_lshl_add_u32 v11, v14, 6, v11\n"
    "v_add_u32 v205, s26, v11\n"
    "ds_read_b64 v[48:49], v204 offset:1024\n"
    "ds_read_b64 v[50:51], v204 offset:2048\n"
    "ds_read_b64 v[52:53], v204 offset:3072\n"
    "ds_read_b64 v[54:55], v204 offset:4096\n"
    "ds_read_b64 v[56:57], v205 offset:1024\n"
    "ds_read_b64 v[58:59], v205 offset:2048\n"
    "ds_read_b64 v[60:61], v205 offset:3072\n"
    "ds_read_b64 v[62:63], v205 offset:4096\n"
    "s_mul_i32 s22, s23, 4352\n"
    "v_lshlrev_b32 v11, 6, v14\n"
    "v_lshl_add_u32 v11, v15, 3, v11\n"
    "v_add_u32 v9, s22, v11\n"
    "v_lshlrev_b32 v13, 16, v13\n"
    "s_waitcnt lgkmcnt(7)\n"
    "v_and_b32 v80, s24, v49\n"
    "v_lshlrev_b32 v81, 16, v49\n"
    "v_and_b32 v82, s24, v48\n"
    "v_lshlrev_b32 v83, 16, v48\n"
    "v_mul_f32 v80, v80, v13\n"
    "v_mul_f32 v81, v81, v13\n"
    "v_mul_f32 v82, v82, v13\n"
    "v_mul_f32 v83, v83, v13\n"
    "v_cvt_pk_bf16_f32 v84, v80, v81\n"
    "v_cvt_pk_bf16_f32 v85, v82, v83\n"
    "v_cvt_pk_bf16_f32 v86, v16, v17\n"
    "v_cvt_pk_bf16_f32 v87, v18, v19\n"
    "v_lshlrev_b32 v80, 16, v84\n"
    "v_and_b32 v81, s24, v84\n"
    "v_lshlrev_b32 v82, 16, v86\n"
    "v_and_b32 v83, s24, v86\n"
    "v_add_f32 v80, v80, v82\n"
    "v_add_f32 v81, v81, v83\n"
    "v_cvt_pk_bf16_f32 v16, v80, v81\n"
    "v_lshlrev_b32 v80, 16, v85\n"
    "v_and_b32 v81, s24, v85\n"
    "v_lshlrev_b32 v82, 16, v87\n"
    "v_and_b32 v83, s24, v87\n"
    "v_add_f32 v80, v80, v82\n"
    "v_add_f32 v81, v81, v83\n"
    "v_cvt_pk_bf16_f32 v17, v80, v81\n"
    "buffer_store_dwordx2 v[16:17], v9, s[16:19], 0 offen offset:0 sc1\n"
    "s_waitcnt lgkmcnt(6)\n"
    "v_and_b32 v88, s24, v51\n"
    "v_lshlrev_b32 v89, 16, v51\n"
    "v_and_b32 v90, s24, v50\n"
    "v_lshlrev_b32 v91, 16, v50\n"
    "v_mul_f32 v88, v88, v13\n"
    "v_mul_f32 v89, v89, v13\n"
    "v_mul_f32 v90, v90, v13\n"
    "v_mul_f32 v91, v91, v13\n"
    "v_cvt_pk_bf16_f32 v92, v88, v89\n"
    "v_cvt_pk_bf16_f32 v93, v90, v91\n"
    "v_cvt_pk_bf16_f32 v94, v20, v21\n"
    "v_cvt_pk_bf16_f32 v95, v22, v23\n"
    "v_lshlrev_b32 v88, 16, v92\n"
    "v_and_b32 v89, s24, v92\n"
    "v_lshlrev_b32 v90, 16, v94\n"
    "v_and_b32 v91, s24, v94\n"
    "v_add_f32 v88, v88, v90\n"
    "v_add_f32 v89, v89, v91\n"
    "v_cvt_pk_bf16_f32 v20, v88, v89\n"
    "v_lshlrev_b32 v88, 16, v93\n"
    "v_and_b32 v89, s24, v93\n"
    "v_lshlrev_b32 v90, 16, v95\n"
    "v_and_b32 v91, s24, v95\n"
    "v_add_f32 v88, v88, v90\n"
    "v_add_f32 v89, v89, v91\n"
    "v_cvt_pk_bf16_f32 v21, v88, v89\n"
    "buffer_store_dwordx2 v[20:21], v9, s[16:19], 0 offen offset:1024 sc1\n"
    "s_waitcnt lgkmcnt(5)\n"
    "v_and_b32 v80, s24, v53\n"
    "v_lshlrev_b32 v81, 16, v53\n"
    "v_and_b32 v82, s24, v52\n"
    "v_lshlrev_b32 v83, 16, v52\n"
    "v_mul_f32 v80, v80, v13\n"
    "v_mul_f32 v81, v81, v13\n"
    "v_mul_f32 v82, v82, v13\n"
    "v_mul_f32 v83, v83, v13\n"
    "v_cvt_pk_bf16_f32 v84, v80, v81\n"
    "v_cvt_pk_bf16_f32 v85, v82, v83\n"
    "v_cvt_pk_bf16_f32 v86, v24, v25\n"
    "v_cvt_pk_bf16_f32 v87, v26, v27\n"
    "v_lshlrev_b32 v80, 16, v84\n"
    "v_and_b32 v81, s24, v84\n"
    "v_lshlrev_b32 v82, 16, v86\n"
    "v_and_b32 v83, s24, v86\n"
    "v_add_f32 v80, v80, v82\n"
    "v_add_f32 v81, v81, v83\n"
    "v_cvt_pk_bf16_f32 v24, v80, v81\n"
    "v_lshlrev_b32 v80, 16, v85\n"
    "v_and_b32 v81, s24, v85\n"
    "v_lshlrev_b32 v82, 16, v87\n"
    "v_and_b32 v83, s24, v87\n"
    "v_add_f32 v80, v80, v82\n"
    "v_add_f32 v81, v81, v83\n"
    "v_cvt_pk_bf16_f32 v25, v80, v81\n"
    "buffer_store_dwordx2 v[24:25], v9, s[16:19], 0 offen offset:2048 sc1\n"
    "s_waitcnt lgkmcnt(4)\n"
    "v_and_b32 v88, s24, v55\n"
    "v_lshlrev_b32 v89, 16, v55\n"
    "v_and_b32 v90, s24, v54\n"
    "v_lshlrev_b32 v91, 16, v54\n"
    "v_mul_f32 v88, v88, v13\n"
    "v_mul_f32 v89, v89, v13\n"
    "v_mul_f32 v90, v90, v13\n"
    "v_mul_f32 v91, v91, v13\n"
    "v_cvt_pk_bf16_f32 v92, v88, v89\n"
    "v_cvt_pk_bf16_f32 v93, v90, v91\n"
    "v_cvt_pk_bf16_f32 v94, v28, v29\n"
    "v_cvt_pk_bf16_f32 v95, v30, v31\n"
    "v_lshlrev_b32 v88, 16, v92\n"
    "v_and_b32 v89, s24, v92\n"
    "v_lshlrev_b32 v90, 16, v94\n"
    "v_and_b32 v91, s24, v94\n"
    "v_add_f32 v88, v88, v90\n"
    "v_add_f32 v89, v89, v91\n"
    "v_cvt_pk_bf16_f32 v28, v88, v89\n"
    "v_lshlrev_b32 v88, 16, v93\n"
    "v_and_b32 v89, s24, v93\n"
    "v_lshlrev_b32 v90, 16, v95\n"
    "v_and_b32 v91, s24, v95\n"
    "v_add_f32 v88, v88, v90\n"
    "v_add_f32 v89, v89, v91\n"
    "v_cvt_pk_bf16_f32 v29, v88, v89\n"
    "buffer_store_dwordx2 v[28:29], v9, s[16:19], 0 offen offset:3072 sc1\n"
    "s_waitcnt lgkmcnt(3)\n"
    "v_and_b32 v80, s24, v57\n"
    "v_lshlrev_b32 v81, 16, v57\n"
    "v_and_b32 v82, s24, v56\n"
    "v_lshlrev_b32 v83, 16, v56\n"
    "v_mul_f32 v80, v80, v13\n"
    "v_mul_f32 v81, v81, v13\n"
    "v_mul_f32 v82, v82, v13\n"
    "v_mul_f32 v83, v83, v13\n"
    "v_cvt_pk_bf16_f32 v84, v80, v81\n"
    "v_cvt_pk_bf16_f32 v85, v82, v83\n"
    "v_cvt_pk_bf16_f32 v86, v32, v33\n"
    "v_cvt_pk_bf16_f32 v87, v34, v35\n"
    "v_lshlrev_b32 v80, 16, v84\n"
    "v_and_b32 v81, s24, v84\n"
    "v_lshlrev_b32 v82, 16, v86\n"
    "v_and_b32 v83, s24, v86\n"
    "v_add_f32 v80, v80, v82\n"
    "v_add_f32 v81, v81, v83\n"
    "v_cvt_pk_bf16_f32 v32, v80, v81\n"
    "v_lshlrev_b32 v80, 16, v85\n"
    "v_and_b32 v81, s24, v85\n"
    "v_lshlrev_b32 v82, 16, v87\n"
    "v_and_b32 v83, s24, v87\n"
    "v_add_f32 v80, v80, v82\n"
    "v_add_f32 v81, v81, v83\n"
    "v_cvt_pk_bf16_f32 v33, v80, v81\n"
    "buffer_store_dwordx2 v[32:33], v9, s[16:19], 0 offen offset:32 sc1\n"
    "s_waitcnt lgkmcnt(2)\n"
    "v_and_b32 v88, s24, v59\n"
    "v_lshlrev_b32 v89, 16, v59\n"
    "v_and_b32 v90, s24, v58\n"
    "v_lshlrev_b32 v91, 16, v58\n"
    "v_mul_f32 v88, v88, v13\n"
    "v_mul_f32 v89, v89, v13\n"
    "v_mul_f32 v90, v90, v13\n"
    "v_mul_f32 v91, v91, v13\n"
    "v_cvt_pk_bf16_f32 v92, v88, v89\n"
    "v_cvt_pk_bf16_f32 v93, v90, v91\n"
    "v_cvt_pk_bf16_f32 v94, v36, v37\n"
    "v_cvt_pk_bf16_f32 v95, v38, v39\n"
    "v_lshlrev_b32 v88, 16, v92\n"
    "v_and_b32 v89, s24, v92\n"
    "v_lshlrev_b32 v90, 16, v94\n"
    "v_and_b32 v91, s24, v94\n"
    "v_add_f32 v88, v88, v90\n"
    "v_add_f32 v89, v89, v91\n"
    "v_cvt_pk_bf16_f32 v36, v88, v89\n"
    "v_lshlrev_b32 v88, 16, v93\n"
    "v_and_b32 v89, s24, v93\n"
    "v_lshlrev_b32 v90, 16, v95\n"
    "v_and_b32 v91, s24, v95\n"
    "v_add_f32 v88, v88, v90\n"
    "v_add_f32 v89, v89, v91\n"
    "v_cvt_pk_bf16_f32 v37, v88, v89\n"
    "buffer_store_dwordx2 v[36:37], v9, s[16:19], 0 offen offset:1056 sc1\n"
    "s_waitcnt lgkmcnt(1)\n"
    "v_and_b32 v80, s24, v61\n"
    "v_lshlrev_b32 v81, 16, v61\n"
    "v_and_b32 v82, s24, v60\n"
    "v_lshlrev_b32 v83, 16, v60\n"
    "v_mul_f32 v80, v80, v13\n"
    "v_mul_f32 v81, v81, v13\n"
    "v_mul_f32 v82, v82, v13\n"
    "v_mul_f32 v83, v83, v13\n"
    "v_cvt_pk_bf16_f32 v84, v80, v81\n"
    "v_cvt_pk_bf16_f32 v85, v82, v83\n"
    "v_cvt_pk_bf16_f32 v86, v40, v41\n"
    "v_cvt_pk_bf16_f32 v87, v42, v43\n"
    "v_lshlrev_b32 v80, 16, v84\n"
    "v_and_b32 v81, s24, v84\n"
    "v_lshlrev_b32 v82, 16, v86\n"
    "v_and_b32 v83, s24, v86\n"
    "v_add_f32 v80, v80, v82\n"
    "v_add_f32 v81, v81, v83\n"
    "v_cvt_pk_bf16_f32 v40, v80, v81\n"
    "v_lshlrev_b32 v80, 16, v85\n"
    "v_and_b32 v81, s24, v85\n"
    "v_lshlrev_b32 v82, 16, v87\n"
    "v_and_b32 v83, s24, v87\n"
    "v_add_f32 v80, v80, v82\n"
    "v_add_f32 v81, v81, v83\n"
    "v_cvt_pk_bf16_f32 v41, v80, v81\n"
    "buffer_store_dwordx2 v[40:41], v9, s[16:19], 0 offen offset:2080 sc1\n"
    "s_waitcnt lgkmcnt(0)\n"
    "v_and_b32 v88, s24, v63\n"
    "v_lshlrev_b32 v89, 16, v63\n"
    "v_and_b32 v90, s24, v62\n"
    "v_lshlrev_b32 v91, 16, v62\n"
    "v_mul_f32 v88, v88, v13\n"
    "v_mul_f32 v89, v89, v13\n"
    "v_mul_f32 v90, v90, v13\n"
    "v_mul_f32 v91, v91, v13\n"
    "v_cvt_pk_bf16_f32 v92, v88, v89\n"
    "v_cvt_pk_bf16_f32 v93, v90, v91\n"
    "v_cvt_pk_bf16_f32 v94, v44, v45\n"
    "v_cvt_pk_bf16_f32 v95, v46, v47\n"
    "v_lshlrev_b32 v88, 16, v92\n"
    "v_and_b32 v89, s24, v92\n"
    "v_lshlrev_b32 v90, 16, v94\n"
    "v_and_b32 v91, s24, v94\n"
    "v_add_f32 v88, v88, v90\n"
    "v_add_f32 v89, v89, v91\n"
    "v_cvt_pk_bf16_f32 v44, v88, v89\n"
    "v_lshlrev_b32 v88, 16, v93\n"
    "v_and_b32 v89, s24, v93\n"
    "v_lshlrev_b32 v90, 16, v95\n"
    "v_and_b32 v91, s24, v95\n"
    "v_add_f32 v88, v88, v90\n"
    "v_add_f32 v89, v89, v91\n"
    "v_cvt_pk_bf16_f32 v45, v88, v89\n"
    "buffer_store_dwordx2 v[44:45], v9, s[16:19], 0 offen offset:3104 sc1\n"
    "s_waitcnt vmcnt(0) lgkmcnt(0)\n"
    "s_cmp_ge_u32 s21, s40\n"
    "s_cbranch_scc1 L_done_%=\n"
    "s_and_b32 s27, s21, 1\n"
    "s_mul_i32 s26, s41, 5120\n"
    "s_lshl_b32 s22, s27, 5\n"
    "v_add_u32 v3, s22, v2\n"
    "v_cndmask_b32 v144, 0, v144, s[36:37]\n"
    "v_cndmask_b32 v145, 0, v145, s[36:37]\n"
    "v_cndmask_b32 v146, 0, v146, s[36:37]\n"
    "v_cndmask_b32 v147, 0, v147, s[36:37]\n"
    "v_cndmask_b32 v148, 0, v148, s[38:39]\n"
    "v_cndmask_b32 v149, 0, v149, s[38:39]\n"
    "v_cndmask_b32 v150, 0, v150, s[38:39]\n"
    "v_cndmask_b32 v151, 0, v151, s[38:39]\n"
    "v_cndmask_b32 v152, 0, v152, s[38:39]\n"
    "v_cndmask_b32 v153, 0, v153, s[38:39]\n"
    "v_cndmask_b32 v154, 0, v154, s[38:39]\n"
    "v_cndmask_b32 v155, 0, v155, s[38:39]\n"
    "v_cndmask_b32 v156, 0, v156, s[38:39]\n"
    "v_cndmask_b32 v157, 0, v157, s[38:39]\n"
    "v_cndmask_b32 v158, 0, v158, s[38:39]\n"
    "v_cndmask_b32 v159, 0, v159, s[38:39]\n"
    "v_alignbit_b32 v160, v145, v144, 16\n"
    "v_alignbit_b32 v161, v146, v145, 16\n"
    "v_alignbit_b32 v162, v147, v146, 16\n"
    "v_alignbit_b32 v163, v148, v147, 16\n"
    "v_alignbit_b32 v164, v149, v148, 16\n"
    "v_alignbit_b32 v165, v150, v149, 16\n"
    "v_alignbit_b32 v166, v151, v150, 16\n"
    "v_alignbit_b32 v167, v152, v151, 16\n"
    "v_alignbit_b32 v168, v153, v152, 16\n"
    "v_alignbit_b32 v169, v154, v153, 16\n"
    "v_alignbit_b32 v170, v155, v154, 16\n"
    "v_alignbit_b32 v171, v156, v155, 16\n"
    "v_alignbit_b32 v172, v146, v145, 16\n"
    "v_alignbit_b32 v173, v147, v146, 16\n"
    "v_alignbit_b32 v174, v148, v147, 16\n"
    "v_alignbit_b32 v175, v149, v148, 16\n"
    "v_alignbit_b32 v176, v150, v149, 16\n"
    "v_alignbit_b32 v177, v151, v150, 16\n"
    "v_alignbit_b32 v178, v152, v151, 16\n"
    "v_alignbit_b32 v179, v153, v152, 16\n"
    "v_alignbit_b32 v180, v154, v153, 16\n"
    "v_alignbit_b32 v181, v155, v154, 16\n"
    "v_alignbit_b32 v182, v156, v155, 16\n"
    "v_alignbit_b32 v183, v157, v156, 16\n"
    "v_mov_b32 v184, v145\n"
    "v_mov_b32 v185, v146\n"
    "v_mov_b32 v186, v147\n"
    "v_mov_b32 v187, v148\n"
    "v_mov_b32 v188, v149\n"
    "v_mov_b32 v189, v150\n"
    "v_mov_b32 v190, v151\n"
    "v_mov_b32 v191, v152\n"
    "v_mov_b32 v192, v153\n"
    "v_mov_b32 v193, v154\n"
    "v_mov_b32 v194, v155\n"
    "v_mov_b32 v195, v156\n"
    "s_mov_b64 s[30:31], exec\n"
    "s_mov_b64 exec, s[28:29]\n"
    "ds_write_b128 v7, v[160:163] offset:0\n"
    "ds_write_b128 v7, v[164:167] offset:16\n"
    "ds_write_b128 v7, v[168:171] offset:32\n"
    "ds_write_b128 v7, v[184:187] offset:576\n"
    "ds_write_b128 v7, v[188:191] offset:592\n"
    "ds_write_b128 v7, v[192:195] offset:608\n"
    "ds_write_b128 v7, v[172:175] offset:1152\n"
    "ds_write_b128 v7, v[176:179] offset:1168\n"
    "ds_write_b128 v7, v[180:183] offset:1184\n"
    "ds_write_b128 v7, v[146:149] offset:1728\n"
    "ds_write_b128 v7, v[150:153] offset:1744\n"
    "ds_write_b128 v7, v[154:157] offset:1760\n"
    "s_mov_b64 exec, s[30:31]\n"
    "s_add_u32 s25, s20, s41\n"
    "s_lshl_b32 s22, s25, 12\n"
    "s_add_u32 s22, s22, 512\n"
    "v_lshl_add_u32 v6, v8, 1, s22\n"
    "buffer_load_dwordx4 v[144:147], v6, s[8:11], 0 offen\n"
    "v_add_u32 v6, 16, v6\n"
    "buffer_load_dwordx4 v[148:151], v6, s[8:11], 0 offen\n"
    "v_add_u32 v6, 16, v6\n"
    "buffer_load_dwordx4 v[152:155], v6, s[8:11], 0 offen\n"
    "v_add_u32 v6, 16, v6\n"
    "buffer_load_dwordx4 v[156:159], v6, s[8:11], 0 offen\n"
    "s_waitcnt lgkmcnt(0)\n"
    "v_add_u32 v11, 8, v14\n"
    "v_bfe_u32 v12, v11, 2, 1\n"
    "v_lshlrev_b32 v12, 1, v12\n"
    "v_xor_b32 v12, v12, v15\n"
    "v_lshlrev_b32 v11, 6, v11\n"
    "v_lshl_add_u32 v11, v12, 4, v11\n"
    "v_add_u32 v208, s26, v11\n"
    "v_add_u32 v11, 7, v14\n"
    "v_bfe_u32 v12, v11, 2, 1\n"
    "v_lshlrev_b32 v12, 1, v12\n"
    "v_xor_b32 v12, v12, v15\n"
    "v_lshlrev_b32 v11, 6, v11\n"
    "v_lshl_add_u32 v11, v12, 4, v11\n"
    "v_add_u32 v209, s26, v11\n"
    "v_add_u32 v11, 6, v14\n"
    "v_bfe_u32 v12, v11, 2, 1\n"
    "v_lshlrev_b32 v12, 1, v12\n"
    "v_xor_b32 v12, v12, v15\n"
    "v_lshlrev_b32 v11, 6, v11\n"
    "v_lshl_add_u32 v11, v12, 4, v11\n"
    "v_add_u32 v210, s26, v11\n"
    "v_add_u32 v11, 5, v14\n"
    "v_bfe_u32 v12, v11, 2, 1\n"
    "v_lshlrev_b32 v12, 1, v12\n"
    "v_xor_b32 v12, v12, v15\n"
    "v_lshlrev_b32 v11, 6, v11\n"
    "v_lshl_add_u32 v11, v12, 4, v11\n"
    "v_add_u32 v211, s26, v11\n"
    "v_add_u32 v11, 4, v14\n"
    "v_bfe_u32 v12, v11, 2, 1\n"
    "v_lshlrev_b32 v12, 1, v12\n"
    "v_xor_b32 v12, v12, v15\n"
    "v_lshlrev_b32 v11, 6, v11\n"
    "v_lshl_add_u32 v11, v12, 4, v11\n"
    "v_add_u32 v212, s26, v11\n"
    "v_add_u32 v11, 3, v14\n"
    "v_bfe_u32 v12, v11, 2, 1\n"
    "v_lshlrev_b32 v12, 1, v12\n"
    "v_xor_b32 v12, v12, v15\n"
    "v_lshlrev_b32 v11, 6, v11\n"
    "v_lshl_add_u32 v11, v12, 4, v11\n"
    "v_add_u32 v213, s26, v11\n"
    "v_add_u32 v11, 2, v14\n"
    "v_bfe_u32 v12, v11, 2, 1\n"
    "v_lshlrev_b32 v12, 1, v12\n"
    "v_xor_b32 v12, v12, v15\n"
    "v_lshlrev_b32 v11, 6, v11\n"
    "v_lshl_add_u32 v11, v12, 4, v11\n"
    "v_add_u32 v214, s26, v11\n"
    "v_add_u32 v11, 1, v14\n"
    "v_bfe_u32 v12, v11, 2, 1\n"
    "v_lshlrev_b32 v12, 1, v12\n"
    "v_xor_b32 v12, v12, v15\n"
    "v_lshlrev_b32 v11, 6, v11\n"
    "v_lshl_add_u32 v11, v12, 4, v11\n"
    "v_add_u32 v215, s26, v11\n"
    "ds_read_b128 v[112:115], v208 offset:512\n"
    "ds_read_b128 v[116:119], v208 offset:1536\n"
    "ds_read_b128 v[120:123], v208 offset:2560\n"
    "ds_read_b128 v[124:127], v208 offset:3584\n"
    "ds_read_b64 v[48:49], v3 offset:0\n"
    "ds_read_b64 v[50:51], v3 offset:8\n"
    "ds_read_b64 v[56:57], v3 offset:2304\n"
    "ds_read_b64 v[58:59], v3 offset:2312\n"
    "ds_read_b64 v[64:65], v3 offset:4608\n"
    "ds_read_b64 v[66:67], v3 offset:4616\n"
    "ds_read_b64 v[72:73], v3 offset:6912\n"
    "ds_read_b64 v[74:75], v3 offset:6920\n"
    "s_waitcnt lgkmcnt(6)\n"
    "v_mfma_f32_16x16x32_bf16 v[16:19], v[48:51], v[112:115], 0\n"
    "ds_read_b128 v[128:131], v209 offset:512\n"
    "v_mfma_f32_16x16x32_bf16 v[20:23], v[48:51], v[116:119], 0\n"
    "ds_read_b128 v[132:135], v209 offset:1536\n"
    "ds_read_b128 v[136:139], v209 offset:2560\n"
    "v_mfma_f32_16x16x32_bf16 v[24:27], v[48:51], v[120:123], 0\n"
    "ds_read_b128 v[140:143], v209 offset:3584\n"
    "v_mfma_f32_16x16x32_bf16 v[28:31], v[48:51], v[124:127], 0\n"
    "ds_read_b64 v[80:81], v3 offset:64\n"
    "ds_read_b64 v[82:83], v3 offset:72\n"
    "s_waitcnt lgkmcnt(10)\n"
    "v_mfma_f32_16x16x32_bf16 v[20:23], v[56:59], v[112:115], v[20:23]\n"
    "ds_read_b64 v[88:89], v3 offset:2368\n"
    "v_mfma_f32_16x16x32_bf16 v[24:27], v[56:59], v[116:119], v[24:27]\n"
    "ds_read_b64 v[90:91], v3 offset:2376\n"
    "ds_read_b64 v[96:97], v3 offset:4672\n"
    "v_mfma_f32_16x16x32_bf16 v[28:31], v[56:59], v[120:123], v[28:31]\n"
    "ds_read_b64 v[98:99], v3 offset:4680\n"
    "s_waitcnt lgkmcnt(12)\n"
    "v_mfma_f32_16x16x32_bf16 v[24:27], v[64:67], v[112:115], v[24:27]\n"
    "ds_read_b64 v[104:105], v3 offset:6976\n"
    "ds_read_b64 v[106:107], v3 offset:6984\n"
    "v_mfma_f32_16x16x32_bf16 v[28:31], v[64:67], v[116:119], v[28:31]\n"
    "s_waitcnt lgkmcnt(12)\n"
    "v_mfma_f32_16x16x32_bf16 v[28:31], v[72:75], v[112:115], v[28:31]\n"
    "s_waitcnt lgkmcnt(6)\n"
    "v_mfma_f32_16x16x32_bf16 v[16:19], v[80:83], v[128:131], v[16:19]\n"
    "ds_read_b128 v[112:115], v210 offset:512\n"
    "v_mfma_f32_16x16x32_bf16 v[20:23], v[80:83], v[132:135], v[20:23]\n"
    "ds_read_b128 v[116:119], v210 offset:1536\n"
    "ds_read_b128 v[120:123], v210 offset:2560\n"
    "v_mfma_f32_16x16x32_bf16 v[24:27], v[80:83], v[136:139], v[24:27]\n"
    "ds_read_b128 v[124:127], v210 offset:3584\n"
    "v_mfma_f32_16x16x32_bf16 v[28:31], v[80:83], v[140:143], v[28:31]\n"
    "ds_read_b64 v[48:49], v3 offset:128\n"
    "ds_read_b64 v[50:51], v3 offset:136\n"
    "s_waitcnt lgkmcnt(10)\n"
    "v_mfma_f32_16x16x32_bf16 v[20:23], v[88:91], v[128:131], v[20:23]\n"
    "ds_read_b64 v[56:57], v3 offset:2432\n"
    "v_mfma_f32_16x16x32_bf16 v[24:27], v[88:91], v[132:135], v[24:27]\n"
    "ds_read_b64 v[58:59], v3 offset:2440\n"
    "ds_read_b64 v[64:65], v3 offset:4736\n"
    "v_mfma_f32_16x16x32_bf16 v[28:31], v[88:91], v[136:139], v[28:31]\n"
    "ds_read_b64 v[66:67], v3 offset:4744\n"
    "s_waitcnt lgkmcnt(12)\n"
    "v_mfma_f32_16x16x32_bf16 v[24:27], v[96:99], v[128:131], v[24:27]\n"
    "ds_read_b64 v[72:73], v3 offset:7040\n"
    "ds_read_b64 v[74:75], v3 offset:7048\n"
    "v_mfma_f32_16x16x32_bf16 v[28:31], v[96:99], v[132:135], v[28:31]\n"
    "s_waitcnt lgkmcnt(12)\n"
    "v_mfma_f32_16x16x32_bf16 v[28:31], v[104:107], v[128:131], v[28:31]\n"
    "s_waitcnt lgkmcnt(6)\n"
    "v_mfma_f32_16x16x32_bf16 v[16:19], v[48:51], v[112:115], v[16:19]\n"
    "ds_read_b128 v[128:131], v211 offset:512\n"
    "v_mfma_f32_16x16x32_bf16 v[20:23], v[48:51], v[116:119], v[20:23]\n"
    "ds_read_b128 v[132:135], v211 offset:1536\n"
    "ds_read_b128 v[136:139], v211 offset:2560\n"
    "v_mfma_f32_16x16x32_bf16 v[24:27], v[48:51], v[120:123], v[24:27]\n"
    "ds_read_b128 v[140:143], v211 offset:3584\n"
    "v_mfma_f32_16x16x32_bf16 v[28:31], v[48:51], v[124:127], v[28:31]\n"
    "ds_read_b64 v[80:81], v3 offset:192\n"
    "ds_read_b64 v[82:83], v3 offset:200\n"
    "s_waitcnt lgkmcnt(10)\n"
    "v_mfma_f32_16x16x32_bf16 v[20:23], v[56:59], v[112:115], v[20:23]\n"
    "ds_read_b64 v[88:89], v3 offset:2496\n"
    "v_mfma_f32_16x16x32_bf16 v[24:27], v[56:59], v[116:119], v[24:27]\n"
    "ds_read_b64 v[90:91], v3 offset:2504\n"
    "ds_read_b64 v[96:97], v3 offset:4800\n"
    "v_mfma_f32_16x16x32_bf16 v[28:31], v[56:59], v[120:123], v[28:31]\n"
    "ds_read_b64 v[98:99], v3 offset:4808\n"
    "s_waitcnt lgkmcnt(12)\n"
    "v_mfma_f32_16x16x32_bf16 v[24:27], v[64:67], v[112:115], v[24:27]\n"
    "ds_read_b64 v[104:105], v3 offset:7104\n"
    "ds_read_b64 v[106:107], v3 offset:7112\n"
    "v_mfma_f32_16x16x32_bf16 v[28:31], v[64:67], v[116:119], v[28:31]\n"
    "s_waitcnt lgkmcnt(12)\n"
    "v_mfma_f32_16x16x32_bf16 v[28:31], v[72:75], v[112:115], v[28:31]\n"
    "s_waitcnt lgkmcnt(6)\n"
    "v_mfma_f32_16x16x32_bf16 v[16:19], v[80:83], v[128:131], v[16:19]\n"
    "ds_read_b128 v[112:115], v212 offset:512\n"
    "v_mfma_f32_16x16x32_bf16 v[20:23], v[80:83], v[132:135], v[20:23]\n"
    "ds_read_b128 v[116:119], v212 offset:1536\n"
    "ds_read_b128 v[120:123], v212 offset:2560\n"
    "v_mfma_f32_16x16x32_bf16 v[24:27], v[80:83], v[136:139], v[24:27]\n"
    "ds_read_b128 v[124:127], v212 offset:3584\n"
    "v_mfma_f32_16x16x32_bf16 v[28:31], v[80:83], v[140:143], v[28:31]\n"
    "ds_read_b64 v[48:49], v3 offset:256\n"
    "ds_read_b64 v[50:51], v3 offset:264\n"
    "s_waitcnt lgkmcnt(10)\n"
    "v_mfma_f32_16x16x32_bf16 v[20:23], v[88:91], v[128:131], v[20:23]\n"
    "ds_read_b64 v[56:57], v3 offset:2560\n"
    "v_mfma_f32_16x16x32_bf16 v[24:27], v[88:91], v[132:135], v[24:27]\n"
    "ds_read_b64 v[58:59], v3 offset:2568\n"
    "ds_read_b64 v[64:65], v3 offset:4864\n"
    "v_mfma_f32_16x16x32_bf16 v[28:31], v[88:91], v[136:139], v[28:31]\n"
    "ds_read_b64 v[66:67], v3 offset:4872\n"
    "s_waitcnt lgkmcnt(12)\n"
    "v_mfma_f32_16x16x32_bf16 v[24:27], v[96:99], v[128:131], v[24:27]\n"
    "ds_read_b64 v[72:73], v3 offset:7168\n"
    "ds_read_b64 v[74:75], v3 offset:7176\n"
    "v_mfma_f32_16x16x32_bf16 v[28:31], v[96:99], v[132:135], v[28:31]\n"
    "s_waitcnt lgkmcnt(12)\n"
    "v_mfma_f32_16x16x32_bf16 v[28:31], v[104:107], v[128:131], v[28:31]\n"
    "s_waitcnt lgkmcnt(6)\n"
    "v_mfma_f32_16x16x32_bf16 v[16:19], v[48:51], v[112:115], v[16:19]\n"
    "ds_read_b128 v[128:131], v213 offset:512\n"
    "v_mfma_f32_16x16x32_bf16 v[20:23], v[48:51], v[116:119], v[20:23]\n"
    "ds_read_b128 v[132:135], v213 offset:1536\n"
    "ds_read_b128 v[136:139], v213 offset:2560\n"
    "v_mfma_f32_16x16x32_bf16 v[24:27], v[48:51], v[120:123], v[24:27]\n"
    "ds_read_b128 v[140:143], v213 offset:3584\n"
    "v_mfma_f32_16x16x32_bf16 v[28:31], v[48:51], v[124:127], v[28:31]\n"
    "ds_read_b64 v[80:81], v3 offset:320\n"
    "ds_read_b64 v[82:83], v3 offset:328\n"
    "s_waitcnt lgkmcnt(10)\n"
    "v_mfma_f32_16x16x32_bf16 v[20:23], v[56:59], v[112:115], v[20:23]\n"
    "ds_read_b64 v[88:89], v3 offset:2624\n"
    "v_mfma_f32_16x16x32_bf16 v[24:27], v[56:59], v[116:119], v[24:27]\n"
    "ds_read_b64 v[90:91], v3 offset:2632\n"
    "ds_read_b64 v[96:97], v3 offset:4928\n"
    "v_mfma_f32_16x16x32_bf16 v[28:31], v[56:59], v[120:123], v[28:31]\n"
    "ds_read_b64 v[98:99], v3 offset:4936\n"
    "s_waitcnt lgkmcnt(12)\n"
    "v_mfma_f32_16x16x32_bf16 v[24:27], v[64:67], v[112:115], v[24:27]\n"
    "ds_read_b64 v[104:105], v3 offset:7232\n"
    "ds_read_b64 v[106:107], v3 offset:7240\n"
    "v_mfma_f32_16x16x32_bf16 v[28:31], v[64:67], v[116:119], v[28:31]\n"
    "s_waitcnt lgkmcnt(12)\n"
    "v_mfma_f32_16x16x32_bf16 v[28:31], v[72:75], v[112:115], v[28:31]\n"
    "s_waitcnt lgkmcnt(6)\n"
    "v_mfma_f32_16x16x32_bf16 v[16:19], v[80:83], v[128:131], v[16:19]\n"
    "ds_read_b128 v[112:115], v214 offset:512\n"
    "v_mfma_f32_16x16x32_bf16 v[20:23], v[80:83], v[132:135], v[20:23]\n"
    "ds_read_b128 v[116:119], v214 offset:1536\n"
    "ds_read_b128 v[120:123], v214 offset:2560\n"
    "v_mfma_f32_16x16x32_bf16 v[24:27], v[80:83], v[136:139], v[24:27]\n"
    "ds_read_b128 v[124:127], v214 offset:3584\n"
    "v_mfma_f32_16x16x32_bf16 v[28:31], v[80:83], v[140:143], v[28:31]\n"
    "ds_read_b64 v[48:49], v3 offset:384\n"
    "ds_read_b64 v[50:51], v3 offset:392\n"
    "s_waitcnt lgkmcnt(10)\n"
    "v_mfma_f32_16x16x32_bf16 v[20:23], v[88:91], v[128:131], v[20:23]\n"
    "ds_read_b64 v[56:57], v3 offset:2688\n"
    "v_mfma_f32_16x16x32_bf16 v[24:27], v[88:91], v[132:135], v[24:27]\n"
    "ds_read_b64 v[58:59], v3 offset:2696\n"
    "ds_read_b64 v[64:65], v3 offset:4992\n"
    "v_mfma_f32_16x16x32_bf16 v[28:31], v[88:91], v[136:139], v[28:31]\n"
    "ds_read_b64 v[66:67], v3 offset:5000\n"
    "s_waitcnt lgkmcnt(12)\n"
    "v_mfma_f32_16x16x32_bf16 v[24:27], v[96:99], v[128:131], v[24:27]\n"
    "ds_read_b64 v[72:73], v3 offset:7296\n"
    "ds_read_b64 v[74:75], v3 offset:7304\n"
    "v_mfma_f32_16x16x32_bf16 v[28:31], v[96:99], v[132:135], v[28:31]\n"
    "s_waitcnt lgkmcnt(12)\n"
    "v_mfma_f32_16x16x32_bf16 v[28:31], v[104:107], v[128:131], v[28:31]\n"
    "s_waitcnt lgkmcnt(6)\n"
    "v_mfma_f32_16x16x32_bf16 v[16:19], v[48:51], v[112:115], v[16:19]\n"
    "ds_read_b128 v[128:131], v215 offset:512\n"
    "v_mfma_f32_16x16x32_bf16 v[20:23], v[48:51], v[116:119], v[20:23]\n"
    "ds_read_b128 v[132:135], v215 offset:1536\n"
    "ds_read_b128 v[136:139], v215 offset:2560\n"
    "v_mfma_f32_16x16x32_bf16 v[24:27], v[48:51], v[120:123], v[24:27]\n"
    "ds_read_b128 v[140:143], v215 offset:3584\n"
    "v_mfma_f32_16x16x32_bf16 v[28:31], v[48:51], v[124:127], v[28:31]\n"
    "ds_read_b64 v[80:81], v3 offset:448\n"
    "ds_read_b64 v[82:83], v3 offset:456\n"
    "s_waitcnt lgkmcnt(10)\n"
    "v_mfma_f32_16x16x32_bf16 v[20:23], v[56:59], v[112:115], v[20:23]\n"
    "ds_read_b64 v[88:89], v3 offset:2752\n"
    "v_mfma_f32_16x16x32_bf16 v[24:27], v[56:59], v[116:119], v[24:27]\n"
    "ds_read_b64 v[90:91], v3 offset:2760\n"
    "ds_read_b64 v[96:97], v3 offset:5056\n"
    "v_mfma_f32_16x16x32_bf16 v[28:31], v[56:59], v[120:123], v[28:31]\n"
    "ds_read_b64 v[98:99], v3 offset:5064\n"
    "s_waitcnt lgkmcnt(12)\n"
    "v_mfma_f32_16x16x32_bf16 v[24:27], v[64:67], v[112:115], v[24:27]\n"
    "ds_read_b64 v[104:105], v3 offset:7360\n"
    "ds_read_b64 v[106:107], v3 offset:7368\n"
    "v_mfma_f32_16x16x32_bf16 v[28:31], v[64:67], v[116:119], v[28:31]\n"
    "s_waitcnt lgkmcnt(12)\n"
    "v_mfma_f32_16x16x32_bf16 v[28:31], v[72:75], v[112:115], v[28:31]\n"
    "s_waitcnt lgkmcnt(6)\n"
    "v_mfma_f32_16x16x32_bf16 v[16:19], v[80:83], v[128:131], v[16:19]\n"
    "v_mfma_f32_16x16x32_bf16 v[20:23], v[80:83], v[132:135], v[20:23]\n"
    "v_mfma_f32_16x16x32_bf16 v[24:27], v[80:83], v[136:139], v[24:27]\n"
    "v_mfma_f32_16x16x32_bf16 v[28:31], v[80:83], v[140:143], v[28:31]\n"
    "s_waitcnt lgkmcnt(4)\n"
    "v_mfma_f32_16x16x32_bf16 v[20:23], v[88:91], v[128:131], v[20:23]\n"
    "v_mfma_f32_16x16x32_bf16 v[24:27], v[88:91], v[132:135], v[24:27]\n"
    "v_mfma_f32_16x16x32_bf16 v[28:31], v[88:91], v[136:139], v[28:31]\n"
    "s_waitcnt lgkmcnt(2)\n"
    "v_mfma_f32_16x16x32_bf16 v[24:27], v[96:99], v[128:131], v[24:27]\n"
    "v_mfma_f32_16x16x32_bf16 v[28:31], v[96:99], v[132:135], v[28:31]\n"
    "s_waitcnt lgkmcnt(0)\n"
    "v_mfma_f32_16x16x32_bf16 v[28:31], v[104:107], v[128:131], v[28:31]\n"
    "s_waitcnt vmcnt(3)\n"
    "v_alignbit_b32 v160, v145, v144, 16\n"
    "v_alignbit_b32 v161, v146, v145, 16\n"
    "v_alignbit_b32 v162, v147, v146, 16\n"
    "s_waitcnt vmcnt(2)\n"
    "v_alignbit_b32 v163, v148, v147, 16\n"
    "v_alignbit_b32 v164, v149, v148, 16\n"
    "v_alignbit_b32 v165, v150, v149, 16\n"
    "v_alignbit_b32 v166, v151, v150, 16\n"
    "s_waitcnt vmcnt(1)\n"
    "v_alignbit_b32 v167, v152, v151, 16\n"
    "v_alignbit_b32 v168, v153, v152, 16\n"
    "v_alignbit_b32 v169, v154, v153, 16\n"
    "v_alignbit_b32 v170, v155, v154, 16\n"
    "s_waitcnt vmcnt(0)\n"
    "v_alignbit_b32 v171, v156, v155, 16\n"
    "v_alignbit_b32 v172, v146, v145, 16\n"
    "v_alignbit_b32 v173, v147, v146, 16\n"
    "v_alignbit_b32 v174, v148, v147, 16\n"
    "v_alignbit_b32 v175, v149, v148, 16\n"
    "v_alignbit_b32 v176, v150, v149, 16\n"
    "v_alignbit_b32 v177, v151, v150, 16\n"
    "v_alignbit_b32 v178, v152, v151, 16\n"
    "v_alignbit_b32 v179, v153, v152, 16\n"
    "v_alignbit_b32 v180, v154, v153, 16\n"
    "v_alignbit_b32 v181, v155, v154, 16\n"
    "v_alignbit_b32 v182, v156, v155, 16\n"
    "v_alignbit_b32 v183, v157, v156, 16\n"
    "v_mov_b32 v184, v145\n"
    "v_mov_b32 v185, v146\n"
    "v_mov_b32 v186, v147\n"
    "v_mov_b32 v187, v148\n"
    "v_mov_b32 v188, v149\n"
    "v_mov_b32 v189, v150\n"
    "v_mov_b32 v190, v151\n"
    "v_mov_b32 v191, v152\n"
    "v_mov_b32 v192, v153\n"
    "v_mov_b32 v193, v154\n"
    "v_mov_b32 v194, v155\n"
    "v_mov_b32 v195, v156\n"
    "s_mov_b64 s[30:31], exec\n"
    "s_mov_b64 exec, s[28:29]\n"
    "ds_write_b128 v7, v[160:163] offset:0\n"
    "ds_write_b128 v7, v[164:167] offset:16\n"
    "ds_write_b128 v7, v[168:171] offset:32\n"
    "ds_write_b128 v7, v[184:187] offset:576\n"
    "ds_write_b128 v7, v[188:191] offset:592\n"
    "ds_write_b128 v7, v[192:195] offset:608\n"
    "ds_write_b128 v7, v[172:175] offset:1152\n"
    "ds_write_b128 v7, v[176:179] offset:1168\n"
    "ds_write_b128 v7, v[180:183] offset:1184\n"
    "ds_write_b128 v7, v[146:149] offset:1728\n"
    "ds_write_b128 v7, v[150:153] offset:1744\n"
    "ds_write_b128 v7, v[154:157] offset:1760\n"
    "s_mov_b64 exec, s[30:31]\n"
    "s_waitcnt lgkmcnt(0)\n"
    "ds_read_b128 v[112:115], v208 offset:0\n"
    "ds_read_b128 v[116:119], v208 offset:1024\n"
    "ds_read_b128 v[120:123], v208 offset:2048\n"
    "ds_read_b128 v[124:127], v208 offset:3072\n"
    "ds_read_b64 v[48:49], v3 offset:0\n"
    "ds_read_b64 v[50:51], v3 offset:8\n"
    "ds_read_b64 v[56:57], v3 offset:2304\n"
    "ds_read_b64 v[58:59], v3 offset:2312\n"
    "ds_read_b64 v[64:65], v3 offset:4608\n"
    "ds_read_b64 v[66:67], v3 offset:4616\n"
    "ds_read_b64 v[72:73], v3 offset:6912\n"
    "ds_read_b64 v[74:75], v3 offset:6920\n"
    "s_waitcnt lgkmcnt(6)\n"
    "v_mfma_f32_16x16x32_bf16 v[16:19], v[48:51], v[112:115], v[16:19]\n"
    "ds_read_b128 v[128:131], v209 offset:0\n"
    "v_mfma_f32_16x16x32_bf16 v[20:23], v[48:51], v[116:119], v[20:23]\n"
    "ds_read_b128 v[132:135], v209 offset:1024\n"
    "ds_read_b128 v[136:139], v209 offset:2048\n"
    "v_mfma_f32_16x16x32_bf16 v[24:27], v[48:51], v[120:123], v[24:27]\n"
    "ds_read_b128 v[140:143], v209 offset:3072\n"
    "v_mfma_f32_16x16x32_bf16 v[28:31], v[48:51], v[124:127], v[28:31]\n"
    "ds_read_b64 v[80:81], v3 offset:64\n"
    "ds_read_b64 v[82:83], v3 offset:72\n"
    "s_waitcnt lgkmcnt(10)\n"
    "v_mfma_f32_16x16x32_bf16 v[20:23], v[56:59], v[112:115], v[20:23]\n"
    "ds_read_b64 v[88:89], v3 offset:2368\n"
    "v_mfma_f32_16x16x32_bf16 v[24:27], v[56:59], v[116:119], v[24:27]\n"
    "ds_read_b64 v[90:91], v3 offset:2376\n"
    "ds_read_b64 v[96:97], v3 offset:4672\n"
    "v_mfma_f32_16x16x32_bf16 v[28:31], v[56:59], v[120:123], v[28:31]\n"
    "ds_read_b64 v[98:99], v3 offset:4680\n"
    "s_waitcnt lgkmcnt(12)\n"
    "v_mfma_f32_16x16x32_bf16 v[24:27], v[64:67], v[112:115], v[24:27]\n"
    "ds_read_b64 v[104:105], v3 offset:6976\n"
    "ds_read_b64 v[106:107], v3 offset:6984\n"
    "v_mfma_f32_16x16x32_bf16 v[28:31], v[64:67], v[116:119], v[28:31]\n"
    "s_waitcnt lgkmcnt(12)\n"
    "v_mfma_f32_16x16x32_bf16 v[28:31], v[72:75], v[112:115], v[28:31]\n"
    "s_waitcnt lgkmcnt(6)\n"
    "v_mfma_f32_16x16x32_bf16 v[16:19], v[80:83], v[128:131], v[16:19]\n"
    "ds_read_b128 v[112:115], v210 offset:0\n"
    "v_mfma_f32_16x16x32_bf16 v[20:23], v[80:83], v[132:135], v[20:23]\n"
    "ds_read_b128 v[116:119], v210 offset:1024\n"
    "ds_read_b128 v[120:123], v210 offset:2048\n"
    "v_mfma_f32_16x16x32_bf16 v[24:27], v[80:83], v[136:139], v[24:27]\n"
    "ds_read_b128 v[124:127], v210 offset:3072\n"
    "v_mfma_f32_16x16x32_bf16 v[28:31], v[80:83], v[140:143], v[28:31]\n"
    "ds_read_b64 v[48:49], v3 offset:128\n"
    "ds_read_b64 v[50:51], v3 offset:136\n"
    "s_waitcnt lgkmcnt(10)\n"
    "v_mfma_f32_16x16x32_bf16 v[20:23], v[88:91], v[128:131], v[20:23]\n"
    "ds_read_b64 v[56:57], v3 offset:2432\n"
    "v_mfma_f32_16x16x32_bf16 v[24:27], v[88:91], v[132:135], v[24:27]\n"
    "ds_read_b64 v[58:59], v3 offset:2440\n"
    "ds_read_b64 v[64:65], v3 offset:4736\n"
    "v_mfma_f32_16x16x32_bf16 v[28:31], v[88:91], v[136:139], v[28:31]\n"
    "ds_read_b64 v[66:67], v3 offset:4744\n"
    "s_waitcnt lgkmcnt(12)\n"
    "v_mfma_f32_16x16x32_bf16 v[24:27], v[96:99], v[128:131], v[24:27]\n"
    "ds_read_b64 v[72:73], v3 offset:7040\n"
    "ds_read_b64 v[74:75], v3 offset:7048\n"
    "v_mfma_f32_16x16x32_bf16 v[28:31], v[96:99], v[132:135], v[28:31]\n"
    "s_waitcnt lgkmcnt(12)\n"
    "v_mfma_f32_16x16x32_bf16 v[28:31], v[104:107], v[128:131], v[28:31]\n"
    "s_waitcnt lgkmcnt(6)\n"
    "v_mfma_f32_16x16x32_bf16 v[16:19], v[48:51], v[112:115], v[16:19]\n"
    "ds_read_b128 v[128:131], v211 offset:0\n"
    "v_mfma_f32_16x16x32_bf16 v[20:23], v[48:51], v[116:119], v[20:23]\n"
    "ds_read_b128 v[132:135], v211 offset:1024\n"
    "ds_read_b128 v[136:139], v211 offset:2048\n"
    "v_mfma_f32_16x16x32_bf16 v[24:27], v[48:51], v[120:123], v[24:27]\n"
    "ds_read_b128 v[140:143], v211 offset:3072\n"
    "v_mfma_f32_16x16x32_bf16 v[28:31], v[48:51], v[124:127], v[28:31]\n"
    "ds_read_b64 v[80:81], v3 offset:192\n"
    "ds_read_b64 v[82:83], v3 offset:200\n"
    "s_waitcnt lgkmcnt(10)\n"
    "v_mfma_f32_16x16x32_bf16 v[20:23], v[56:59], v[112:115], v[20:23]\n"
    "ds_read_b64 v[88:89], v3 offset:2496\n"
    "v_mfma_f32_16x16x32_bf16 v[24:27], v[56:59], v[116:119], v[24:27]\n"
    "ds_read_b64 v[90:91], v3 offset:2504\n"
    "ds_read_b64 v[96:97], v3 offset:4800\n"
    "v_mfma_f32_16x16x32_bf16 v[28:31], v[56:59], v[120:123], v[28:31]\n"
    "ds_read_b64 v[98:99], v3 offset:4808\n"
    "s_waitcnt lgkmcnt(12)\n"
    "v_mfma_f32_16x16x32_bf16 v[24:27], v[64:67], v[112:115], v[24:27]\n"
    "ds_read_b64 v[104:105], v3 offset:7104\n"
    "ds_read_b64 v[106:107], v3 offset:7112\n"
    "v_mfma_f32_16x16x32_bf16 v[28:31], v[64:67], v[116:119], v[28:31]\n"
    "s_waitcnt lgkmcnt(12)\n"
    "v_mfma_f32_16x16x32_bf16 v[28:31], v[72:75], v[112:115], v[28:31]\n"
    "s_waitcnt lgkmcnt(6)\n"
    "v_mfma_f32_16x16x32_bf16 v[16:19], v[80:83], v[128:131], v[16:19]\n"
    "ds_read_b128 v[112:115], v212 offset:0\n"
    "v_mfma_f32_16x16x32_bf16 v[20:23], v[80:83], v[132:135], v[20:23]\n"
    "ds_read_b128 v[116:119], v212 offset:1024\n"
    "ds_read_b128 v[120:123], v212 offset:2048\n"
    "v_mfma_f32_16x16x32_bf16 v[24:27], v[80:83], v[136:139], v[24:27]\n"
    "ds_read_b128 v[124:127], v212 offset:3072\n"
    "v_mfma_f32_16x16x32_bf16 v[28:31], v[80:83], v[140:143], v[28:31]\n"
    "ds_read_b64 v[48:49], v3 offset:256\n"
    "ds_read_b64 v[50:51], v3 offset:264\n"
    "s_waitcnt lgkmcnt(10)\n"
    "v_mfma_f32_16x16x32_bf16 v[20:23], v[88:91], v[128:131], v[20:23]\n"
    "ds_read_b64 v[56:57], v3 offset:2560\n"
    "v_mfma_f32_16x16x32_bf16 v[24:27], v[88:91], v[132:135], v[24:27]\n"
    "ds_read_b64 v[58:59], v3 offset:2568\n"
    "ds_read_b64 v[64:65], v3 offset:4864\n"
    "v_mfma_f32_16x16x32_bf16 v[28:31], v[88:91], v[136:139], v[28:31]\n"
    "ds_read_b64 v[66:67], v3 offset:4872\n"
    "s_waitcnt lgkmcnt(12)\n"
    "v_mfma_f32_16x16x32_bf16 v[24:27], v[96:99], v[128:131], v[24:27]\n"
    "ds_read_b64 v[72:73], v3 offset:7168\n"
    "ds_read_b64 v[74:75], v3 offset:7176\n"
    "v_mfma_f32_16x16x32_bf16 v[28:31], v[96:99], v[132:135], v[28:31]\n"
    "s_waitcnt lgkmcnt(12)\n"
    "v_mfma_f32_16x16x32_bf16 v[28:31], v[104:107], v[128:131], v[28:31]\n"
    "s_waitcnt lgkmcnt(6)\n"
    "v_mfma_f32_16x16x32_bf16 v[16:19], v[48:51], v[112:115], v[16:19]\n"
    "ds_read_b128 v[128:131], v213 offset:0\n"
    "v_mfma_f32_16x16x32_bf16 v[20:23], v[48:51], v[116:119], v[20:23]\n"
    "ds_read_b128 v[132:135], v213 offset:1024\n"
    "ds_read_b128 v[136:139], v213 offset:2048\n"
    "v_mfma_f32_16x16x32_bf16 v[24:27], v[48:51], v[120:123], v[24:27]\n"
    "ds_read_b128 v[140:143], v213 offset:3072\n"
    "v_mfma_f32_16x16x32_bf16 v[28:31], v[48:51], v[124:127], v[28:31]\n"
    "ds_read_b64 v[80:81], v3 offset:320\n"
    "ds_read_b64 v[82:83], v3 offset:328\n"
    "s_waitcnt lgkmcnt(10)\n"
    "v_mfma_f32_16x16x32_bf16 v[20:23], v[56:59], v[112:115], v[20:23]\n"
    "ds_read_b64 v[88:89], v3 offset:2624\n"
    "v_mfma_f32_16x16x32_bf16 v[24:27], v[56:59], v[116:119], v[24:27]\n"
    "ds_read_b64 v[90:91], v3 offset:2632\n"
    "ds_read_b64 v[96:97], v3 offset:4928\n"
    "v_mfma_f32_16x16x32_bf16 v[28:31], v[56:59], v[120:123], v[28:31]\n"
    "ds_read_b64 v[98:99], v3 offset:4936\n"
    "s_waitcnt lgkmcnt(12)\n"
    "v_mfma_f32_16x16x32_bf16 v[24:27], v[64:67], v[112:115], v[24:27]\n"
    "ds_read_b64 v[104:105], v3 offset:7232\n"
    "ds_read_b64 v[106:107], v3 offset:7240\n"
    "v_mfma_f32_16x16x32_bf16 v[28:31], v[64:67], v[116:119], v[28:31]\n"
    "s_waitcnt lgkmcnt(12)\n"
    "v_mfma_f32_16x16x32_bf16 v[28:31], v[72:75], v[112:115], v[28:31]\n"
    "s_waitcnt lgkmcnt(6)\n"
    "v_mfma_f32_16x16x32_bf16 v[16:19], v[80:83], v[128:131], v[16:19]\n"
    "ds_read_b128 v[112:115], v214 offset:0\n"
    "v_mfma_f32_16x16x32_bf16 v[20:23], v[80:83], v[132:135], v[20:23]\n"
    "ds_read_b128 v[116:119], v214 offset:1024\n"
    "ds_read_b128 v[120:123], v214 offset:2048\n"
    "v_mfma_f32_16x16x32_bf16 v[24:27], v[80:83], v[136:139], v[24:27]\n"
    "ds_read_b128 v[124:127], v214 offset:3072\n"
    "v_mfma_f32_16x16x32_bf16 v[28:31], v[80:83], v[140:143], v[28:31]\n"
    "ds_read_b64 v[48:49], v3 offset:384\n"
    "ds_read_b64 v[50:51], v3 offset:392\n"
    "s_waitcnt lgkmcnt(10)\n"
    "v_mfma_f32_16x16x32_bf16 v[20:23], v[88:91], v[128:131], v[20:23]\n"
    "ds_read_b64 v[56:57], v3 offset:2688\n"
    "v_mfma_f32_16x16x32_bf16 v[24:27], v[88:91], v[132:135], v[24:27]\n"
    "ds_read_b64 v[58:59], v3 offset:2696\n"
    "ds_read_b64 v[64:65], v3 offset:4992\n"
    "v_mfma_f32_16x16x32_bf16 v[28:31], v[88:91], v[136:139], v[28:31]\n"
    "ds_read_b64 v[66:67], v3 offset:5000\n"
    "s_waitcnt lgkmcnt(12)\n"
    "v_mfma_f32_16x16x32_bf16 v[24:27], v[96:99], v[128:131], v[24:27]\n"
    "ds_read_b64 v[72:73], v3 offset:7296\n"
    "ds_read_b64 v[74:75], v3 offset:7304\n"
    "v_mfma_f32_16x16x32_bf16 v[28:31], v[96:99], v[132:135], v[28:31]\n"
    "s_waitcnt lgkmcnt(12)\n"
    "v_mfma_f32_16x16x32_bf16 v[28:31], v[104:107], v[128:131], v[28:31]\n"
    "s_waitcnt lgkmcnt(6)\n"
    "v_mfma_f32_16x16x32_bf16 v[16:19], v[48:51], v[112:115], v[16:19]\n"
    "ds_read_b128 v[128:131], v215 offset:0\n"
    "v_mfma_f32_16x16x32_bf16 v[20:23], v[48:51], v[116:119], v[20:23]\n"
    "ds_read_b128 v[132:135], v215 offset:1024\n"
    "ds_read_b128 v[136:139], v215 offset:2048\n"
    "v_mfma_f32_16x16x32_bf16 v[24:27], v[48:51], v[120:123], v[24:27]\n"
    "ds_read_b128 v[140:143], v215 offset:3072\n"
    "v_mfma_f32_16x16x32_bf16 v[28:31], v[48:51], v[124:127], v[28:31]\n"
    "ds_read_b64 v[80:81], v3 offset:448\n"
    "ds_read_b64 v[82:83], v3 offset:456\n"
    "s_waitcnt lgkmcnt(10)\n"
    "v_mfma_f32_16x16x32_bf16 v[20:23], v[56:59], v[112:115], v[20:23]\n"
    "ds_read_b64 v[88:89], v3 offset:2752\n"
    "v_mfma_f32_16x16x32_bf16 v[24:27], v[56:59], v[116:119], v[24:27]\n"
    "ds_read_b64 v[90:91], v3 offset:2760\n"
    "ds_read_b64 v[96:97], v3 offset:5056\n"
    "v_mfma_f32_16x16x32_bf16 v[28:31], v[56:59], v[120:123], v[28:31]\n"
    "ds_read_b64 v[98:99], v3 offset:5064\n"
    "s_waitcnt lgkmcnt(12)\n"
    "v_mfma_f32_16x16x32_bf16 v[24:27], v[64:67], v[112:115], v[24:27]\n"
    "ds_read_b64 v[104:105], v3 offset:7360\n"
    "ds_read_b64 v[106:107], v3 offset:7368\n"
    "v_mfma_f32_16x16x32_bf16 v[28:31], v[64:67], v[116:119], v[28:31]\n"
    "s_waitcnt lgkmcnt(12)\n"
    "v_mfma_f32_16x16x32_bf16 v[28:31], v[72:75], v[112:115], v[28:31]\n"
    "s_waitcnt lgkmcnt(6)\n"
    "v_mfma_f32_16x16x32_bf16 v[16:19], v[80:83], v[128:131], v[16:19]\n"
    "v_mfma_f32_16x16x32_bf16 v[20:23], v[80:83], v[132:135], v[20:23]\n"
    "v_mfma_f32_16x16x32_bf16 v[24:27], v[80:83], v[136:139], v[24:27]\n"
    "v_mfma_f32_16x16x32_bf16 v[28:31], v[80:83], v[140:143], v[28:31]\n"
    "s_waitcnt lgkmcnt(4)\n"
    "v_mfma_f32_16x16x32_bf16 v[20:23], v[88:91], v[128:131], v[20:23]\n"
    "v_mfma_f32_16x16x32_bf16 v[24:27], v[88:91], v[132:135], v[24:27]\n"
    "v_mfma_f32_16x16x32_bf16 v[28:31], v[88:91], v[136:139], v[28:31]\n"
    "s_waitcnt lgkmcnt(2)\n"
    "v_mfma_f32_16x16x32_bf16 v[24:27], v[96:99], v[128:131], v[24:27]\n"
    "v_mfma_f32_16x16x32_bf16 v[28:31], v[96:99], v[132:135], v[28:31]\n"
    "s_waitcnt lgkmcnt(0)\n"
    "v_mfma_f32_16x16x32_bf16 v[28:31], v[104:107], v[128:131], v[28:31]\n"
    "s_add_u32 s23, s20, s41\n"
    "s_lshl_b32 s22, s27, 2\n"
    "s_sub_u32 s22, 7, s22\n"
    "v_sub_u32 v11, s22, v15\n"
    "v_lshrrev_b32 v12, 1, v11\n"
    "v_bfe_u32 v204, v14, 2, 1\n"
    "v_lshlrev_b32 v204, 1, v204\n"
    "v_xor_b32 v12, v12, v204\n"
    "v_and_b32 v11, 1, v11\n"
    "v_lshlrev_b32 v11, 3, v11\n"
    "v_lshl_add_u32 v11, v12, 4, v11\n"
    "v_lshl_add_u32 v11, v14, 6, v11\n"
    "v_add_u32 v204, s26, v11\n"
    "ds_read_b64 v[48:49], v204 offset:1024\n"
    "ds_read_b64 v[50:51], v204 offset:2048\n"
    "ds_read_b64 v[52:53], v204 offset:3072\n"
    "ds_read_b64 v[54:55], v204 offset:4096\n"
    "s_mul_i32 s22, s23, 4352\n"
    "s_lshl_b32 s23, s27, 5\n"
    "s_add_u32 s22, s22, s23\n"
    "v_lshlrev_b32 v11, 6, v14\n"
    "v_lshl_add_u32 v11, v15, 3, v11\n"
    "v_add_u32 v9, s22, v11\n"
    "v_lshlrev_b32 v10, 16, v10\n"
    "s_waitcnt lgkmcnt(3)\n"
    "v_and_b32 v80, s24, v49\n"
    "v_lshlrev_b32 v81, 16, v49\n"
    "v_and_b32 v82, s24, v48\n"
    "v_lshlrev_b32 v83, 16, v48\n"
    "v_mul_f32 v80, v80, v10\n"
    "v_mul_f32 v81, v81, v10\n"
    "v_mul_f32 v82, v82, v10\n"
    "v_mul_f32 v83, v83, v10\n"
    "v_cvt_pk_bf16_f32 v84, v80, v81\n"
    "v_cvt_pk_bf16_f32 v85, v82, v83\n"
    "v_cvt_pk_bf16_f32 v86, v16, v17\n"
    "v_cvt_pk_bf16_f32 v87, v18, v19\n"
    "v_lshlrev_b32 v80, 16, v84\n"
    "v_and_b32 v81, s24, v84\n"
    "v_lshlrev_b32 v82, 16, v86\n"
    "v_and_b32 v83, s24, v86\n"
    "v_add_f32 v80, v80, v82\n"
    "v_add_f32 v81, v81, v83\n"
    "v_cvt_pk_bf16_f32 v16, v80, v81\n"
    "v_lshlrev_b32 v80, 16, v85\n"
    "v_and_b32 v81, s24, v85\n"
    "v_lshlrev_b32 v82, 16, v87\n"
    "v_and_b32 v83, s24, v87\n"
    "v_add_f32 v80, v80, v82\n"
    "v_add_f32 v81, v81, v83\n"
    "v_cvt_pk_bf16_f32 v17, v80, v81\n"
    "buffer_store_dwordx2 v[16:17], v9, s[16:19], 0 offen offset:0 sc1\n"
    "s_waitcnt lgkmcnt(2)\n"
    "v_and_b32 v88, s24, v51\n"
    "v_lshlrev_b32 v89, 16, v51\n"
    "v_and_b32 v90, s24, v50\n"
    "v_lshlrev_b32 v91, 16, v50\n"
    "v_mul_f32 v88, v88, v10\n"
    "v_mul_f32 v89, v89, v10\n"
    "v_mul_f32 v90, v90, v10\n"
    "v_mul_f32 v91, v91, v10\n"
    "v_cvt_pk_bf16_f32 v92, v88, v89\n"
    "v_cvt_pk_bf16_f32 v93, v90, v91\n"
    "v_cvt_pk_bf16_f32 v94, v20, v21\n"
    "v_cvt_pk_bf16_f32 v95, v22, v23\n"
    "v_lshlrev_b32 v88, 16, v92\n"
    "v_and_b32 v89, s24, v92\n"
    "v_lshlrev_b32 v90, 16, v94\n"
    "v_and_b32 v91, s24, v94\n"
    "v_add_f32 v88, v88, v90\n"
    "v_add_f32 v89, v89, v91\n"
    "v_cvt_pk_bf16_f32 v20, v88, v89\n"
    "v_lshlrev_b32 v88, 16, v93\n"
    "v_and_b32 v89, s24, v93\n"
    "v_lshlrev_b32 v90, 16, v95\n"
    "v_and_b32 v91, s24, v95\n"
    "v_add_f32 v88, v88, v90\n"
    "v_add_f32 v89, v89, v91\n"
    "v_cvt_pk_bf16_f32 v21, v88, v89\n"
    "buffer_store_dwordx2 v[20:21], v9, s[16:19], 0 offen offset:1024 sc1\n"
    "s_waitcnt lgkmcnt(1)\n"
    "v_and_b32 v80, s24, v53\n"
    "v_lshlrev_b32 v81, 16, v53\n"
    "v_and_b32 v82, s24, v52\n"
    "v_lshlrev_b32 v83, 16, v52\n"
    "v_mul_f32 v80, v80, v10\n"
    "v_mul_f32 v81, v81, v10\n"
    "v_mul_f32 v82, v82, v10\n"
    "v_mul_f32 v83, v83, v10\n"
    "v_cvt_pk_bf16_f32 v84, v80, v81\n"
    "v_cvt_pk_bf16_f32 v85, v82, v83\n"
    "v_cvt_pk_bf16_f32 v86, v24, v25\n"
    "v_cvt_pk_bf16_f32 v87, v26, v27\n"
    "v_lshlrev_b32 v80, 16, v84\n"
    "v_and_b32 v81, s24, v84\n"
    "v_lshlrev_b32 v82, 16, v86\n"
    "v_and_b32 v83, s24, v86\n"
    "v_add_f32 v80, v80, v82\n"
    "v_add_f32 v81, v81, v83\n"
    "v_cvt_pk_bf16_f32 v24, v80, v81\n"
    "v_lshlrev_b32 v80, 16, v85\n"
    "v_and_b32 v81, s24, v85\n"
    "v_lshlrev_b32 v82, 16, v87\n"
    "v_and_b32 v83, s24, v87\n"
    "v_add_f32 v80, v80, v82\n"
    "v_add_f32 v81, v81, v83\n"
    "v_cvt_pk_bf16_f32 v25, v80, v81\n"
    "buffer_store_dwordx2 v[24:25], v9, s[16:19], 0 offen offset:2048 sc1\n"
    "s_waitcnt lgkmcnt(0)\n"
    "v_and_b32 v88, s24, v55\n"
    "v_lshlrev_b32 v89, 16, v55\n"
    "v_and_b32 v90, s24, v54\n"
    "v_lshlrev_b32 v91, 16, v54\n"
    "v_mul_f32 v88, v88, v10\n"
    "v_mul_f32 v89, v89, v10\n"
    "v_mul_f32 v90, v90, v10\n"
    "v_mul_f32 v91, v91, v10\n"
    "v_cvt_pk_bf16_f32 v92, v88, v89\n"
    "v_cvt_pk_bf16_f32 v93, v90, v91\n"
    "v_cvt_pk_bf16_f32 v94, v28, v29\n"
    "v_cvt_pk_bf16_f32 v95, v30, v31\n"
    "v_lshlrev_b32 v88, 16, v92\n"
    "v_and_b32 v89, s24, v92\n"
    "v_lshlrev_b32 v90, 16, v94\n"
    "v_and_b32 v91, s24, v94\n"
    "v_add_f32 v88, v88, v90\n"
    "v_add_f32 v89, v89, v91\n"
    "v_cvt_pk_bf16_f32 v28, v88, v89\n"
    "v_lshlrev_b32 v88, 16, v93\n"
    "v_and_b32 v89, s24, v93\n"
    "v_lshlrev_b32 v90, 16, v95\n"
    "v_and_b32 v91, s24, v95\n"
    "v_add_f32 v88, v88, v90\n"
    "v_add_f32 v89, v89, v91\n"
    "v_cvt_pk_bf16_f32 v29, v88, v89\n"
    "buffer_store_dwordx2 v[28:29], v9, s[16:19], 0 offen offset:3072 sc1\n"
    "s_waitcnt vmcnt(0) lgkmcnt(0)\n"
    "L_done_%=:\n"
          :
          : "s"(gp), "s"(Dp), "s"(zT), "s"(c0), "s"(nex), "s"(pw2), "s"(pw1), "{v0}"(threadIdx.x)
          : "memory", "vcc", "scc",
        "v1", "v2", "v3", "v4", "v5", "v6", "v7", "v8", "v9", "v10", "v11", "v12", "v13", "v14", "v15", "v16", "v17", "v18", "v19", "v20", "v21", "v22", "v23", "v24", "v25", "v26", "v27", "v28", "v29", "v30", "v31", "v32", "v33", "v34", "v35", "v36", "v37", "v38", "v39", "v40", "v41", "v42", "v43", "v44", "v45", "v46", "v47", "v48", "v49", "v50", "v51", "v52", "v53", "v54", "v55", "v56", "v57", "v58", "v59", "v60", "v61", "v62", "v63", "v64", "v65", "v66", "v67", "v68", "v69", "v70", "v71", "v72", "v73", "v74", "v75", "v76", "v77", "v78", "v79", "v80", "v81", "v82", "v83", "v84", "v85", "v86", "v87", "v88", "v89", "v90", "v91", "v92", "v93", "v94", "v95", "v96", "v97", "v98", "v99", "v100", "v101", "v102", "v103", "v104", "v105", "v106", "v107", "v108", "v109", "v110", "v111", "v112", "v113", "v114", "v115", "v116", "v117", "v118", "v119", "v120", "v121", "v122", "v123", "v124", "v125", "v126", "v127", "v128", "v129", "v130", "v131", "v132", "v133", "v134", "v135", "v136", "v137", "v138", "v139", "v140", "v141", "v142", "v143", "v144", "v145", "v146", "v147", "v148", "v149", "v150", "v151", "v152", "v153", "v154", "v155", "v156", "v157", "v158", "v159", "v160", "v161", "v162", "v163", "v164", "v165", "v166", "v167", "v168", "v169", "v170", "v171", "v172", "v173", "v174", "v175", "v176", "v177", "v178", "v179", "v180", "v181", "v182", "v183", "v184", "v185", "v186", "v187", "v188", "v189", "v190", "v191", "v192", "v193", "v194", "v195", "v196", "v197", "v198", "v199", "v200", "v201", "v202", "v203", "v204", "v205", "v206", "v207", "v208", "v209", "v210", "v211", "v212", "v213", "v214", "v215", "s8", "s9", "s10", "s11", "s12", "s13", "s14", "s15", "s16", "s17", "s18", "s19", "s20", "s21", "s22", "s23", "s24", "s25", "s26", "s27", "s28", "s29", "s30", "s31", "s36", "s37", "s38", "s39", "s40", "s41", "s42", "s43", "s44", "s45", "s46", "s47", "s52", "s53", "s54", "s55"
      );
    }
    if (tid < 4) ((volatile LAS u32*)(sm + 143360))[tid] = ~tag;
    __syncthreads();
    put_flag(rc, CT_ZF + 128 * cw, tag, tid);
  } else {
    tag = (u32)__builtin_amdgcn_readfirstlane(epoch) + 1u;
    put_flag(rc, CT_SEEN + 128 * blockIdx.x, tag, tid);
    if (tid < 4) ((volatile LAS u32*)(sm + 143360))[tid] = ~tag;
    __syncthreads();
    if (tid >= 448) {
      for (int it = 0; it < (1 << 14); ++it) {
        bool ok = true;
#pragma unroll
        for (int i = 0; i < 4; ++i) {
          const u32x2 v = __builtin_amdgcn_raw_buffer_load_b64(rc, CT_SEEN + 128u * (u32)(64 * i + l), 0, SC1);
          ok = ok && v[0] == tag && v[1] == ~tag;
        }
        if (__all(ok)) break;
        __builtin_amdgcn_s_sleep(8);
      }
      if (l == 0) __builtin_amdgcn_raw_buffer_store_b32(tag, rc, 0, 0, SC1);
    }
  }
  STAMP(12);
  const int w = __builtin_amdgcn_readfirstlane(tid >> 6);
  {
    const int xq = cw >> 3, jj = cw & 7;
    const int rrb = 8 * (1 - (xq & 1)) + 2 * (xq >> 1) + (jj >> 2), rtg = jj & 3;
    reduce_units<3>(rrb, rtg, w >> 2, w & 3, tag, x, b2, ws, out, sm + w * 4096, (volatile LAS u32*)(sm + 143360) + 2 * (w >> 2), l);
  }
  STAMP(13);
  STAMP(14);
}

__global__ __launch_bounds__(512, 2) void kA(const u16* __restrict__ x, const u16* __restrict__ kp, const u16* __restrict__ vp, const u16* __restrict__ skp,
                                             const u16* __restrict__ gp, const u16* __restrict__ Dp, const u16* __restrict__ w1, const u16* __restrict__ b1,
                                             const u16* __restrict__ w2, const u16* __restrict__ b2, u16* __restrict__ out, char* __restrict__ ws) {
  extern __shared__ __attribute__((aligned(16))) char smem[];
  const int b = blockIdx.x, xcd = b & 7, j = b >> 3;
  if (j < 24) {
    const int rb = 8 * (xcd & 1) + (j & 7), sp = 3 * (xcd >> 1) + (j >> 3);
    const char* px = (const char*)x + (size_t)rb * 128 * 1152;
    const char* pw1 = (const char*)w1 + (size_t)sp * FC * 2;
    const char* pw2 = (const char*)w2 + (size_t)sp * FC * 1152;
    const char* pb1 = (const char*)b1 + (size_t)sp * FC * 2;
    char* pp = ws + WS_PART + (size_t)(rb * NSP + sp) * SLAB;
    char* ps = ws + WS_STATS + (size_t)rb * 1024;
    char* pc = ws + WS_CTRL;
    const u32 stn = sp == 0 ? 1024u : 0u;
    const u32 oseen = CT_SEEN + 128 * b, opf = CT_PF + (rb * 9 * 12 + sp) * 128;
    u32 tag;
    asm volatile(
    "s_mov_b64 s[8:9], %1\n"
    "s_mov_b64 s[12:13], %2\n"
    "s_mov_b64 s[16:17], %3\n"
    "s_mov_b64 s[20:21], %4\n"
    "s_mov_b64 s[24:25], %5\n"
    "s_mov_b64 s[28:29], %6\n"
    "s_mov_b32 s37, %7\n"
    "s_mov_b64 s[48:49], %8\n"
    "s_mov_b32 s52, %9\n"
    "s_mov_b32 s53, %10\n"
    "s_and_b32 s9, s9, 0xffff\n"
    "s_mov_b32 s10, 147456\n"
    "s_mov_b32 s11, 0x20000\n"
    "s_sub_u32 s12, s12, 4096\n"
    "s_subb_u32 s13, s13, 0\n"
    "s_and_b32 s13, s13, 0xffff\n"
    "s_mov_b32 s14, 5304064\n"
    "s_mov_b32 s15, 0x20000\n"
    "s_sub_u32 s16, s16, 4096\n"
    "s_subb_u32 s17, s17, 0\n"
    "s_and_b32 s17, s17, 0xffff\n"
    "s_mov_b32 s18, 446464\n"
    "s_mov_b32 s19, 0x20000\n"
    "s_and_b32 s21, s21, 0xffff\n"
    "s_mov_b32 s22, 768\n"
    "s_mov_b32 s23, 0x20000\n"
    "s_and_b32 s25, s25, 0xffff\n"
    "s_mov_b32 s26, 147456\n"
    "s_mov_b32 s27, 0x20000\n"
    "s_and_b32 s29, s29, 0xffff\n"
    "s_mov_b32 s30, s37\n"
    "s_mov_b32 s31, 0x20000\n"
    "s_and_b32 s49, s49, 0xffff\n"
    "s_mov_b32 s50, 327680\n"
    "s_mov_b32 s51, 0x20000\n"
    "v_and_b32 v14, 63, v0\n"
    "v_lshrrev_b32 v15, 6, v0\n"
    "s_nop 0\n"
    "v_readfirstlane_b32 s44, v15\n"
    "s_nop 3\n"
    "s_and_b32 s45, s44, 3\n"
    "s_lshr_b32 s46, s44, 2\n"
    "s_lshl_b32 s42, s44, 13\n"
    "v_mov_b32 v8, 0\n"
    "buffer_load_dword v8, v8, s[48:51], 0 offen sc1\n"
    "v_lshlrev_b32 v2, 1, v0\n"
    "buffer_load_ushort v15, v2, s[20:23], 0 offen\n"
    "s_mov_b32 s40, 0xffff0000\n"
    "s_mov_b32 s41, 0x3f803f80\n"
    "s_mov_b32 s54, 65536\n"
    "s_mov_b32 s55, 73728\n"
    "v_and_b32 v248, 15, v14\n"
    "v_lshrrev_b32 v249, 4, v14\n"
    "v_lshrrev_b32 v246, 3, v14\n"
    "v_and_b32 v247, 7, v14\n"
    "s_lshl_b32 s38, s45, 5\n"
    "s_lshl_b32 s39, s46, 4\n"
    "s_add_u32 s38, s38, s39\n"
    "v_add_u32 v252, s38, v248\n"
    "s_mul_i32 s38, s38, 1152\n"
    "v_lshrrev_b32 v240, 1, v246\n"
    "v_xor_b32 v241, v240, v247\n"
    "v_mul_u32_u24 v242, 1152, v246\n"
    "v_lshl_add_u32 v156, v241, 4, v242\n"
    "v_add_u32 v156, s38, v156\n"
    "v_xor_b32 v241, 4, v241\n"
    "v_lshl_add_u32 v157, v241, 4, v242\n"
    "s_add_u32 s38, s38, 9216\n"
    "v_add_u32 v157, s38, v157\n"
    "s_mul_i32 s39, s44, 12288\n"
    "s_add_u32 s39, s39, 65536\n"
    "s_lshl_b32 s47, s44, 13\n"
    "s_add_u32 s47, s47, 2048\n"
    "s_add_u32 m0, s39, 0\n"
    "s_mov_b32 s43, 0\n"
    "buffer_load_dwordx4 v156, s[8:11], s43 offen lds\n"
    "s_add_u32 m0, s39, 1024\n"
    "s_mov_b32 s43, 0\n"
    "buffer_load_dwordx4 v157, s[8:11], s43 offen lds\n"
    "s_add_u32 m0, s39, 2048\n"
    "s_mov_b32 s43, 128\n"
    "buffer_load_dwordx4 v156, s[8:11], s43 offen lds\n"
    "s_add_u32 m0, s39, 3072\n"
    "s_mov_b32 s43, 128\n"
    "buffer_load_dwordx4 v157, s[8:11], s43 offen lds\n"
    "s_add_u32 m0, s39, 4096\n"
    "s_mov_b32 s43, 256\n"
    "buffer_load_dwordx4 v156, s[8:11], s43 offen lds\n"
    "s_add_u32 m0, s39, 5120\n"
    "s_mov_b32 s43, 256\n"
    "buffer_load_dwordx4 v157, s[8:11], s43 offen lds\n"
    "s_add_u32 m0, s39, 6144\n"
    "s_mov_b32 s43, 384\n"
    "buffer_load_dwordx4 v156, s[8:11], s43 offen lds\n"
    "s_add_u32 m0, s39, 7168\n"
    "s_mov_b32 s43, 384\n"
    "buffer_load_dwordx4 v157, s[8:11], s43 offen lds\n"
    "s_add_u32 m0, s39, 8192\n"
    "s_mov_b32 s43, 512\n"
    "buffer_load_dwordx4 v156, s[8:11], s43 offen lds\n"
    "s_add_u32 m0, s39, 9216\n"
    "s_mov_b32 s43, 512\n"
    "buffer_load_dwordx4 v157, s[8:11], s43 offen lds\n"
    "s_add_u32 m0, s39, 10240\n"
    "s_mov_b32 s43, 640\n"
    "buffer_load_dwordx4 v156, s[8:11], s43 offen lds\n"
    "s_add_u32 m0, s39, 11264\n"
    "s_mov_b32 s43, 640\n"
    "buffer_load_dwordx4 v157, s[8:11], s43 offen lds\n"
    "s_add_u32 m0, s47, 0\n"
    "s_mov_b32 s43, 768\n"
    "buffer_load_dwordx4 v156, s[8:11], s43 offen lds\n"
    "s_add_u32 m0, s47, 1024\n"
    "s_mov_b32 s43, 768\n"
    "buffer_load_dwordx4 v157, s[8:11], s43 offen lds\n"
    "s_add_u32 m0, s47, 2048\n"
    "s_mov_b32 s43, 896\n"
    "buffer_load_dwordx4 v156, s[8:11], s43 offen lds\n"
    "s_add_u32 m0, s47, 3072\n"
    "s_mov_b32 s43, 896\n"
    "buffer_load_dwordx4 v157, s[8:11], s43 offen lds\n"
    "s_add_u32 m0, s47, 4096\n"
    "s_mov_b32 s43, 1024\n"
    "buffer_load_dwordx4 v156, s[8:11], s43 offen lds\n"
    "s_add_u32 m0, s47, 5120\n"
    "s_mov_b32 s43, 1024\n"
    "buffer_load_dwordx4 v157, s[8:11], s43 offen lds\n"
    "v_lshrrev_b32 v240, 1, v248\n"
    "v_xor_b32 v241, v240, v249\n"
    "v_lshlrev_b32 v242, 7, v248\n"
    "v_lshl_add_u32 v242, v241, 4, v242\n"
    "v_add_u32 v253, s39, v242\n"
    "v_xor_b32 v254, 64, v253\n"
    "v_add_u32 v216, s47, v242\n"
    "v_xor_b32 v217, 64, v216\n"
    "s_lshl_b32 s38, s44, 3\n"
    "v_add_u32 v242, s38, v246\n"
    "v_and_b32 v243, 1, v249\n"
    "v_lshlrev_b32 v244, 1, v243\n"
    "v_lshrrev_b32 v245, 1, v247\n"
    "v_xor_b32 v245, v245, v244\n"
    "v_lshlrev_b32 v245, 1, v245\n"
    "v_and_b32 v240, 1, v247\n"
    "v_or_b32 v245, v245, v240\n"
    "v_mul_u32_u24 v5, 9216, v242\n"
    "v_lshl_add_u32 v5, v245, 4, v5\n"
    "s_and_b32 s38, s44, 2\n"
    "v_or_b32 v244, s38, v243\n"
    "v_lshrrev_b32 v245, 1, v247\n"
    "v_xor_b32 v245, v245, v244\n"
    "v_lshlrev_b32 v245, 1, v245\n"
    "v_or_b32 v245, v245, v240\n"
    "v_mul_u32_u24 v6, 1152, v242\n"
    "v_lshl_add_u32 v6, v245, 4, v6\n"
    "s_add_u32 m0, s42, 0\n"
    "s_mov_b32 s43, 4096\n"
    "buffer_load_dwordx4 v5, s[12:15], s43 offen offset:0 lds\n"
    "s_mov_b32 s43, 592896\n"
    "buffer_load_dwordx4 v5, s[12:15], s43 offen offset:1024 lds\n"
    "s_lshl_b32 s38, s46, 6\n"
    "v_lshrrev_b32 v240, 5, v14\n"
    "v_lshl_add_u32 v11, v240, 3, s38\n"
    "buffer_load_dwordx2 v[224:225], v11, s[20:23], 0 offen offset:0\n"
    "buffer_load_dwordx2 v[226:227], v11, s[20:23], 0 offen offset:16\n"
    "buffer_load_dwordx2 v[228:229], v11, s[20:23], 0 offen offset:32\n"
    "buffer_load_dwordx2 v[230:231], v11, s[20:23], 0 offen offset:48\n"
    "v_bfe_u32 v250, v14, 2, 2\n"
    "v_and_b32 v251, 3, v14\n"
    "s_lshl_b32 s38, s46, 1\n"
    "v_and_b32 v240, 1, v249\n"
    "v_or_b32 v240, s38, v240\n"
    "v_and_b32 v241, 2, v250\n"
    "v_xor_b32 v240, v240, v241\n"
    "v_lshrrev_b32 v241, 1, v249\n"
    "v_lshlrev_b32 v241, 13, v241\n"
    "v_lshl_add_u32 v241, v250, 7, v241\n"
    "v_lshl_add_u32 v241, v251, 3, v241\n"
    "v_lshl_add_u32 v1, v240, 5, v241\n"
    "v_lshrrev_b32 v240, 1, v250\n"
    "v_and_b32 v241, 1, v249\n"
    "v_lshl_or_b32 v240, v241, 1, v240\n"
    "v_xor_b32 v240, s45, v240\n"
    "v_lshrrev_b32 v242, 1, v249\n"
    "v_lshl_add_u32 v242, v242, 2, v250\n"
    "v_lshlrev_b32 v242, 7, v242\n"
    "v_lshl_add_u32 v242, v241, 14, v242\n"
    "v_lshl_add_u32 v242, v251, 3, v242\n"
    "v_lshl_add_u32 v3, v240, 5, v242\n"
    "s_mul_i32 s43, s46, 49152\n"
    "s_add_u32 s43, s43, 65536\n"
    "v_lshl_add_u32 v9, v14, 4, s43\n"
    "v_and_b32 v241, 1, v249\n"
    "v_lshlrev_b32 v241, 12, v241\n"
    "v_add3_u32 v10, v3, v241, s55\n"
    "s_mul_i32 s38, s45, 24576\n"
    "s_add_u32 s38, s38, 65536\n"
    "s_lshl_b32 s43, s46, 11\n"
    "s_add_u32 s38, s38, s43\n"
    "v_and_b32 v240, 1, v249\n"
    "v_lshlrev_b32 v240, 10, v240\n"
    "v_lshrrev_b32 v241, 1, v249\n"
    "v_lshl_add_u32 v240, v241, 9, v240\n"
    "v_lshl_add_u32 v240, v248, 4, v240\n"
    "v_add_u32 v7, s38, v240\n"
    "s_mul_i32 s38, s46, 73728\n"
    "s_lshl_b32 s43, s45, 10\n"
    "s_add_u32 s38, s38, s43\n"
    "v_lshl_add_u32 v12, v14, 4, s38\n"
    "v_mov_b32 v13, 0xbdd2d3e8\n"
    "v_lshl_add_u32 v244, v14, 4, s39\n"
    "v_lshl_add_u32 v245, v14, 4, s47\n"
    "v_and_b32 v240, 1, v249\n"
    "v_lshlrev_b32 v240, 2, v240\n"
    "v_add_u32 v240, s45, v240\n"
    "v_lshrrev_b32 v241, 1, v249\n"
    "v_lshlrev_b32 v241, 8, v241\n"
    "v_lshl_add_u32 v241, v248, 4, v241\n"
    "v_mul_u32_u24 v218, 12288, v240\n"
    "v_add3_u32 v218, v218, v241, s54\n"
    "v_lshl_add_u32 v219, v240, 13, v241\n"
    "v_add_u32 v219, 2048, v219\n"
    "v_mov_b32 v232, 0\n"
    "v_mov_b32 v233, 0\n"
    "v_mov_b32 v234, 0\n"
    "v_mov_b32 v235, 0\n"
    "v_mov_b32 v236, 0\n"
    "v_mov_b32 v237, 0\n"
    "v_mov_b32 v238, 0\n"
    "v_mov_b32 v239, 0\n"
    "s_waitcnt vmcnt(22)\n"
    "ds_read_b128 v[88:91], v253 offset:0\n"
    "ds_read_b128 v[92:95], v254 offset:0\n"
    "s_waitcnt vmcnt(20)\n"
    "ds_read_b128 v[96:99], v253 offset:2048\n"
    "ds_read_b128 v[100:103], v254 offset:2048\n"
    "s_waitcnt lgkmcnt(3)\n"
    "v_dot2c_f32_bf16 v232, s41, v88\n"
    "v_dot2c_f32_bf16 v236, v88, v88\n"
    "v_dot2c_f32_bf16 v233, s41, v89\n"
    "v_dot2c_f32_bf16 v237, v89, v89\n"
    "v_dot2c_f32_bf16 v234, s41, v90\n"
    "v_dot2c_f32_bf16 v238, v90, v90\n"
    "v_dot2c_f32_bf16 v235, s41, v91\n"
    "v_dot2c_f32_bf16 v239, v91, v91\n"
    "s_waitcnt lgkmcnt(2)\n"
    "v_dot2c_f32_bf16 v232, s41, v92\n"
    "v_dot2c_f32_bf16 v236, v92, v92\n"
    "v_dot2c_f32_bf16 v233, s41, v93\n"
    "v_dot2c_f32_bf16 v237, v93, v93\n"
    "v_dot2c_f32_bf16 v234, s41, v94\n"
    "v_dot2c_f32_bf16 v238, v94, v94\n"
    "v_dot2c_f32_bf16 v235, s41, v95\n"
    "v_dot2c_f32_bf16 v239, v95, v95\n"
    "s_waitcnt vmcnt(18)\n"
    "ds_read_b128 v[104:107], v253 offset:4096\n"
    "ds_read_b128 v[108:111], v254 offset:4096\n"
    "s_waitcnt lgkmcnt(3)\n"
    "v_dot2c_f32_bf16 v232, s41, v96\n"
    "v_dot2c_f32_bf16 v236, v96, v96\n"
    "v_dot2c_f32_bf16 v233, s41, v97\n"
    "v_dot2c_f32_bf16 v237, v97, v97\n"
    "v_dot2c_f32_bf16 v234, s41, v98\n"
    "v_dot2c_f32_bf16 v238, v98, v98\n"
    "v_dot2c_f32_bf16 v235, s41, v99\n"
    "v_dot2c_f32_bf16 v239, v99, v99\n"
    "s_waitcnt lgkmcnt(2)\n"
    "v_dot2c_f32_bf16 v232, s41, v100\n"
    "v_dot2c_f32_bf16 v236, v100, v100\n"
    "v_dot2c_f32_bf16 v233, s41, v101\n"
    "v_dot2c_f32_bf16 v237, v101, v101\n"
    "v_dot2c_f32_bf16 v234, s41, v102\n"
    "v_dot2c_f32_bf16 v238, v102, v102\n"
    "v_dot2c_f32_bf16 v235, s41, v103\n"
    "v_dot2c_f32_bf16 v239, v103, v103\n"
    "s_waitcnt vmcnt(16)\n"
    "ds_read_b128 v[112:115], v253 offset:6144\n"
    "ds_read_b128 v[116:119], v254 offset:6144\n"
    "s_waitcnt lgkmcnt(3)\n"
    "v_dot2c_f32_bf16 v232, s41, v104\n"
    "v_dot2c_f32_bf16 v236, v104, v104\n"
    "v_dot2c_f32_bf16 v233, s41, v105\n"
    "v_dot2c_f32_bf16 v237, v105, v105\n"
    "v_dot2c_f32_bf16 v234, s41, v106\n"
    "v_dot2c_f32_bf16 v238, v106, v106\n"
    "v_dot2c_f32_bf16 v235, s41, v107\n"
    "v_dot2c_f32_bf16 v239, v107, v107\n"
    "s_waitcnt lgkmcnt(2)\n"
    "v_dot2c_f32_bf16 v232, s41, v108\n"
    "v_dot2c_f32_bf16 v236, v108, v108\n"
    "v_dot2c_f32_bf16 v233, s41, v109\n"
    "v_dot2c_f32_bf16 v237, v109, v109\n"
    "v_dot2c_f32_bf16 v234, s41, v110\n"
    "v_dot2c_f32_bf16 v238, v110, v110\n"
    "v_dot2c_f32_bf16 v235, s41, v111\n"
    "v_dot2c_f32_bf16 v239, v111, v111\n"
    "s_waitcnt vmcnt(14)\n"
    "ds_read_b128 v[120:123], v253 offset:8192\n"
    "ds_read_b128 v[124:127], v254 offset:8192\n"
    "s_waitcnt lgkmcnt(3)\n"
    "v_dot2c_f32_bf16 v232, s41, v112\n"
    "v_dot2c_f32_bf16 v236, v112, v112\n"
    "v_dot2c_f32_bf16 v233, s41, v113\n"
    "v_dot2c_f32_bf16 v237, v113, v113\n"
    "v_dot2c_f32_bf16 v234, s41, v114\n"
    "v_dot2c_f32_bf16 v238, v114, v114\n"
    "v_dot2c_f32_bf16 v235, s41, v115\n"
    "v_dot2c_f32_bf16 v239, v115, v115\n"
    "s_waitcnt lgkmcnt(2)\n"
    "v_dot2c_f32_bf16 v232, s41, v116\n"
    "v_dot2c_f32_bf16 v236, v116, v116\n"
    "v_dot2c_f32_bf16 v233, s41, v117\n"
    "v_dot2c_f32_bf16 v237, v117, v117\n"
    "v_dot2c_f32_bf16 v234, s41, v118\n"
    "v_dot2c_f32_bf16 v238, v118, v118\n"
    "v_dot2c_f32_bf16 v235, s41, v119\n"
    "v_dot2c_f32_bf16 v239, v119, v119\n"
    "s_waitcnt vmcnt(12)\n"
    "ds_read_b128 v[128:131], v253 offset:10240\n"
    "ds_read_b128 v[132:135], v254 offset:10240\n"
    "s_waitcnt lgkmcnt(3)\n"
    "v_dot2c_f32_bf16 v232, s41, v120\n"
    "v_dot2c_f32_bf16 v236, v120, v120\n"
    "v_dot2c_f32_bf16 v233, s41, v121\n"
    "v_dot2c_f32_bf16 v237, v121, v121\n"
    "v_dot2c_f32_bf16 v234, s41, v122\n"
    "v_dot2c_f32_bf16 v238, v122, v122\n"
    "v_dot2c_f32_bf16 v235, s41, v123\n"
    "v_dot2c_f32_bf16 v239, v123, v123\n"
    "s_waitcnt lgkmcnt(2)\n"
    "v_dot2c_f32_bf16 v232, s41, v124\n"
    "v_dot2c_f32_bf16 v236, v124, v124\n"
    "v_dot2c_f32_bf16 v233, s41, v125\n"
    "v_dot2c_f32_bf16 v237, v125, v125\n"
    "v_dot2c_f32_bf16 v234, s41, v126\n"
    "v_dot2c_f32_bf16 v238, v126, v126\n"
    "v_dot2c_f32_bf16 v235, s41, v127\n"
    "v_dot2c_f32_bf16 v239, v127, v127\n"
    "s_waitcnt vmcnt(10)\n"
    "ds_read_b128 v[136:139], v216 offset:0\n"
    "ds_read_b128 v[140:143], v217 offset:0\n"
    "s_waitcnt lgkmcnt(3)\n"
    "v_dot2c_f32_bf16 v232, s41, v128\n"
    "v_dot2c_f32_bf16 v236, v128, v128\n"
    "v_dot2c_f32_bf16 v233, s41, v129\n"
    "v_dot2c_f32_bf16 v237, v129, v129\n"
    "v_dot2c_f32_bf16 v234, s41, v130\n"
    "v_dot2c_f32_bf16 v238, v130, v130\n"
    "v_dot2c_f32_bf16 v235, s41, v131\n"
    "v_dot2c_f32_bf16 v239, v131, v131\n"
    "s_waitcnt lgkmcnt(2)\n"
    "v_dot2c_f32_bf16 v232, s41, v132\n"
    "v_dot2c_f32_bf16 v236, v132, v132\n"
    "v_dot2c_f32_bf16 v233, s41, v133\n"
    "v_dot2c_f32_bf16 v237, v133, v133\n"
    "v_dot2c_f32_bf16 v234, s41, v134\n"
    "v_dot2c_f32_bf16 v238, v134, v134\n"
    "v_dot2c_f32_bf16 v235, s41, v135\n"
    "v_dot2c_f32_bf16 v239, v135, v135\n"
    "s_waitcnt vmcnt(8)\n"
    "ds_read_b128 v[144:147], v216 offset:2048\n"
    "ds_read_b128 v[148:151], v217 offset:2048\n"
    "s_waitcnt lgkmcnt(3)\n"
    "v_dot2c_f32_bf16 v232, s41, v136\n"
    "v_dot2c_f32_bf16 v236, v136, v136\n"
    "v_dot2c_f32_bf16 v233, s41, v137\n"
    "v_dot2c_f32_bf16 v237, v137, v137\n"
    "v_dot2c_f32_bf16 v234, s41, v138\n"
    "v_dot2c_f32_bf16 v238, v138, v138\n"
    "v_dot2c_f32_bf16 v235, s41, v139\n"
    "v_dot2c_f32_bf16 v239, v139, v139\n"
    "s_waitcnt lgkmcnt(2)\n"
    "v_dot2c_f32_bf16 v232, s41, v140\n"
    "v_dot2c_f32_bf16 v236, v140, v140\n"
    "v_dot2c_f32_bf16 v233, s41, v141\n"
    "v_dot2c_f32_bf16 v237, v141, v141\n"
    "v_dot2c_f32_bf16 v234, s41, v142\n"
    "v_dot2c_f32_bf16 v238, v142, v142\n"
    "v_dot2c_f32_bf16 v235, s41, v143\n"
    "v_dot2c_f32_bf16 v239, v143, v143\n"
    "s_waitcnt vmcnt(6)\n"
    "ds_read_b128 v[152:155], v216 offset:4096\n"
    "ds_read_b128 v[156:159], v217 offset:4096\n"
    "s_waitcnt lgkmcnt(3)\n"
    "v_dot2c_f32_bf16 v232, s41, v144\n"
    "v_dot2c_f32_bf16 v236, v144, v144\n"
    "v_dot2c_f32_bf16 v233, s41, v145\n"
    "v_dot2c_f32_bf16 v237, v145, v145\n"
    "v_dot2c_f32_bf16 v234, s41, v146\n"
    "v_dot2c_f32_bf16 v238, v146, v146\n"
    "v_dot2c_f32_bf16 v235, s41, v147\n"
    "v_dot2c_f32_bf16 v239, v147, v147\n"
    "s_waitcnt lgkmcnt(2)\n"
    "v_dot2c_f32_bf16 v232, s41, v148\n"
    "v_dot2c_f32_bf16 v236, v148, v148\n"
    "v_dot2c_f32_bf16 v233, s41, v149\n"
    "v_dot2c_f32_bf16 v237, v149, v149\n"
    "v_dot2c_f32_bf16 v234, s41, v150\n"
    "v_dot2c_f32_bf16 v238, v150, v150\n"
    "v_dot2c_f32_bf16 v235, s41, v151\n"
    "v_dot2c_f32_bf16 v239, v151, v151\n"
    "s_waitcnt lgkmcnt(1)\n"
    "v_dot2c_f32_bf16 v232, s41, v152\n"
    "v_dot2c_f32_bf16 v236, v152, v152\n"
    "v_dot2c_f32_bf16 v233, s41, v153\n"
    "v_dot2c_f32_bf16 v237, v153, v153\n"
    "v_dot2c_f32_bf16 v234, s41, v154\n"
    "v_dot2c_f32_bf16 v238, v154, v154\n"
    "v_dot2c_f32_bf16 v235, s41, v155\n"
    "v_dot2c_f32_bf16 v239, v155, v155\n"
    "s_waitcnt lgkmcnt(0)\n"
    "v_dot2c_f32_bf16 v232, s41, v156\n"
    "v_dot2c_f32_bf16 v236, v156, v156\n"
    "v_dot2c_f32_bf16 v233, s41, v157\n"
    "v_dot2c_f32_bf16 v237, v157, v157\n"
    "v_dot2c_f32_bf16 v234, s41, v158\n"
    "v_dot2c_f32_bf16 v238, v158, v158\n"
    "v_dot2c_f32_bf16 v235, s41, v159\n"
    "v_dot2c_f32_bf16 v239, v159, v159\n"
    "v_add_f32 v232, v232, v233\n"
    "s_nop 0\n"
    "v_add_f32 v234, v234, v235\n"
    "v_add_f32 v236, v236, v237\n"
    "v_add_f32 v238, v238, v239\n"
    "v_add_f32 v232, v232, v234\n"
    "v_add_f32 v236, v236, v238\n"
    "v_mov_b32 v233, v232\n"
    "v_mov_b32 v237, v236\n"
    "s_nop 0\n"
    "v_permlane32_swap_b32 v232, v233\n"
    "v_permlane32_swap_b32 v236, v237\n"
    "v_add_f32 v232, v232, v233\n"
    "v_add_f32 v236, v236, v237\n"
    "v_mov_b32 v233, v232\n"
    "v_mov_b32 v237, v236\n"
    "s_nop 0\n"
    "v_permlane16_swap_b32 v232, v233\n"
    "v_permlane16_swap_b32 v236, v237\n"
    "v_add_f32 v232, v232, v233\n"
    "v_add_f32 v236, v236, v237\n"
    "v_mul_f32 v232, 0x3ae38e39, v232\n"
    "v_mul_f32 v236, 0x3ae38e39, v236\n"
    "v_mul_f32 v234, v232, v232\n"
    "v_sub_f32 v236, v236, v234\n"
    "v_max_f32 v236, 0, v236\n"
    "v_cvt_pk_bf16_f32 v234, v232, v236\n"
    "v_lshlrev_b32 v235, 16, v234\n"
    "v_and_b32 v236, s40, v234\n"
    "v_add_f32 v236, 0x3727c5ac, v236\n"
    "v_cvt_pk_bf16_f32 v234, v236, v236\n"
    "v_and_b32 v236, s40, v234\n"
    "v_sqrt_f32 v236, v236\n"
    "s_nop 0\n"
    "v_cvt_pk_bf16_f32 v234, v236, v236\n"
    "v_and_b32 v236, s40, v234\n"
    "v_rcp_f32 v238, v236\n"
    "s_nop 0\n"
    "v_mul_f32 v239, v235, v238\n"
    "v_sub_f32 v239, 0, v239\n"
    "v_mov_b32 v240, v235\n"
    "v_mov_b32 v241, v238\n"
    "v_lshlrev_b32 v242, 3, v252\n"
    "buffer_store_dwordx2 v[240:241], v242, s[28:31], 0 offen sc1\n"
    "v_lshlrev_b32 v192, 16, v88\n"
    "v_and_b32 v193, s40, v88\n"
    "v_lshlrev_b32 v194, 16, v89\n"
    "v_and_b32 v195, s40, v89\n"
    "v_lshlrev_b32 v196, 16, v90\n"
    "v_and_b32 v197, s40, v90\n"
    "v_lshlrev_b32 v198, 16, v91\n"
    "v_and_b32 v199, s40, v91\n"
    "v_pk_fma_f32 v[192:193], v[192:193], v[238:239], v[238:239] op_sel:[0,0,1] op_sel_hi:[1,0,1]\n"
    "v_pk_fma_f32 v[194:195], v[194:195], v[238:239], v[238:239] op_sel:[0,0,1] op_sel_hi:[1,0,1]\n"
    "v_pk_fma_f32 v[196:197], v[196:197], v[238:239], v[238:239] op_sel:[0,0,1] op_sel_hi:[1,0,1]\n"
    "v_pk_fma_f32 v[198:199], v[198:199], v[238:239], v[238:239] op_sel:[0,0,1] op_sel_hi:[1,0,1]\n"
    "v_cvt_pk_bf16_f32 v88, v192, v193\n"
    "v_cvt_pk_bf16_f32 v89, v194, v195\n"
    "v_cvt_pk_bf16_f32 v90, v196, v197\n"
    "v_cvt_pk_bf16_f32 v91, v198, v199\n"
    "ds_write_b128 v244, v[88:91] offset:0\n"
    "v_lshlrev_b32 v200, 16, v92\n"
    "v_and_b32 v201, s40, v92\n"
    "v_lshlrev_b32 v202, 16, v93\n"
    "v_and_b32 v203, s40, v93\n"
    "v_lshlrev_b32 v204, 16, v94\n"
    "v_and_b32 v205, s40, v94\n"
    "v_lshlrev_b32 v206, 16, v95\n"
    "v_and_b32 v207, s40, v95\n"
    "v_pk_fma_f32 v[200:201], v[200:201], v[238:239], v[238:239] op_sel:[0,0,1] op_sel_hi:[1,0,1]\n"
    "v_pk_fma_f32 v[202:203], v[202:203], v[238:239], v[238:239] op_sel:[0,0,1] op_sel_hi:[1,0,1]\n"
    "v_pk_fma_f32 v[204:205], v[204:205], v[238:239], v[238:239] op_sel:[0,0,1] op_sel_hi:[1,0,1]\n"
    "v_pk_fma_f32 v[206:207], v[206:207], v[238:239], v[238:239] op_sel:[0,0,1] op_sel_hi:[1,0,1]\n"
    "v_cvt_pk_bf16_f32 v92, v200, v201\n"
    "v_cvt_pk_bf16_f32 v93, v202, v203\n"
    "v_cvt_pk_bf16_f32 v94, v204, v205\n"
    "v_cvt_pk_bf16_f32 v95, v206, v207\n"
    "ds_write_b128 v244, v[92:95] offset:1024\n"
    "v_lshlrev_b32 v192, 16, v96\n"
    "v_and_b32 v193, s40, v96\n"
    "v_lshlrev_b32 v194, 16, v97\n"
    "v_and_b32 v195, s40, v97\n"
    "v_lshlrev_b32 v196, 16, v98\n"
    "v_and_b32 v197, s40, v98\n"
    "v_lshlrev_b32 v198, 16, v99\n"
    "v_and_b32 v199, s40, v99\n"
    "v_pk_fma_f32 v[192:193], v[192:193], v[238:239], v[238:239] op_sel:[0,0,1] op_sel_hi:[1,0,1]\n"
    "v_pk_fma_f32 v[194:195], v[194:195], v[238:239], v[238:239] op_sel:[0,0,1] op_sel_hi:[1,0,1]\n"
    "v_pk_fma_f32 v[196:197], v[196:197], v[238:239], v[238:239] op_sel:[0,0,1] op_sel_hi:[1,0,1]\n"
    "v_pk_fma_f32 v[198:199], v[198:199], v[238:239], v[238:239] op_sel:[0,0,1] op_sel_hi:[1,0,1]\n"
    "v_cvt_pk_bf16_f32 v96, v192, v193\n"
    "v_cvt_pk_bf16_f32 v97, v194, v195\n"
    "v_cvt_pk_bf16_f32 v98, v196, v197\n"
    "v_cvt_pk_bf16_f32 v99, v198, v199\n"
    "ds_write_b128 v244, v[96:99] offset:2048\n"
    "v_lshlrev_b32 v200, 16, v100\n"
    "v_and_b32 v201, s40, v100\n"
    "v_lshlrev_b32 v202, 16, v101\n"
    "v_and_b32 v203, s40, v101\n"
    "v_lshlrev_b32 v204, 16, v102\n"
    "v_and_b32 v205, s40, v102\n"
    "v_lshlrev_b32 v206, 16, v103\n"
    "v_and_b32 v207, s40, v103\n"
    "v_pk_fma_f32 v[200:201], v[200:201], v[238:239], v[238:239] op_sel:[0,0,1] op_sel_hi:[1,0,1]\n"
    "v_pk_fma_f32 v[202:203], v[202:203], v[238:239], v[238:239] op_sel:[0,0,1] op_sel_hi:[1,0,1]\n"
    "v_pk_fma_f32 v[204:205], v[204:205], v[238:239], v[238:239] op_sel:[0,0,1] op_sel_hi:[1,0,1]\n"
    "v_pk_fma_f32 v[206:207], v[206:207], v[238:239], v[238:239] op_sel:[0,0,1] op_sel_hi:[1,0,1]\n"
    "v_cvt_pk_bf16_f32 v100, v200, v201\n"
    "v_cvt_pk_bf16_f32 v101, v202, v203\n"
    "v_cvt_pk_bf16_f32 v102, v204, v205\n"
    "v_cvt_pk_bf16_f32 v103, v206, v207\n"
    "ds_write_b128 v244, v[100:103] offset:3072\n"
    "v_lshlrev_b32 v192, 16, v104\n"
    "v_and_b32 v193, s40, v104\n"
    "v_lshlrev_b32 v194, 16, v105\n"
    "v_and_b32 v195, s40, v105\n"
    "v_lshlrev_b32 v196, 16, v106\n"
    "v_and_b32 v197, s40, v106\n"
    "v_lshlrev_b32 v198, 16, v107\n"
    "v_and_b32 v199, s40, v107\n"
    "v_pk_fma_f32 v[192:193], v[192:193], v[238:239], v[238:239] op_sel:[0,0,1] op_sel_hi:[1,0,1]\n"
    "v_pk_fma_f32 v[194:195], v[194:195], v[238:239], v[238:239] op_sel:[0,0,1] op_sel_hi:[1,0,1]\n"
    "v_pk_fma_f32 v[196:197], v[196:197], v[238:239], v[238:239] op_sel:[0,0,1] op_sel_hi:[1,0,1]\n"
    "v_pk_fma_f32 v[198:199], v[198:199], v[238:239], v[238:239] op_sel:[0,0,1] op_sel_hi:[1,0,1]\n"
    "v_cvt_pk_bf16_f32 v104, v192, v193\n"
    "v_cvt_pk_bf16_f32 v105, v194, v195\n"
    "v_cvt_pk_bf16_f32 v106, v196, v197\n"
    "v_cvt_pk_bf16_f32 v107, v198, v199\n"
    "ds_write_b128 v244, v[104:107] offset:4096\n"
    "v_lshlrev_b32 v200, 16, v108\n"
    "v_and_b32 v201, s40, v108\n"
    "v_lshlrev_b32 v202, 16, v109\n"
    "v_and_b32 v203, s40, v109\n"
    "v_lshlrev_b32 v204, 16, v110\n"
    "v_and_b32 v205, s40, v110\n"
    "v_lshlrev_b32 v206, 16, v111\n"
    "v_and_b32 v207, s40, v111\n"
    "v_pk_fma_f32 v[200:201], v[200:201], v[238:239], v[238:239] op_sel:[0,0,1] op_sel_hi:[1,0,1]\n"
    "v_pk_fma_f32 v[202:203], v[202:203], v[238:239], v[238:239] op_sel:[0,0,1] op_sel_hi:[1,0,1]\n"
    "v_pk_fma_f32 v[204:205], v[204:205], v[238:239], v[238:239] op_sel:[0,0,1] op_sel_hi:[1,0,1]\n"
    "v_pk_fma_f32 v[206:207], v[206:207], v[238:239], v[238:239] op_sel:[0,0,1] op_sel_hi:[1,0,1]\n"
    "v_cvt_pk_bf16_f32 v108, v200, v201\n"
    "v_cvt_pk_bf16_f32 v109, v202, v203\n"
    "v_cvt_pk_bf16_f32 v110, v204, v205\n"
    "v_cvt_pk_bf16_f32 v111, v206, v207\n"
    "ds_write_b128 v244, v[108:111] offset:5120\n"
    "v_lshlrev_b32 v192, 16, v112\n"
    "v_and_b32 v193, s40, v112\n"
    "v_lshlrev_b32 v194, 16, v113\n"
    "v_and_b32 v195, s40, v113\n"
    "v_lshlrev_b32 v196, 16, v114\n"
    "v_and_b32 v197, s40, v114\n"
    "v_lshlrev_b32 v198, 16, v115\n"
    "v_and_b32 v199, s40, v115\n"
    "v_pk_fma_f32 v[192:193], v[192:193], v[238:239], v[238:239] op_sel:[0,0,1] op_sel_hi:[1,0,1]\n"
    "v_pk_fma_f32 v[194:195], v[194:195], v[238:239], v[238:239] op_sel:[0,0,1] op_sel_hi:[1,0,1]\n"
    "v_pk_fma_f32 v[196:197], v[196:197], v[238:239], v[238:239] op_sel:[0,0,1] op_sel_hi:[1,0,1]\n"
    "v_pk_fma_f32 v[198:199], v[198:199], v[238:239], v[238:239] op_sel:[0,0,1] op_sel_hi:[1,0,1]\n"
    "v_cvt_pk_bf16_f32 v112, v192, v193\n"
    "v_cvt_pk_bf16_f32 v113, v194, v195\n"
    "v_cvt_pk_bf16_f32 v114, v196, v197\n"
    "v_cvt_pk_bf16_f32 v115, v198, v199\n"
    "ds_write_b128 v244, v[112:115] offset:6144\n"
    "v_lshlrev_b32 v200, 16, v116\n"
    "v_and_b32 v201, s40, v116\n"
    "v_lshlrev_b32 v202, 16, v117\n"
    "v_and_b32 v203, s40, v117\n"
    "v_lshlrev_b32 v204, 16, v118\n"
    "v_and_b32 v205, s40, v118\n"
    "v_lshlrev_b32 v206, 16, v119\n"
    "v_and_b32 v207, s40, v119\n"
    "v_pk_fma_f32 v[200:201], v[200:201], v[238:239], v[238:239] op_sel:[0,0,1] op_sel_hi:[1,0,1]\n"
    "v_pk_fma_f32 v[202:203], v[202:203], v[238:239], v[238:239] op_sel:[0,0,1] op_sel_hi:[1,0,1]\n"
    "v_pk_fma_f32 v[204:205], v[204:205], v[238:239], v[238:239] op_sel:[0,0,1] op_sel_hi:[1,0,1]\n"
    "v_pk_fma_f32 v[206:207], v[206:207], v[238:239], v[238:239] op_sel:[0,0,1] op_sel_hi:[1,0,1]\n"
    "v_cvt_pk_bf16_f32 v116, v200, v201\n"
    "v_cvt_pk_bf16_f32 v117, v202, v203\n"
    "v_cvt_pk_bf16_f32 v118, v204, v205\n"
    "v_cvt_pk_bf16_f32 v119, v206, v207\n"
    "ds_write_b128 v244, v[116:119] offset:7168\n"
    "v_lshlrev_b32 v192, 16, v120\n"
    "v_and_b32 v193, s40, v120\n"
    "v_lshlrev_b32 v194, 16, v121\n"
    "v_and_b32 v195, s40, v121\n"
    "v_lshlrev_b32 v196, 16, v122\n"
    "v_and_b32 v197, s40, v122\n"
    "v_lshlrev_b32 v198, 16, v123\n"
    "v_and_b32 v199, s40, v123\n"
    "v_pk_fma_f32 v[192:193], v[192:193], v[238:239], v[238:239] op_sel:[0,0,1] op_sel_hi:[1,0,1]\n"
    "v_pk_fma_f32 v[194:195], v[194:195], v[238:239], v[238:239] op_sel:[0,0,1] op_sel_hi:[1,0,1]\n"
    "v_pk_fma_f32 v[196:197], v[196:197], v[238:239], v[238:239] op_sel:[0,0,1] op_sel_hi:[1,0,1]\n"
    "v_pk_fma_f32 v[198:199], v[198:199], v[238:239], v[238:239] op_sel:[0,0,1] op_sel_hi:[1,0,1]\n"
    "v_cvt_pk_bf16_f32 v120, v192, v193\n"
    "v_cvt_pk_bf16_f32 v121, v194, v195\n"
    "v_cvt_pk_bf16_f32 v122, v196, v197\n"
    "v_cvt_pk_bf16_f32 v123, v198, v199\n"
    "ds_write_b128 v244, v[120:123] offset:8192\n"
    "v_lshlrev_b32 v200, 16, v124\n"
    "v_and_b32 v201, s40, v124\n"
    "v_lshlrev_b32 v202, 16, v125\n"
    "v_and_b32 v203, s40, v125\n"
    "v_lshlrev_b32 v204, 16, v126\n"
    "v_and_b32 v205, s40, v126\n"
    "v_lshlrev_b32 v206, 16, v127\n"
    "v_and_b32 v207, s40, v127\n"
    "v_pk_fma_f32 v[200:201], v[200:201], v[238:239], v[238:239] op_sel:[0,0,1] op_sel_hi:[1,0,1]\n"
    "v_pk_fma_f32 v[202:203], v[202:203], v[238:239], v[238:239] op_sel:[0,0,1] op_sel_hi:[1,0,1]\n"
    "v_pk_fma_f32 v[204:205], v[204:205], v[238:239], v[238:239] op_sel:[0,0,1] op_sel_hi:[1,0,1]\n"
    "v_pk_fma_f32 v[206:207], v[206:207], v[238:239], v[238:239] op_sel:[0,0,1] op_sel_hi:[1,0,1]\n"
    "v_cvt_pk_bf16_f32 v124, v200, v201\n"
    "v_cvt_pk_bf16_f32 v125, v202, v203\n"
    "v_cvt_pk_bf16_f32 v126, v204, v205\n"
    "v_cvt_pk_bf16_f32 v127, v206, v207\n"
    "ds_write_b128 v244, v[124:127] offset:9216\n"
    "v_lshlrev_b32 v192, 16, v128\n"
    "v_and_b32 v193, s40, v128\n"
    "v_lshlrev_b32 v194, 16, v129\n"
    "v_and_b32 v195, s40, v129\n"
    "v_lshlrev_b32 v196, 16, v130\n"
    "v_and_b32 v197, s40, v130\n"
    "v_lshlrev_b32 v198, 16, v131\n"
    "v_and_b32 v199, s40, v131\n"
    "v_pk_fma_f32 v[192:193], v[192:193], v[238:239], v[238:239] op_sel:[0,0,1] op_sel_hi:[1,0,1]\n"
    "v_pk_fma_f32 v[194:195], v[194:195], v[238:239], v[238:239] op_sel:[0,0,1] op_sel_hi:[1,0,1]\n"
    "v_pk_fma_f32 v[196:197], v[196:197], v[238:239], v[238:239] op_sel:[0,0,1] op_sel_hi:[1,0,1]\n"
    "v_pk_fma_f32 v[198:199], v[198:199], v[238:239], v[238:239] op_sel:[0,0,1] op_sel_hi:[1,0,1]\n"
    "v_cvt_pk_bf16_f32 v128, v192, v193\n"
    "v_cvt_pk_bf16_f32 v129, v194, v195\n"
    "v_cvt_pk_bf16_f32 v130, v196, v197\n"
    "v_cvt_pk_bf16_f32 v131, v198, v199\n"
    "ds_write_b128 v244, v[128:131] offset:10240\n"
    "v_lshlrev_b32 v200, 16, v132\n"
    "v_and_b32 v201, s40, v132\n"
    "v_lshlrev_b32 v202, 16, v133\n"
    "v_and_b32 v203, s40, v133\n"
    "v_lshlrev_b32 v204, 16, v134\n"
    "v_and_b32 v205, s40, v134\n"
    "v_lshlrev_b32 v206, 16, v135\n"
    "v_and_b32 v207, s40, v135\n"
    "v_pk_fma_f32 v[200:201], v[200:201], v[238:239], v[238:239] op_sel:[0,0,1] op_sel_hi:[1,0,1]\n"
    "v_pk_fma_f32 v[202:203], v[202:203], v[238:239], v[238:239] op_sel:[0,0,1] op_sel_hi:[1,0,1]\n"
    "v_pk_fma_f32 v[204:205], v[204:205], v[238:239], v[238:239] op_sel:[0,0,1] op_sel_hi:[1,0,1]\n"
    "v_pk_fma_f32 v[206:207], v[206:207], v[238:239], v[238:239] op_sel:[0,0,1] op_sel_hi:[1,0,1]\n"
    "v_cvt_pk_bf16_f32 v132, v200, v201\n"
    "v_cvt_pk_bf16_f32 v133, v202, v203\n"
    "v_cvt_pk_bf16_f32 v134, v204, v205\n"
    "v_cvt_pk_bf16_f32 v135, v206, v207\n"
    "ds_write_b128 v244, v[132:135] offset:11264\n"
    "v_lshlrev_b32 v192, 16, v136\n"
    "v_and_b32 v193, s40, v136\n"
    "v_lshlrev_b32 v194, 16, v137\n"
    "v_and_b32 v195, s40, v137\n"
    "v_lshlrev_b32 v196, 16, v138\n"
    "v_and_b32 v197, s40, v138\n"
    "v_lshlrev_b32 v198, 16, v139\n"
    "v_and_b32 v199, s40, v139\n"
    "v_pk_fma_f32 v[192:193], v[192:193], v[238:239], v[238:239] op_sel:[0,0,1] op_sel_hi:[1,0,1]\n"
    "v_pk_fma_f32 v[194:195], v[194:195], v[238:239], v[238:239] op_sel:[0,0,1] op_sel_hi:[1,0,1]\n"
    "v_pk_fma_f32 v[196:197], v[196:197], v[238:239], v[238:239] op_sel:[0,0,1] op_sel_hi:[1,0,1]\n"
    "v_pk_fma_f32 v[198:199], v[198:199], v[238:239], v[238:239] op_sel:[0,0,1] op_sel_hi:[1,0,1]\n"
    "v_cvt_pk_bf16_f32 v136, v192, v193\n"
    "v_cvt_pk_bf16_f32 v137, v194, v195\n"
    "v_cvt_pk_bf16_f32 v138, v196, v197\n"
    "v_cvt_pk_bf16_f32 v139, v198, v199\n"
    "ds_write_b128 v245, v[136:139] offset:0\n"
    "v_lshlrev_b32 v200, 16, v140\n"
    "v_and_b32 v201, s40, v140\n"
    "v_lshlrev_b32 v202, 16, v141\n"
    "v_and_b32 v203, s40, v141\n"
    "v_lshlrev_b32 v204, 16, v142\n"
    "v_and_b32 v205, s40, v142\n"
    "v_lshlrev_b32 v206, 16, v143\n"
    "v_and_b32 v207, s40, v143\n"
    "v_pk_fma_f32 v[200:201], v[200:201], v[238:239], v[238:239] op_sel:[0,0,1] op_sel_hi:[1,0,1]\n"
    "v_pk_fma_f32 v[202:203], v[202:203], v[238:239], v[238:239] op_sel:[0,0,1] op_sel_hi:[1,0,1]\n"
    "v_pk_fma_f32 v[204:205], v[204:205], v[238:239], v[238:239] op_sel:[0,0,1] op_sel_hi:[1,0,1]\n"
    "v_pk_fma_f32 v[206:207], v[206:207], v[238:239], v[238:239] op_sel:[0,0,1] op_sel_hi:[1,0,1]\n"
    "v_cvt_pk_bf16_f32 v140, v200, v201\n"
    "v_cvt_pk_bf16_f32 v141, v202, v203\n"
    "v_cvt_pk_bf16_f32 v142, v204, v205\n"
    "v_cvt_pk_bf16_f32 v143, v206, v207\n"
    "ds_write_b128 v245, v[140:143] offset:1024\n"
    "v_lshlrev_b32 v192, 16, v144\n"
    "v_and_b32 v193, s40, v144\n"
    "v_lshlrev_b32 v194, 16, v145\n"
    "v_and_b32 v195, s40, v145\n"
    "v_lshlrev_b32 v196, 16, v146\n"
    "v_and_b32 v197, s40, v146\n"
    "v_lshlrev_b32 v198, 16, v147\n"
    "v_and_b32 v199, s40, v147\n"
    "v_pk_fma_f32 v[192:193], v[192:193], v[238:239], v[238:239] op_sel:[0,0,1] op_sel_hi:[1,0,1]\n"
    "v_pk_fma_f32 v[194:195], v[194:195], v[238:239], v[238:239] op_sel:[0,0,1] op_sel_hi:[1,0,1]\n"
    "v_pk_fma_f32 v[196:197], v[196:197], v[238:239], v[238:239] op_sel:[0,0,1] op_sel_hi:[1,0,1]\n"
    "v_pk_fma_f32 v[198:199], v[198:199], v[238:239], v[238:239] op_sel:[0,0,1] op_sel_hi:[1,0,1]\n"
    "v_cvt_pk_bf16_f32 v144, v192, v193\n"
    "v_cvt_pk_bf16_f32 v145, v194, v195\n"
    "v_cvt_pk_bf16_f32 v146, v196, v197\n"
    "v_cvt_pk_bf16_f32 v147, v198, v199\n"
    "ds_write_b128 v245, v[144:147] offset:2048\n"
    "v_lshlrev_b32 v200, 16, v148\n"
    "v_and_b32 v201, s40, v148\n"
    "v_lshlrev_b32 v202, 16, v149\n"
    "v_and_b32 v203, s40, v149\n"
    "v_lshlrev_b32 v204, 16, v150\n"
    "v_and_b32 v205, s40, v150\n"
    "v_lshlrev_b32 v206, 16, v151\n"
    "v_and_b32 v207, s40, v151\n"
    "v_pk_fma_f32 v[200:201], v[200:201], v[238:239], v[238:239] op_sel:[0,0,1] op_sel_hi:[1,0,1]\n"
    "v_pk_fma_f32 v[202:203], v[202:203], v[238:239], v[238:239] op_sel:[0,0,1] op_sel_hi:[1,0,1]\n"
    "v_pk_fma_f32 v[204:205], v[204:205], v[238:239], v[238:239] op_sel:[0,0,1] op_sel_hi:[1,0,1]\n"
    "v_pk_fma_f32 v[206:207], v[206:207], v[238:239], v[238:239] op_sel:[0,0,1] op_sel_hi:[1,0,1]\n"
    "v_cvt_pk_bf16_f32 v148, v200, v201\n"
    "v_cvt_pk_bf16_f32 v149, v202, v203\n"
    "v_cvt_pk_bf16_f32 v150, v204, v205\n"
    "v_cvt_pk_bf16_f32 v151, v206, v207\n"
    "ds_write_b128 v245, v[148:151] offset:3072\n"
    "v_lshlrev_b32 v192, 16, v152\n"
    "v_and_b32 v193, s40, v152\n"
    "v_lshlrev_b32 v194, 16, v153\n"
    "v_and_b32 v195, s40, v153\n"
    "v_lshlrev_b32 v196, 16, v154\n"
    "v_and_b32 v197, s40, v154\n"
    "v_lshlrev_b32 v198, 16, v155\n"
    "v_and_b32 v199, s40, v155\n"
    "v_pk_fma_f32 v[192:193], v[192:193], v[238:239], v[238:239] op_sel:[0,0,1] op_sel_hi:[1,0,1]\n"
    "v_pk_fma_f32 v[194:195], v[194:195], v[238:239], v[238:239] op_sel:[0,0,1] op_sel_hi:[1,0,1]\n"
    "v_pk_fma_f32 v[196:197], v[196:197], v[238:239], v[238:239] op_sel:[0,0,1] op_sel_hi:[1,0,1]\n"
    "v_pk_fma_f32 v[198:199], v[198:199], v[238:239], v[238:239] op_sel:[0,0,1] op_sel_hi:[1,0,1]\n"
    "v_cvt_pk_bf16_f32 v152, v192, v193\n"
    "v_cvt_pk_bf16_f32 v153, v194, v195\n"
    "v_cvt_pk_bf16_f32 v154, v196, v197\n"
    "v_cvt_pk_bf16_f32 v155, v198, v199\n"
    "ds_write_b128 v245, v[152:155] offset:4096\n"
    "v_lshlrev_b32 v200, 16, v156\n"
    "v_and_b32 v201, s40, v156\n"
    "v_lshlrev_b32 v202, 16, v157\n"
    "v_and_b32 v203, s40, v157\n"
    "v_lshlrev_b32 v204, 16, v158\n"
    "v_and_b32 v205, s40, v158\n"
    "v_lshlrev_b32 v206, 16, v159\n"
    "v_and_b32 v207, s40, v159\n"
    "v_pk_fma_f32 v[200:201], v[200:201], v[238:239], v[238:239] op_sel:[0,0,1] op_sel_hi:[1,0,1]\n"
    "v_pk_fma_f32 v[202:203], v[202:203], v[238:239], v[238:239] op_sel:[0,0,1] op_sel_hi:[1,0,1]\n"
    "v_pk_fma_f32 v[204:205], v[204:205], v[238:239], v[238:239] op_sel:[0,0,1] op_sel_hi:[1,0,1]\n"
    "v_pk_fma_f32 v[206:207], v[206:207], v[238:239], v[238:239] op_sel:[0,0,1] op_sel_hi:[1,0,1]\n"
    "v_cvt_pk_bf16_f32 v156, v200, v201\n"
    "v_cvt_pk_bf16_f32 v157, v202, v203\n"
    "v_cvt_pk_bf16_f32 v158, v204, v205\n"
    "v_cvt_pk_bf16_f32 v159, v206, v207\n"
    "ds_write_b128 v245, v[156:159] offset:5120\n"
    "v_add_u32 v252, 1, v8\n"
    "v_not_b32 v253, v252\n"
    "v_cmp_gt_u32 vcc, 16, v0\n"
    "v_mov_b32 v254, 0x7ffffff0\n"
    "v_lshlrev_b32 v255, 3, v0\n"
    "v_cndmask_b32 v254, v254, v255, vcc\n"
    "buffer_store_dwordx2 v[252:253], v254, s[48:51], s52 offen sc1\n"
    "s_waitcnt lgkmcnt(0)\n"
    "s_barrier\n"
    "ds_read_b128 v[16:19], v218 offset:0\n"
    "ds_read_b128 v[20:23], v218 offset:512\n"
    "ds_read_b128 v[24:27], v218 offset:1024\n"
    "ds_read_b128 v[28:31], v218 offset:1536\n"
    "ds_read_b128 v[32:35], v218 offset:2048\n"
    "ds_read_b128 v[36:39], v218 offset:2560\n"
    "ds_read_b128 v[40:43], v218 offset:3072\n"
    "ds_read_b128 v[44:47], v218 offset:3584\n"
    "ds_read_b128 v[48:51], v218 offset:4096\n"
    "ds_read_b128 v[52:55], v218 offset:4608\n"
    "ds_read_b128 v[56:59], v218 offset:5120\n"
    "ds_read_b128 v[60:63], v218 offset:5632\n"
    "ds_read_b128 v[64:67], v218 offset:6144\n"
    "ds_read_b128 v[68:71], v218 offset:6656\n"
    "ds_read_b128 v[72:75], v218 offset:7168\n"
    "ds_read_b128 v[76:79], v218 offset:7680\n"
    "ds_read_b128 v[80:83], v218 offset:8192\n"
    "ds_read_b128 v[84:87], v218 offset:8704\n"
    "ds_read_b128 v[88:91], v218 offset:9216\n"
    "ds_read_b128 v[92:95], v218 offset:9728\n"
    "ds_read_b128 v[96:99], v218 offset:10240\n"
    "ds_read_b128 v[100:103], v218 offset:10752\n"
    "ds_read_b128 v[104:107], v218 offset:11264\n"
    "ds_read_b128 v[108:111], v218 offset:11776\n"
    "ds_read_b128 v[112:115], v219 offset:0\n"
    "ds_read_b128 v[116:119], v219 offset:512\n"
    "ds_read_b128 v[120:123], v219 offset:1024\n"
    "ds_read_b128 v[124:127], v219 offset:1536\n"
    "ds_read_b128 v[128:131], v219 offset:2048\n"
    "ds_read_b128 v[132:135], v219 offset:2560\n"
    "ds_read_b128 v[136:139], v219 offset:3072\n"
    "ds_read_b128 v[140:143], v219 offset:3584\n"
    "ds_read_b128 v[144:147], v219 offset:4096\n"
    "ds_read_b128 v[148:151], v219 offset:4608\n"
    "ds_read_b128 v[152:155], v219 offset:5120\n"
    "ds_read_b128 v[156:159], v219 offset:5632\n"
    "s_waitcnt vmcnt(5)\n"
    "v_lshlrev_b32 v208, 16, v224\n"
    "v_and_b32 v209, s40, v224\n"
    "v_lshlrev_b32 v210, 16, v225\n"
    "v_and_b32 v211, s40, v225\n"
    "s_waitcnt vmcnt(4)\n"
    "v_lshlrev_b32 v212, 16, v226\n"
    "v_and_b32 v213, s40, v226\n"
    "v_lshlrev_b32 v214, 16, v227\n"
    "v_and_b32 v215, s40, v227\n"
    "s_waitcnt vmcnt(3)\n"
    "v_lshlrev_b32 v216, 16, v228\n"
    "v_and_b32 v217, s40, v228\n"
    "v_lshlrev_b32 v218, 16, v229\n"
    "v_and_b32 v219, s40, v229\n"
    "s_waitcnt vmcnt(2)\n"
    "v_lshlrev_b32 v220, 16, v230\n"
    "v_and_b32 v221, s40, v230\n"
    "v_lshlrev_b32 v222, 16, v231\n"
    "v_and_b32 v223, s40, v231\n"
    "s_waitcnt lgkmcnt(0)\n"
    "s_barrier\n"
    "s_mov_b32 s43, 1181696\n"
    "buffer_load_dwordx4 v5, s[12:15], s43 offen offset:2048 lds\n"
    "s_mov_b32 s43, 1770496\n"
    "buffer_load_dwordx4 v5, s[12:15], s43 offen offset:3072 lds\n"
    "s_add_u32 m0, s42, 4096\n"
    "s_mov_b32 s43, 2363392\n"
    "buffer_load_dwordx4 v5, s[12:15], s43 offen offset:0 lds\n"
    "s_mov_b32 s43, 2952192\n"
    "buffer_load_dwordx4 v5, s[12:15], s43 offen offset:1024 lds\n"
    "s_mul_i32 s47, s44, 10240\n"
    "s_add_u32 s47, s47, 73728\n"
    "v_lshrrev_b32 v248, 6, v0\n"
    "v_lshlrev_b32 v248, 12, v248\n"
    "v_and_b32 v249, 63, v0\n"
    "v_lshl_add_u32 v248, v249, 2, v248\n"
    "v_add_u32 v248, 65536, v248\n"
    "v_cmp_gt_u32 vcc, 384, v0\n"
    "v_mov_b32 v250, 65536\n"
    "s_nop 0\n"
    "v_cndmask_b32 v248, v250, v248, vcc\n"
    "s_lshl_b32 s38, s46, 7\n"
    "s_add_u32 s38, s38, 65536\n"
    "v_lshrrev_b32 v2, 5, v14\n"
    "v_lshl_add_u32 v2, v2, 4, s38\n"
    "ds_read_b64_tr_b16 v[192:193], v1 offset:0\n"
    "ds_read_b64_tr_b16 v[194:195], v1 offset:512\n"
    "ds_read_b64_tr_b16 v[196:197], v1 offset:16384\n"
    "ds_read_b64_tr_b16 v[198:199], v1 offset:16896\n"
    "ds_read_b64_tr_b16 v[200:201], v1 offset:32768\n"
    "ds_read_b64_tr_b16 v[202:203], v1 offset:33280\n"
    "s_waitcnt lgkmcnt(4)\n"
    "v_mfma_f32_32x32x16_bf16 v[160:175], v[192:195], v[16:19], v[208:223]\n"
    "ds_read_b64_tr_b16 v[204:205], v1 offset:49152\n"
    "ds_read_b64_tr_b16 v[206:207], v1 offset:49664\n"
    "v_lshlrev_b32 v15, 16, v15\n"
    "ds_write_b32 v248, v15\n"
    "s_waitcnt lgkmcnt(5)\n"
    "v_mfma_f32_32x32x16_bf16 v[160:175], v[196:199], v[20:23], v[160:175]\n"
    "ds_read_b64_tr_b16 v[192:193], v1 offset:1024\n"
    "ds_read_b64_tr_b16 v[194:195], v1 offset:1536\n"
    "s_waitcnt lgkmcnt(5)\n"
    "v_mfma_f32_32x32x16_bf16 v[160:175], v[200:203], v[24:27], v[160:175]\n"
    "ds_read_b64_tr_b16 v[196:197], v1 offset:17408\n"
    "ds_read_b64_tr_b16 v[198:199], v1 offset:17920\n"
    "s_waitcnt lgkmcnt(5)\n"
    "v_mfma_f32_32x32x16_bf16 v[160:175], v[204:207], v[28:31], v[160:175]\n"
    "ds_read_b64_tr_b16 v[200:201], v1 offset:33792\n"
    "ds_read_b64_tr_b16 v[202:203], v1 offset:34304\n"
    "s_waitcnt lgkmcnt(4)\n"
    "v_mfma_f32_32x32x16_bf16 v[160:175], v[192:195], v[32:35], v[160:175]\n"
    "ds_read_b64_tr_b16 v[204:205], v1 offset:50176\n"
    "ds_read_b64_tr_b16 v[206:207], v1 offset:50688\n"
    "s_waitcnt lgkmcnt(4)\n"
    "v_mfma_f32_32x32x16_bf16 v[160:175], v[196:199], v[36:39], v[160:175]\n"
    "s_waitcnt vmcnt(2) lgkmcnt(0)\n"
    "s_barrier\n"
    "v_mfma_f32_32x32x16_bf16 v[160:175], v[200:203], v[40:43], v[160:175]\n"
    "ds_read_b64_tr_b16 v[192:193], v1 offset:2048\n"
    "ds_read_b64_tr_b16 v[194:195], v1 offset:2560\n"
    "ds_read_b64_tr_b16 v[196:197], v1 offset:18432\n"
    "ds_read_b64_tr_b16 v[198:199], v1 offset:18944\n"
    "s_mov_b32 s43, 3540992\n"
    "buffer_load_dwordx4 v5, s[12:15], s43 offen offset:2048 lds\n"
    "s_mov_b32 s43, 4129792\n"
    "buffer_load_dwordx4 v5, s[12:15], s43 offen offset:3072 lds\n"
    "v_mfma_f32_32x32x16_bf16 v[160:175], v[204:207], v[44:47], v[160:175]\n"
    "ds_read_b64_tr_b16 v[200:201], v1 offset:34816\n"
    "ds_read_b64_tr_b16 v[202:203], v1 offset:35328\n"
    "s_waitcnt lgkmcnt(4)\n"
    "v_mfma_f32_32x32x16_bf16 v[160:175], v[192:195], v[48:51], v[160:175]\n"
    "ds_read_b64_tr_b16 v[204:205], v1 offset:51200\n"
    "ds_read_b64_tr_b16 v[206:207], v1 offset:51712\n"
    "s_waitcnt lgkmcnt(4)\n"
    "v_mfma_f32_32x32x16_bf16 v[160:175], v[196:199], v[52:55], v[160:175]\n"
    "ds_read_b64_tr_b16 v[192:193], v1 offset:3072\n"
    "ds_read_b64_tr_b16 v[194:195], v1 offset:3584\n"
    "s_waitcnt lgkmcnt(4)\n"
    "v_mfma_f32_32x32x16_bf16 v[160:175], v[200:203], v[56:59], v[160:175]\n"
    "ds_read_b64_tr_b16 v[196:197], v1 offset:19456\n"
    "ds_read_b64_tr_b16 v[198:199], v1 offset:19968\n"
    "s_waitcnt lgkmcnt(4)\n"
    "v_mfma_f32_32x32x16_bf16 v[160:175], v[204:207], v[60:63], v[160:175]\n"
    "ds_read_b64_tr_b16 v[200:201], v1 offset:35840\n"
    "ds_read_b64_tr_b16 v[202:203], v1 offset:36352\n"
    "s_waitcnt lgkmcnt(4)\n"
    "v_mfma_f32_32x32x16_bf16 v[160:175], v[192:195], v[64:67], v[160:175]\n"
    "ds_read_b64_tr_b16 v[204:205], v1 offset:52224\n"
    "ds_read_b64_tr_b16 v[206:207], v1 offset:52736\n"
    "s_waitcnt lgkmcnt(4)\n"
    "v_mfma_f32_32x32x16_bf16 v[160:175], v[196:199], v[68:71], v[160:175]\n"
    "s_waitcnt vmcnt(2)\n"
    "s_barrier\n"
    "s_waitcnt lgkmcnt(2)\n"
    "v_mfma_f32_32x32x16_bf16 v[160:175], v[200:203], v[72:75], v[160:175]\n"
    "ds_read_b64_tr_b16 v[192:193], v1 offset:4096\n"
    "ds_read_b64_tr_b16 v[194:195], v1 offset:4608\n"
    "ds_read_b64_tr_b16 v[196:197], v1 offset:20480\n"
    "ds_read_b64_tr_b16 v[198:199], v1 offset:20992\n"
    "s_add_u32 m0, s42, 0\n"
    "s_mov_b32 s43, 4722688\n"
    "buffer_load_dwordx4 v5, s[12:15], s43 offen offset:0 lds\n"
    "s_mov_b32 s43, 3200\n"
    "buffer_load_dwordx4 v5, s[12:15], s43 offen offset:1024 lds\n"
    "s_waitcnt lgkmcnt(4)\n"
    "v_mfma_f32_32x32x16_bf16 v[160:175], v[204:207], v[76:79], v[160:175]\n"
    "ds_read_b64_tr_b16 v[200:201], v1 offset:36864\n"
    "ds_read_b64_tr_b16 v[202:203], v1 offset:37376\n"
    "s_waitcnt lgkmcnt(4)\n"
    "v_mfma_f32_32x32x16_bf16 v[160:175], v[192:195], v[80:83], v[160:175]\n"
    "ds_read_b64_tr_b16 v[204:205], v1 offset:53248\n"
    "ds_read_b64_tr_b16 v[206:207], v1 offset:53760\n"
    "s_waitcnt lgkmcnt(4)\n"
    "v_mfma_f32_32x32x16_bf16 v[160:175], v[196:199], v[84:87], v[160:175]\n"
    "ds_read_b64_tr_b16 v[192:193], v1 offset:5120\n"
    "ds_read_b64_tr_b16 v[194:195], v1 offset:5632\n"
    "s_waitcnt lgkmcnt(4)\n"
    "v_mfma_f32_32x32x16_bf16 v[160:175], v[200:203], v[88:91], v[160:175]\n"
    "ds_read_b64_tr_b16 v[196:197], v1 offset:21504\n"
    "ds_read_b64_tr_b16 v[198:199], v1 offset:22016\n"
    "s_waitcnt lgkmcnt(4)\n"
    "v_mfma_f32_32x32x16_bf16 v[160:175], v[204:207], v[92:95], v[160:175]\n"
    "ds_read_b64_tr_b16 v[200:201], v1 offset:37888\n"
    "ds_read_b64_tr_b16 v[202:203], v1 offset:38400\n"
    "s_waitcnt lgkmcnt(4)\n"
    "v_mfma_f32_32x32x16_bf16 v[160:175], v[192:195], v[96:99], v[160:175]\n"
    "ds_read_b64_tr_b16 v[204:205], v1 offset:54272\n"
    "ds_read_b64_tr_b16 v[206:207], v1 offset:54784\n"
    "s_waitcnt lgkmcnt(4)\n"
    "v_mfma_f32_32x32x16_bf16 v[160:175], v[196:199], v[100:103], v[160:175]\n"
    "s_waitcnt vmcnt(2)\n"
    "s_barrier\n"
    "s_waitcnt lgkmcnt(2)\n"
    "v_mfma_f32_32x32x16_bf16 v[160:175], v[200:203], v[104:107], v[160:175]\n"
    "ds_read_b64_tr_b16 v[192:193], v1 offset:6144\n"
    "ds_read_b64_tr_b16 v[194:195], v1 offset:6656\n"
    "ds_read_b64_tr_b16 v[196:197], v1 offset:22528\n"
    "ds_read_b64_tr_b16 v[198:199], v1 offset:23040\n"
    "s_mov_b32 s43, 592000\n"
    "buffer_load_dwordx4 v5, s[12:15], s43 offen offset:2048 lds\n"
    "s_mov_b32 s43, 1180800\n"
    "buffer_load_dwordx4 v5, s[12:15], s43 offen offset:3072 lds\n"
    "s_waitcnt lgkmcnt(4)\n"
    "v_mfma_f32_32x32x16_bf16 v[160:175], v[204:207], v[108:111], v[160:175]\n"
    "ds_read_b64_tr_b16 v[200:201], v1 offset:38912\n"
    "ds_read_b64_tr_b16 v[202:203], v1 offset:39424\n"
    "s_waitcnt lgkmcnt(4)\n"
    "v_mfma_f32_32x32x16_bf16 v[160:175], v[192:195], v[112:115], v[160:175]\n"
    "ds_read_b64_tr_b16 v[204:205], v1 offset:55296\n"
    "ds_read_b64_tr_b16 v[206:207], v1 offset:55808\n"
    "s_waitcnt lgkmcnt(4)\n"
    "v_mfma_f32_32x32x16_bf16 v[160:175], v[196:199], v[116:119], v[160:175]\n"
    "ds_read_b64_tr_b16 v[192:193], v1 offset:7168\n"
    "ds_read_b64_tr_b16 v[194:195], v1 offset:7680\n"
    "s_waitcnt lgkmcnt(4)\n"
    "v_mfma_f32_32x32x16_bf16 v[160:175], v[200:203], v[120:123], v[160:175]\n"
    "ds_read_b64_tr_b16 v[196:197], v1 offset:23552\n"
    "ds_read_b64_tr_b16 v[198:199], v1 offset:24064\n"
    "ds_read_b128 v[208:211], v2 offset:4096\n"
    "s_waitcnt lgkmcnt(5)\n"
    "v_mfma_f32_32x32x16_bf16 v[160:175], v[204:207], v[124:127], v[160:175]\n"
    "ds_read_b64_tr_b16 v[200:201], v1 offset:39936\n"
    "ds_read_b64_tr_b16 v[202:203], v1 offset:40448\n"
    "ds_read_b128 v[212:215], v2 offset:4128\n"
    "s_waitcnt lgkmcnt(6)\n"
    "v_mfma_f32_32x32x16_bf16 v[160:175], v[192:195], v[128:131], v[160:175]\n"
    "ds_read_b64_tr_b16 v[204:205], v1 offset:56320\n"
    "ds_read_b64_tr_b16 v[206:207], v1 offset:56832\n"
    "ds_read_b128 v[216:219], v2 offset:4160\n"
    "s_waitcnt lgkmcnt(7)\n"
    "v_mfma_f32_32x32x16_bf16 v[160:175], v[196:199], v[132:135], v[160:175]\n"
    "s_waitcnt vmcnt(2)\n"
    "s_barrier\n"
    "s_waitcnt lgkmcnt(4)\n"
    "v_mfma_f32_32x32x16_bf16 v[160:175], v[200:203], v[136:139], v[160:175]\n"
    "ds_read_b64_tr_b16 v[192:193], v1 offset:0\n"
    "ds_read_b64_tr_b16 v[194:195], v1 offset:512\n"
    "ds_read_b128 v[220:223], v2 offset:4192\n"
    "ds_read_b64_tr_b16 v[196:197], v1 offset:16384\n"
    "ds_read_b64_tr_b16 v[198:199], v1 offset:16896\n"
    "s_add_u32 m0, s42, 4096\n"
    "s_mov_b32 s43, 1773696\n"
    "buffer_load_dwordx4 v5, s[12:15], s43 offen offset:0 lds\n"
    "s_mov_b32 s43, 2362496\n"
    "buffer_load_dwordx4 v5, s[12:15], s43 offen offset:1024 lds\n"
    "s_waitcnt lgkmcnt(6)\n"
    "v_mfma_f32_32x32x16_bf16 v[160:175], v[204:207], v[140:143], v[160:175]\n"
    "ds_read_b64_tr_b16 v[200:201], v1 offset:32768\n"
    "ds_read_b64_tr_b16 v[202:203], v1 offset:33280\n"
    "s_waitcnt lgkmcnt(5)\n"
    "v_mfma_f32_32x32x16_bf16 v[160:175], v[192:195], v[144:147], v[160:175]\n"
    "ds_read_b64_tr_b16 v[204:205], v1 offset:49152\n"
    "ds_read_b64_tr_b16 v[206:207], v1 offset:49664\n"
    "s_waitcnt lgkmcnt(4)\n"
    "v_mfma_f32_32x32x16_bf16 v[160:175], v[196:199], v[148:151], v[160:175]\n"
    "ds_read_b64_tr_b16 v[192:193], v1 offset:1024\n"
    "ds_read_b64_tr_b16 v[194:195], v1 offset:1536\n"
    "s_waitcnt lgkmcnt(4)\n"
    "v_mfma_f32_32x32x16_bf16 v[160:175], v[200:203], v[152:155], v[160:175]\n"
    "ds_read_b64_tr_b16 v[196:197], v1 offset:17408\n"
    "ds_read_b64_tr_b16 v[198:199], v1 offset:17920\n"
    "s_waitcnt lgkmcnt(4)\n"
    "v_mfma_f32_32x32x16_bf16 v[160:175], v[204:207], v[156:159], v[160:175]\n"
    "ds_read_b64_tr_b16 v[200:201], v1 offset:33792\n"
    "ds_read_b64_tr_b16 v[202:203], v1 offset:34304\n"
    "s_waitcnt lgkmcnt(4)\n"
    "v_mfma_f32_32x32x16_bf16 v[176:191], v[192:195], v[16:19], v[208:223]\n"
    "ds_read_b64_tr_b16 v[204:205], v1 offset:50176\n"
    "ds_read_b64_tr_b16 v[206:207], v1 offset:50688\n"
    "s_waitcnt lgkmcnt(4)\n"
    "v_mfma_f32_32x32x16_bf16 v[176:191], v[196:199], v[20:23], v[176:191]\n"
    "s_waitcnt vmcnt(2)\n"
    "s_barrier\n"
    "s_waitcnt lgkmcnt(2)\n"
    "v_mfma_f32_32x32x16_bf16 v[176:191], v[200:203], v[24:27], v[176:191]\n"
    "ds_read_b64_tr_b16 v[192:193], v1 offset:2048\n"
    "ds_read_b64_tr_b16 v[194:195], v1 offset:2560\n"
    "ds_read_b64_tr_b16 v[196:197], v1 offset:18432\n"
    "ds_read_b64_tr_b16 v[198:199], v1 offset:18944\n"
    "s_mov_b32 s43, 2951296\n"
    "buffer_load_dwordx4 v5, s[12:15], s43 offen offset:2048 lds\n"
    "v_mul_f32 v224, v160, v160\n"
    "v_fmaak_f32 v224, v13, v224, 0xc0135761\n"
    "v_mul_f32 v224, v160, v224\n"
    "v_exp_f32 v224, v224\n"
    "v_mul_f32 v225, v161, v161\n"
    "v_fmaak_f32 v225, v13, v225, 0xc0135761\n"
    "s_mov_b32 s43, 3540096\n"
    "buffer_load_dwordx4 v5, s[12:15], s43 offen offset:3072 lds\n"
    "v_add_f32 v224, 1.0, v224\n"
    "v_mul_f32 v225, v161, v225\n"
    "v_rcp_f32 v224, v224\n"
    "s_waitcnt lgkmcnt(4)\n"
    "v_mfma_f32_32x32x16_bf16 v[176:191], v[204:207], v[28:31], v[176:191]\n"
    "ds_read_b64_tr_b16 v[200:201], v1 offset:34816\n"
    "ds_read_b64_tr_b16 v[202:203], v1 offset:35328\n"
    "v_exp_f32 v225, v225\n"
    "v_mul_f32 v226, v162, v162\n"
    "v_fmaak_f32 v226, v13, v226, 0xc0135761\n"
    "v_mul_f32 v160, v160, v224\n"
    "s_waitcnt lgkmcnt(4)\n"
    "v_mfma_f32_32x32x16_bf16 v[176:191], v[192:195], v[32:35], v[176:191]\n"
    "ds_read_b64_tr_b16 v[204:205], v1 offset:51200\n"
    "ds_read_b64_tr_b16 v[206:207], v1 offset:51712\n"
    "v_add_f32 v225, 1.0, v225\n"
    "v_mul_f32 v226, v162, v226\n"
    "v_rcp_f32 v225, v225\n"
    "s_waitcnt lgkmcnt(4)\n"
    "v_mfma_f32_32x32x16_bf16 v[176:191], v[196:199], v[36:39], v[176:191]\n"
    "ds_read_b64_tr_b16 v[192:193], v1 offset:3072\n"
    "ds_read_b64_tr_b16 v[194:195], v1 offset:3584\n"
    "v_exp_f32 v226, v226\n"
    "v_mul_f32 v227, v163, v163\n"
    "v_fmaak_f32 v227, v13, v227, 0xc0135761\n"
    "v_mul_f32 v161, v161, v225\n"
    "s_waitcnt lgkmcnt(4)\n"
    "v_mfma_f32_32x32x16_bf16 v[176:191], v[200:203], v[40:43], v[176:191]\n"
    "ds_read_b64_tr_b16 v[196:197], v1 offset:19456\n"
    "ds_read_b64_tr_b16 v[198:199], v1 offset:19968\n"
    "v_add_f32 v226, 1.0, v226\n"
    "v_mul_f32 v227, v163, v227\n"
    "v_rcp_f32 v226, v226\n"
    "v_cvt_pk_bf16_f32 v232, v160, v161\n"
    "s_waitcnt lgkmcnt(4)\n"
    "v_mfma_f32_32x32x16_bf16 v[176:191], v[204:207], v[44:47], v[176:191]\n"
    "ds_read_b64_tr_b16 v[200:201], v1 offset:35840\n"
    "ds_read_b64_tr_b16 v[202:203], v1 offset:36352\n"
    "v_exp_f32 v227, v227\n"
    "v_mul_f32 v224, v164, v164\n"
    "v_fmaak_f32 v224, v13, v224, 0xc0135761\n"
    "v_mul_f32 v162, v162, v226\n"
    "s_waitcnt lgkmcnt(4)\n"
    "v_mfma_f32_32x32x16_bf16 v[176:191], v[192:195], v[48:51], v[176:191]\n"
    "ds_read_b64_tr_b16 v[204:205], v1 offset:52224\n"
    "ds_read_b64_tr_b16 v[206:207], v1 offset:52736\n"
    "v_add_f32 v227, 1.0, v227\n"
    "v_mul_f32 v224, v164, v224\n"
    "v_rcp_f32 v227, v227\n"
    "s_waitcnt lgkmcnt(4)\n"
    "v_mfma_f32_32x32x16_bf16 v[176:191], v[196:199], v[52:55], v[176:191]\n"
    "s_waitcnt vmcnt(2)\n"
    "s_barrier\n"
    "s_waitcnt lgkmcnt(2)\n"
    "v_mfma_f32_32x32x16_bf16 v[176:191], v[200:203], v[56:59], v[176:191]\n"
    "ds_read_b64_tr_b16 v[192:193], v1 offset:4096\n"
    "ds_read_b64_tr_b16 v[194:195], v1 offset:4608\n"
    "ds_read_b64_tr_b16 v[196:197], v1 offset:20480\n"
    "ds_read_b64_tr_b16 v[198:199], v1 offset:20992\n"
    "s_add_u32 m0, s42, 0\n"
    "s_mov_b32 s43, 4132992\n"
    "buffer_load_dwordx4 v5, s[12:15], s43 offen offset:0 lds\n"
    "v_exp_f32 v224, v224\n"
    "v_mul_f32 v225, v165, v165\n"
    "v_fmaak_f32 v225, v13, v225, 0xc0135761\n"
    "v_mul_f32 v163, v163, v227\n"
    "s_mov_b32 s43, 4721792\n"
    "buffer_load_dwordx4 v5, s[12:15], s43 offen offset:1024 lds\n"
    "s_waitcnt lgkmcnt(4)\n"
    "v_mfma_f32_32x32x16_bf16 v[176:191], v[204:207], v[60:63], v[176:191]\n"
    "ds_read_b64_tr_b16 v[200:201], v1 offset:36864\n"
    "ds_read_b64_tr_b16 v[202:203], v1 offset:37376\n"
    "v_add_f32 v224, 1.0, v224\n"
    "v_mul_f32 v225, v165, v225\n"
    "v_rcp_f32 v224, v224\n"
    "v_cvt_pk_bf16_f32 v233, v162, v163\n"
    "s_waitcnt lgkmcnt(4)\n"
    "v_mfma_f32_32x32x16_bf16 v[176:191], v[192:195], v[64:67], v[176:191]\n"
    "ds_read_b64_tr_b16 v[204:205], v1 offset:53248\n"
    "ds_read_b64_tr_b16 v[206:207], v1 offset:53760\n"
    "v_exp_f32 v225, v225\n"
    "v_mul_f32 v226, v166, v166\n"
    "v_fmaak_f32 v226, v13, v226, 0xc0135761\n"
    "v_mul_f32 v164, v164, v224\n"
    "s_waitcnt lgkmcnt(4)\n"
    "v_mfma_f32_32x32x16_bf16 v[176:191], v[196:199], v[68:71], v[176:191]\n"
    "ds_read_b64_tr_b16 v[192:193], v1 offset:5120\n"
    "ds_read_b64_tr_b16 v[194:195], v1 offset:5632\n"
    "v_add_f32 v225, 1.0, v225\n"
    "v_mul_f32 v226, v166, v226\n"
    "v_rcp_f32 v225, v225\n"
    "s_waitcnt lgkmcnt(4)\n"
    "v_mfma_f32_32x32x16_bf16 v[176:191], v[200:203], v[72:75], v[176:191]\n"
    "ds_read_b64_tr_b16 v[196:197], v1 offset:21504\n"
    "ds_read_b64_tr_b16 v[198:199], v1 offset:22016\n"
    "v_exp_f32 v226, v226\n"
    "v_mul_f32 v227, v167, v167\n"
    "v_fmaak_f32 v227, v13, v227, 0xc0135761\n"
    "v_mul_f32 v165, v165, v225\n"
    "s_waitcnt lgkmcnt(4)\n"
    "v_mfma_f32_32x32x16_bf16 v[176:191], v[204:207], v[76:79], v[176:191]\n"
    "ds_read_b64_tr_b16 v[200:201], v1 offset:37888\n"
    "ds_read_b64_tr_b16 v[202:203], v1 offset:38400\n"
    "v_add_f32 v226, 1.0, v226\n"
    "v_mul_f32 v227, v167, v227\n"
    "v_rcp_f32 v226, v226\n"
    "v_cvt_pk_bf16_f32 v234, v164, v165\n"
    "s_waitcnt lgkmcnt(4)\n"
    "v_mfma_f32_32x32x16_bf16 v[176:191], v[192:195], v[80:83], v[176:191]\n"
    "ds_read_b64_tr_b16 v[204:205], v1 offset:54272\n"
    "ds_read_b64_tr_b16 v[206:207], v1 offset:54784\n"
    "v_exp_f32 v227, v227\n"
    "v_mul_f32 v224, v168, v168\n"
    "v_fmaak_f32 v224, v13, v224, 0xc0135761\n"
    "v_mul_f32 v166, v166, v226\n"
    "v_add_f32 v227, 1.0, v227\n"
    "v_mul_f32 v224, v168, v224\n"
    "v_rcp_f32 v227, v227\n"
    "s_waitcnt lgkmcnt(4)\n"
    "v_mfma_f32_32x32x16_bf16 v[176:191], v[196:199], v[84:87], v[176:191]\n"
    "s_waitcnt vmcnt(2)\n"
    "s_barrier\n"
    "s_waitcnt lgkmcnt(2)\n"
    "v_mfma_f32_32x32x16_bf16 v[176:191], v[200:203], v[88:91], v[176:191]\n"
    "ds_read_b64_tr_b16 v[192:193], v1 offset:6144\n"
    "ds_read_b64_tr_b16 v[194:195], v1 offset:6656\n"
    "ds_read_b64_tr_b16 v[196:197], v1 offset:22528\n"
    "ds_read_b64_tr_b16 v[198:199], v1 offset:23040\n"
    "s_mov_b32 s43, 2304\n"
    "buffer_load_dwordx4 v5, s[12:15], s43 offen offset:2048 lds\n"
    "v_exp_f32 v224, v224\n"
    "v_mul_f32 v225, v169, v169\n"
    "v_fmaak_f32 v225, v13, v225, 0xc0135761\n"
    "v_mul_f32 v167, v167, v227\n"
    "s_mov_b32 s43, 591104\n"
    "buffer_load_dwordx4 v5, s[12:15], s43 offen offset:3072 lds\n"
    "s_waitcnt lgkmcnt(4)\n"
    "v_mfma_f32_32x32x16_bf16 v[176:191], v[204:207], v[92:95], v[176:191]\n"
    "ds_read_b64_tr_b16 v[200:201], v1 offset:38912\n"
    "ds_read_b64_tr_b16 v[202:203], v1 offset:39424\n"
    "v_add_f32 v224, 1.0, v224\n"
    "v_mul_f32 v225, v169, v225\n"
    "v_rcp_f32 v224, v224\n"
    "v_cvt_pk_bf16_f32 v235, v166, v167\n"
    "s_waitcnt lgkmcnt(4)\n"
    "v_mfma_f32_32x32x16_bf16 v[176:191], v[192:195], v[96:99], v[176:191]\n"
    "ds_read_b64_tr_b16 v[204:205], v1 offset:55296\n"
    "ds_read_b64_tr_b16 v[206:207], v1 offset:55808\n"
    "v_exp_f32 v225, v225\n"
    "v_mul_f32 v226, v170, v170\n"
    "v_fmaak_f32 v226, v13, v226, 0xc0135761\n"
    "v_mul_f32 v168, v168, v224\n"
    "s_waitcnt lgkmcnt(4)\n"
    "v_mfma_f32_32x32x16_bf16 v[176:191], v[196:199], v[100:103], v[176:191]\n"
    "ds_read_b64_tr_b16 v[192:193], v1 offset:7168\n"
    "ds_read_b64_tr_b16 v[194:195], v1 offset:7680\n"
    "v_add_f32 v225, 1.0, v225\n"
    "v_mul_f32 v226, v170, v226\n"
    "v_rcp_f32 v225, v225\n"
    "ds_write_b128 v7, v[232:235] offset:0\n"
    "s_waitcnt lgkmcnt(5)\n"
    "v_mfma_f32_32x32x16_bf16 v[176:191], v[200:203], v[104:107], v[176:191]\n"
    "ds_read_b64_tr_b16 v[196:197], v1 offset:23552\n"
    "ds_read_b64_tr_b16 v[198:199], v1 offset:24064\n"
    "v_exp_f32 v226, v226\n"
    "v_mul_f32 v227, v171, v171\n"
    "v_fmaak_f32 v227, v13, v227, 0xc0135761\n"
    "v_mul_f32 v169, v169, v225\n"
    "s_waitcnt lgkmcnt(5)\n"
    "v_mfma_f32_32x32x16_bf16 v[176:191], v[204:207], v[108:111], v[176:191]\n"
    "ds_read_b64_tr_b16 v[200:201], v1 offset:39936\n"
    "ds_read_b64_tr_b16 v[202:203], v1 offset:40448\n"
    "v_add_f32 v226, 1.0, v226\n"
    "v_mul_f32 v227, v171, v227\n"
    "v_rcp_f32 v226, v226\n"
    "v_cvt_pk_bf16_f32 v236, v168, v169\n"
    "s_waitcnt lgkmcnt(5)\n"
    "v_mfma_f32_32x32x16_bf16 v[176:191], v[192:195], v[112:115], v[176:191]\n"
    "ds_read_b64_tr_b16 v[204:205], v1 offset:56320\n"
    "ds_read_b64_tr_b16 v[206:207], v1 offset:56832\n"
    "v_exp_f32 v227, v227\n"
    "v_mul_f32 v224, v172, v172\n"
    "v_fmaak_f32 v224, v13, v224, 0xc0135761\n"
    "v_mul_f32 v170, v170, v226\n"
    "s_waitcnt lgkmcnt(4)\n"
    "v_mfma_f32_32x32x16_bf16 v[176:191], v[196:199], v[116:119], v[176:191]\n"
    "s_waitcnt vmcnt(2)\n"
    "s_barrier\n"
    "s_waitcnt lgkmcnt(2)\n"
    "v_mfma_f32_32x32x16_bf16 v[176:191], v[200:203], v[120:123], v[176:191]\n"
    "ds_read_b64_tr_b16 v[192:193], v1 offset:0\n"
    "ds_read_b64_tr_b16 v[194:195], v1 offset:512\n"
    "ds_read_b64_tr_b16 v[196:197], v1 offset:16384\n"
    "ds_read_b64_tr_b16 v[198:199], v1 offset:16896\n"
    "ds_read_b128 v[208:211], v2 offset:8192\n"
    "s_add_u32 m0, s42, 4096\n"
    "s_mov_b32 s43, 1184000\n"
    "buffer_load_dwordx4 v5, s[12:15], s43 offen offset:0 lds\n"
    "v_add_f32 v227, 1.0, v227\n"
    "v_mul_f32 v224, v172, v224\n"
    "v_rcp_f32 v227, v227\n"
    "v_exp_f32 v224, v224\n"
    "v_mul_f32 v225, v173, v173\n"
    "v_fmaak_f32 v225, v13, v225, 0xc0135761\n"
    "v_mul_f32 v171, v171, v227\n"
    "s_waitcnt lgkmcnt(5)\n"
    "v_mfma_f32_32x32x16_bf16 v[176:191], v[204:207], v[124:127], v[176:191]\n"
    "ds_read_b64_tr_b16 v[200:201], v1 offset:32768\n"
    "ds_read_b64_tr_b16 v[202:203], v1 offset:33280\n"
    "ds_read_b128 v[212:215], v2 offset:8224\n"
    "v_add_f32 v224, 1.0, v224\n"
    "v_mul_f32 v225, v173, v225\n"
    "v_rcp_f32 v224, v224\n"
    "v_cvt_pk_bf16_f32 v237, v170, v171\n"
    "s_waitcnt lgkmcnt(6)\n"
    "v_mfma_f32_32x32x16_bf16 v[176:191], v[192:195], v[128:131], v[176:191]\n"
    "ds_read_b64_tr_b16 v[204:205], v1 offset:49152\n"
    "ds_read_b64_tr_b16 v[206:207], v1 offset:49664\n"
    "ds_read_b128 v[216:219], v2 offset:8256\n"
    "v_exp_f32 v225, v225\n"
    "v_mul_f32 v226, v174, v174\n"
    "v_fmaak_f32 v226, v13, v226, 0xc0135761\n"
    "v_mul_f32 v172, v172, v224\n"
    "s_waitcnt lgkmcnt(7)\n"
    "v_mfma_f32_32x32x16_bf16 v[176:191], v[196:199], v[132:135], v[176:191]\n"
    "ds_read_b64_tr_b16 v[192:193], v1 offset:1024\n"
    "ds_read_b64_tr_b16 v[194:195], v1 offset:1536\n"
    "ds_read_b128 v[220:223], v2 offset:8288\n"
    "v_add_f32 v225, 1.0, v225\n"
    "v_mul_f32 v226, v174, v226\n"
    "v_rcp_f32 v225, v225\n"
    "s_waitcnt lgkmcnt(7)\n"
    "v_mfma_f32_32x32x16_bf16 v[176:191], v[200:203], v[136:139], v[176:191]\n"
    "ds_read_b64_tr_b16 v[196:197], v1 offset:17408\n"
    "ds_read_b64_tr_b16 v[198:199], v1 offset:17920\n"
    "v_exp_f32 v226, v226\n"
    "v_mul_f32 v227, v175, v175\n"
    "v_fmaak_f32 v227, v13, v227, 0xc0135761\n"
    "v_mul_f32 v173, v173, v225\n"
    "s_waitcnt lgkmcnt(6)\n"
    "v_mfma_f32_32x32x16_bf16 v[176:191], v[204:207], v[140:143], v[176:191]\n"
    "ds_read_b64_tr_b16 v[200:201], v1 offset:33792\n"
    "ds_read_b64_tr_b16 v[202:203], v1 offset:34304\n"
    "v_add_f32 v226, 1.0, v226\n"
    "v_mul_f32 v227, v175, v227\n"
    "v_rcp_f32 v226, v226\n"
    "v_cvt_pk_bf16_f32 v238, v172, v173\n"
    "s_waitcnt lgkmcnt(5)\n"
    "v_mfma_f32_32x32x16_bf16 v[176:191], v[192:195], v[144:147], v[176:191]\n"
    "ds_read_b64_tr_b16 v[204:205], v1 offset:50176\n"
    "ds_read_b64_tr_b16 v[206:207], v1 offset:50688\n"
    "v_exp_f32 v227, v227\n"
    "v_mul_f32 v174, v174, v226\n"
    "s_mov_b32 s43, 1772800\n"
    "buffer_load_dwordx4 v5, s[12:15], s43 offen offset:1024 lds\n"
    "s_waitcnt lgkmcnt(4)\n"
    "v_mfma_f32_32x32x16_bf16 v[176:191], v[196:199], v[148:151], v[176:191]\n"
    "s_waitcnt vmcnt(2)\n"
    "s_barrier\n"
    "s_waitcnt lgkmcnt(2)\n"
    "v_mfma_f32_32x32x16_bf16 v[176:191], v[200:203], v[152:155], v[176:191]\n"
    "ds_read_b64_tr_b16 v[192:193], v1 offset:2048\n"
    "ds_read_b64_tr_b16 v[194:195], v1 offset:2560\n"
    "ds_read_b64_tr_b16 v[196:197], v1 offset:18432\n"
    "ds_read_b64_tr_b16 v[198:199], v1 offset:18944\n"
    "v_add_f32 v227, 1.0, v227\n"
    "v_rcp_f32 v227, v227\n"
    "s_mov_b32 s43, 2361600\n"
    "buffer_load_dwordx4 v5, s[12:15], s43 offen offset:2048 lds\n"
    "v_mul_f32 v175, v175, v227\n"
    "v_cvt_pk_bf16_f32 v239, v174, v175\n"
    "s_waitcnt lgkmcnt(4)\n"
    "v_mfma_f32_32x32x16_bf16 v[176:191], v[204:207], v[156:159], v[176:191]\n"
    "ds_read_b64_tr_b16 v[200:201], v1 offset:34816\n"
    "ds_read_b64_tr_b16 v[202:203], v1 offset:35328\n"
    "s_mov_b32 s43, 2950400\n"
    "buffer_load_dwordx4 v5, s[12:15], s43 offen offset:3072 lds\n"
    "ds_write_b128 v7, v[236:239] offset:256\n"
    "s_waitcnt lgkmcnt(5)\n"
    "v_mfma_f32_32x32x16_bf16 v[160:175], v[192:195], v[16:19], v[208:223]\n"
    "ds_read_b64_tr_b16 v[204:205], v1 offset:51200\n"
    "ds_read_b64_tr_b16 v[206:207], v1 offset:51712\n"
    "s_waitcnt lgkmcnt(5)\n"
    "v_mfma_f32_32x32x16_bf16 v[160:175], v[196:199], v[20:23], v[160:175]\n"
    "ds_read_b64_tr_b16 v[192:193], v1 offset:3072\n"
    "ds_read_b64_tr_b16 v[194:195], v1 offset:3584\n"
    "v_mul_f32 v224, v176, v176\n"
    "v_fmaak_f32 v224, v13, v224, 0xc0135761\n"
    "v_mul_f32 v224, v176, v224\n"
    "v_exp_f32 v224, v224\n"
    "v_mul_f32 v225, v177, v177\n"
    "v_fmaak_f32 v225, v13, v225, 0xc0135761\n"
    "s_waitcnt lgkmcnt(5)\n"
    "v_mfma_f32_32x32x16_bf16 v[160:175], v[200:203], v[24:27], v[160:175]\n"
    "ds_read_b64_tr_b16 v[196:197], v1 offset:19456\n"
    "ds_read_b64_tr_b16 v[198:199], v1 offset:19968\n"
    "v_add_f32 v224, 1.0, v224\n"
    "v_mul_f32 v225, v177, v225\n"
    "v_rcp_f32 v224, v224\n"
    "s_waitcnt lgkmcnt(4)\n"
    "v_mfma_f32_32x32x16_bf16 v[160:175], v[204:207], v[28:31], v[160:175]\n"
    "ds_read_b64_tr_b16 v[200:201], v1 offset:35840\n"
    "ds_read_b64_tr_b16 v[202:203], v1 offset:36352\n"
    "v_exp_f32 v225, v225\n"
    "v_mul_f32 v226, v178, v178\n"
    "v_fmaak_f32 v226, v13, v226, 0xc0135761\n"
    "v_mul_f32 v176, v176, v224\n"
    "s_waitcnt lgkmcnt(4)\n"
    "v_mfma_f32_32x32x16_bf16 v[160:175], v[192:195], v[32:35], v[160:175]\n"
    "ds_read_b64_tr_b16 v[204:205], v1 offset:52224\n"
    "ds_read_b64_tr_b16 v[206:207], v1 offset:52736\n"
    "v_add_f32 v225, 1.0, v225\n"
    "v_mul_f32 v226, v178, v226\n"
    "v_rcp_f32 v225, v225\n"
    "s_waitcnt lgkmcnt(4)\n"
    "v_mfma_f32_32x32x16_bf16 v[160:175], v[196:199], v[36:39], v[160:175]\n"
    "s_waitcnt vmcnt(2)\n"
    "s_barrier\n"
    "s_waitcnt lgkmcnt(2)\n"
    "v_mfma_f32_32x32x16_bf16 v[160:175], v[200:203], v[40:43], v[160:175]\n"
    "ds_read_b64_tr_b16 v[192:193], v1 offset:4096\n"
    "ds_read_b64_tr_b16 v[194:195], v1 offset:4608\n"
    "ds_read_b64_tr_b16 v[196:197], v1 offset:20480\n"
    "ds_read_b64_tr_b16 v[198:199], v1 offset:20992\n"
    "s_add_u32 m0, s42, 0\n"
    "s_mov_b32 s43, 3543296\n"
    "buffer_load_dwordx4 v5, s[12:15], s43 offen offset:0 lds\n"
    "v_exp_f32 v226, v226\n"
    "v_mul_f32 v227, v179, v179\n"
    "v_fmaak_f32 v227, v13, v227, 0xc0135761\n"
    "v_mul_f32 v177, v177, v225\n"
    "s_mov_b32 s43, 4132096\n"
    "buffer_load_dwordx4 v5, s[12:15], s43 offen offset:1024 lds\n"
    "v_add_f32 v226, 1.0, v226\n"
    "v_mul_f32 v227, v179, v227\n"
    "v_rcp_f32 v226, v226\n"
    "v_cvt_pk_bf16_f32 v232, v176, v177\n"
    "s_waitcnt lgkmcnt(4)\n"
    "v_mfma_f32_32x32x16_bf16 v[160:175], v[204:207], v[44:47], v[160:175]\n"
    "ds_read_b64_tr_b16 v[200:201], v1 offset:36864\n"
    "ds_read_b64_tr_b16 v[202:203], v1 offset:37376\n"
    "v_exp_f32 v227, v227\n"
    "v_mul_f32 v224, v180, v180\n"
    "v_fmaak_f32 v224, v13, v224, 0xc0135761\n"
    "v_mul_f32 v178, v178, v226\n"
    "s_waitcnt lgkmcnt(4)\n"
    "v_mfma_f32_32x32x16_bf16 v[160:175], v[192:195], v[48:51], v[160:175]\n"
    "ds_read_b64_tr_b16 v[204:205], v1 offset:53248\n"
    "ds_read_b64_tr_b16 v[206:207], v1 offset:53760\n"
    "v_add_f32 v227, 1.0, v227\n"
    "v_mul_f32 v224, v180, v224\n"
    "v_rcp_f32 v227, v227\n"
    "s_waitcnt lgkmcnt(4)\n"
    "v_mfma_f32_32x32x16_bf16 v[160:175], v[196:199], v[52:55], v[160:175]\n"
    "ds_read_b64_tr_b16 v[192:193], v1 offset:5120\n"
    "ds_read_b64_tr_b16 v[194:195], v1 offset:5632\n"
    "v_exp_f32 v224, v224\n"
    "v_mul_f32 v225, v181, v181\n"
    "v_fmaak_f32 v225, v13, v225, 0xc0135761\n"
    "v_mul_f32 v179, v179, v227\n"
    "s_waitcnt lgkmcnt(4)\n"
    "v_mfma_f32_32x32x16_bf16 v[160:175], v[200:203], v[56:59], v[160:175]\n"
    "ds_read_b64_tr_b16 v[196:197], v1 offset:21504\n"
    "ds_read_b64_tr_b16 v[198:199], v1 offset:22016\n"
    "v_add_f32 v224, 1.0, v224\n"
    "v_mul_f32 v225, v181, v225\n"
    "v_rcp_f32 v224, v224\n"
    "v_cvt_pk_bf16_f32 v233, v178, v179\n"
    "s_waitcnt lgkmcnt(4)\n"
    "v_mfma_f32_32x32x16_bf16 v[160:175], v[204:207], v[60:63], v[160:175]\n"
    "ds_read_b64_tr_b16 v[200:201], v1 offset:37888\n"
    "ds_read_b64_tr_b16 v[202:203], v1 offset:38400\n"
    "v_exp_f32 v225, v225\n"
    "v_mul_f32 v226, v182, v182\n"
    "v_fmaak_f32 v226, v13, v226, 0xc0135761\n"
    "v_mul_f32 v180, v180, v224\n"
    "s_waitcnt lgkmcnt(4)\n"
    "v_mfma_f32_32x32x16_bf16 v[160:175], v[192:195], v[64:67], v[160:175]\n"
    "ds_read_b64_tr_b16 v[204:205], v1 offset:54272\n"
    "ds_read_b64_tr_b16 v[206:207], v1 offset:54784\n"
    "v_add_f32 v225, 1.0, v225\n"
    "v_mul_f32 v226, v182, v226\n"
    "v_rcp_f32 v225, v225\n"
    "s_waitcnt lgkmcnt(4)\n"
    "v_mfma_f32_32x32x16_bf16 v[160:175], v[196:199], v[68:71], v[160:175]\n"
    "s_waitcnt vmcnt(2)\n"
    "s_barrier\n"
    "s_waitcnt lgkmcnt(2)\n"
    "v_mfma_f32_32x32x16_bf16 v[160:175], v[200:203], v[72:75], v[160:175]\n"
    "ds_read_b64_tr_b16 v[192:193], v1 offset:6144\n"
    "ds_read_b64_tr_b16 v[194:195], v1 offset:6656\n"
    "ds_read_b64_tr_b16 v[196:197], v1 offset:22528\n"
    "ds_read_b64_tr_b16 v[198:199], v1 offset:23040\n"
    "s_mov_b32 s43, 4720896\n"
    "buffer_load_dwordx4 v5, s[12:15], s43 offen offset:2048 lds\n"
    "v_exp_f32 v226, v226\n"
    "v_mul_f32 v227, v183, v183\n"
    "v_fmaak_f32 v227, v13, v227, 0xc0135761\n"
    "v_mul_f32 v181, v181, v225\n"
    "s_mov_b32 s43, 1408\n"
    "buffer_load_dwordx4 v5, s[12:15], s43 offen offset:3072 lds\n"
    "s_waitcnt lgkmcnt(4)\n"
    "v_mfma_f32_32x32x16_bf16 v[160:175], v[204:207], v[76:79], v[160:175]\n"
    "ds_read_b64_tr_b16 v[200:201], v1 offset:38912\n"
    "ds_read_b64_tr_b16 v[202:203], v1 offset:39424\n"
    "v_add_f32 v226, 1.0, v226\n"
    "v_mul_f32 v227, v183, v227\n"
    "v_rcp_f32 v226, v226\n"
    "v_cvt_pk_bf16_f32 v234, v180, v181\n"
    "s_waitcnt lgkmcnt(4)\n"
    "v_mfma_f32_32x32x16_bf16 v[160:175], v[192:195], v[80:83], v[160:175]\n"
    "ds_read_b64_tr_b16 v[204:205], v1 offset:55296\n"
    "ds_read_b64_tr_b16 v[206:207], v1 offset:55808\n"
    "v_exp_f32 v227, v227\n"
    "v_mul_f32 v224, v184, v184\n"
    "v_fmaak_f32 v224, v13, v224, 0xc0135761\n"
    "v_mul_f32 v182, v182, v226\n"
    "s_waitcnt lgkmcnt(4)\n"
    "v_mfma_f32_32x32x16_bf16 v[160:175], v[196:199], v[84:87], v[160:175]\n"
    "ds_read_b64_tr_b16 v[192:193], v1 offset:7168\n"
    "ds_read_b64_tr_b16 v[194:195], v1 offset:7680\n"
    "v_add_f32 v227, 1.0, v227\n"
    "v_mul_f32 v224, v184, v224\n"
    "v_rcp_f32 v227, v227\n"
    "s_waitcnt lgkmcnt(4)\n"
    "v_mfma_f32_32x32x16_bf16 v[160:175], v[200:203], v[88:91], v[160:175]\n"
    "ds_read_b64_tr_b16 v[196:197], v1 offset:23552\n"
    "ds_read_b64_tr_b16 v[198:199], v1 offset:24064\n"
    "v_exp_f32 v224, v224\n"
    "v_mul_f32 v225, v185, v185\n"
    "v_fmaak_f32 v225, v13, v225, 0xc0135761\n"
    "v_mul_f32 v183, v183, v227\n"
    "s_waitcnt lgkmcnt(4)\n"
    "v_mfma_f32_32x32x16_bf16 v[160:175], v[204:207], v[92:95], v[160:175]\n"
    "ds_read_b64_tr_b16 v[200:201], v1 offset:39936\n"
    "ds_read_b64_tr_b16 v[202:203], v1 offset:40448\n"
    "v_add_f32 v224, 1.0, v224\n"
    "v_mul_f32 v225, v185, v225\n"
    "v_rcp_f32 v224, v224\n"
    "v_cvt_pk_bf16_f32 v235, v182, v183\n"
    "s_waitcnt lgkmcnt(4)\n"
    "v_mfma_f32_32x32x16_bf16 v[160:175], v[192:195], v[96:99], v[160:175]\n"
    "ds_read_b64_tr_b16 v[204:205], v1 offset:56320\n"
    "ds_read_b64_tr_b16 v[206:207], v1 offset:56832\n"
    "v_exp_f32 v225, v225\n"
    "v_mul_f32 v226, v186, v186\n"
    "v_fmaak_f32 v226, v13, v226, 0xc0135761\n"
    "v_mul_f32 v184, v184, v224\n"
    "s_waitcnt lgkmcnt(4)\n"
    "v_mfma_f32_32x32x16_bf16 v[160:175], v[196:199], v[100:103], v[160:175]\n"
    "s_waitcnt vmcnt(2)\n"
    "s_barrier\n"
    "s_waitcnt lgkmcnt(2)\n"
    "v_mfma_f32_32x32x16_bf16 v[160:175], v[200:203], v[104:107], v[160:175]\n"
    "ds_read_b64_tr_b16 v[192:193], v1 offset:0\n"
    "ds_read_b64_tr_b16 v[194:195], v1 offset:512\n"
    "ds_read_b64_tr_b16 v[196:197], v1 offset:16384\n"
    "ds_read_b64_tr_b16 v[198:199], v1 offset:16896\n"
    "s_add_u32 m0, s42, 4096\n"
    "s_mov_b32 s43, 594304\n"
    "buffer_load_dwordx4 v5, s[12:15], s43 offen offset:0 lds\n"
    "v_add_f32 v225, 1.0, v225\n"
    "v_mul_f32 v226, v186, v226\n"
    "v_rcp_f32 v225, v225\n"
    "ds_write_b128 v7, v[232:235] offset:4096\n"
    "s_mov_b32 s43, 1183104\n"
    "buffer_load_dwordx4 v5, s[12:15], s43 offen offset:1024 lds\n"
    "v_exp_f32 v226, v226\n"
    "v_mul_f32 v227, v187, v187\n"
    "v_fmaak_f32 v227, v13, v227, 0xc0135761\n"
    "v_mul_f32 v185, v185, v225\n"
    "s_waitcnt lgkmcnt(5)\n"
    "v_mfma_f32_32x32x16_bf16 v[160:175], v[204:207], v[108:111], v[160:175]\n"
    "ds_read_b64_tr_b16 v[200:201], v1 offset:32768\n"
    "ds_read_b64_tr_b16 v[202:203], v1 offset:33280\n"
    "v_add_f32 v226, 1.0, v226\n"
    "v_mul_f32 v227, v187, v227\n"
    "v_rcp_f32 v226, v226\n"
    "v_cvt_pk_bf16_f32 v236, v184, v185\n"
    "s_waitcnt lgkmcnt(5)\n"
    "v_mfma_f32_32x32x16_bf16 v[160:175], v[192:195], v[112:115], v[160:175]\n"
    "ds_read_b64_tr_b16 v[204:205], v1 offset:49152\n"
    "ds_read_b64_tr_b16 v[206:207], v1 offset:49664\n"
    "v_exp_f32 v227, v227\n"
    "v_mul_f32 v224, v188, v188\n"
    "v_fmaak_f32 v224, v13, v224, 0xc0135761\n"
    "v_mul_f32 v186, v186, v226\n"
    "s_waitcnt lgkmcnt(5)\n"
    "v_mfma_f32_32x32x16_bf16 v[160:175], v[196:199], v[116:119], v[160:175]\n"
    "ds_read_b64_tr_b16 v[192:193], v1 offset:1024\n"
    "ds_read_b64_tr_b16 v[194:195], v1 offset:1536\n"
    "v_add_f32 v227, 1.0, v227\n"
    "v_mul_f32 v224, v188, v224\n"
    "v_rcp_f32 v227, v227\n"
    "s_waitcnt lgkmcnt(4)\n"
    "v_mfma_f32_32x32x16_bf16 v[160:175], v[200:203], v[120:123], v[160:175]\n"
    "ds_read_b64_tr_b16 v[196:197], v1 offset:17408\n"
    "ds_read_b64_tr_b16 v[198:199], v1 offset:17920\n"
    "ds_read_b128 v[208:211], v2 offset:12288\n"
    "v_exp_f32 v224, v224\n"
    "v_mul_f32 v225, v189, v189\n"
    "v_fmaak_f32 v225, v13, v225, 0xc0135761\n"
    "v_mul_f32 v187, v187, v227\n"
    "s_waitcnt lgkmcnt(5)\n"
    "v_mfma_f32_32x32x16_bf16 v[160:175], v[204:207], v[124:127], v[160:175]\n"
    "ds_read_b64_tr_b16 v[200:201], v1 offset:33792\n"
    "ds_read_b64_tr_b16 v[202:203], v1 offset:34304\n"
    "ds_read_b128 v[212:215], v2 offset:12320\n"
    "v_add_f32 v224, 1.0, v224\n"
    "v_mul_f32 v225, v189, v225\n"
    "v_rcp_f32 v224, v224\n"
    "v_cvt_pk_bf16_f32 v237, v186, v187\n"
    "s_waitcnt lgkmcnt(6)\n"
    "v_mfma_f32_32x32x16_bf16 v[160:175], v[192:195], v[128:131], v[160:175]\n"
    "ds_read_b64_tr_b16 v[204:205], v1 offset:50176\n"
    "ds_read_b64_tr_b16 v[206:207], v1 offset:50688\n"
    "ds_read_b128 v[216:219], v2 offset:12352\n"
    "v_exp_f32 v225, v225\n"
    "v_mul_f32 v226, v190, v190\n"
    "v_fmaak_f32 v226, v13, v226, 0xc0135761\n"
    "v_mul_f32 v188, v188, v224\n"
    "s_waitcnt lgkmcnt(7)\n"
    "v_mfma_f32_32x32x16_bf16 v[160:175], v[196:199], v[132:135], v[160:175]\n"
    "s_waitcnt vmcnt(2)\n"
    "s_barrier\n"
    "s_waitcnt lgkmcnt(4)\n"
    "v_mfma_f32_32x32x16_bf16 v[160:175], v[200:203], v[136:139], v[160:175]\n"
    "ds_read_b64_tr_b16 v[192:193], v1 offset:2048\n"
    "ds_read_b64_tr_b16 v[194:195], v1 offset:2560\n"
    "ds_read_b128 v[220:223], v2 offset:12384\n"
    "ds_read_b64_tr_b16 v[196:197], v1 offset:18432\n"
    "ds_read_b64_tr_b16 v[198:199], v1 offset:18944\n"
    "v_add_f32 v225, 1.0, v225\n"
    "v_mul_f32 v226, v190, v226\n"
    "v_rcp_f32 v225, v225\n"
    "v_exp_f32 v226, v226\n"
    "v_mul_f32 v227, v191, v191\n"
    "v_fmaak_f32 v227, v13, v227, 0xc0135761\n"
    "v_mul_f32 v189, v189, v225\n"
    "s_waitcnt lgkmcnt(6)\n"
    "v_mfma_f32_32x32x16_bf16 v[160:175], v[204:207], v[140:143], v[160:175]\n"
    "ds_read_b64_tr_b16 v[200:201], v1 offset:34816\n"
    "ds_read_b64_tr_b16 v[202:203], v1 offset:35328\n"
    "v_add_f32 v226, 1.0, v226\n"
    "v_mul_f32 v227, v191, v227\n"
    "v_rcp_f32 v226, v226\n"
    "v_cvt_pk_bf16_f32 v238, v188, v189\n"
    "s_waitcnt lgkmcnt(5)\n"
    "v_mfma_f32_32x32x16_bf16 v[160:175], v[192:195], v[144:147], v[160:175]\n"
    "ds_read_b64_tr_b16 v[204:205], v1 offset:51200\n"
    "ds_read_b64_tr_b16 v[206:207], v1 offset:51712\n"
    "v_exp_f32 v227, v227\n"
    "v_mul_f32 v190, v190, v226\n"
    "s_waitcnt lgkmcnt(4)\n"
    "v_mfma_f32_32x32x16_bf16 v[160:175], v[196:199], v[148:151], v[160:175]\n"
    "ds_read_b64_tr_b16 v[192:193], v1 offset:3072\n"
    "ds_read_b64_tr_b16 v[194:195], v1 offset:3584\n"
    "s_mov_b32 s43, 1771904\n"
    "buffer_load_dwordx4 v5, s[12:15], s43 offen offset:2048 lds\n"
    "v_add_f32 v227, 1.0, v227\n"
    "v_rcp_f32 v227, v227\n"
    "s_waitcnt lgkmcnt(4)\n"
    "v_mfma_f32_32x32x16_bf16 v[160:175], v[200:203], v[152:155], v[160:175]\n"
    "ds_read_b64_tr_b16 v[196:197], v1 offset:19456\n"
    "ds_read_b64_tr_b16 v[198:199], v1 offset:19968\n"
    "s_mov_b32 s43, 2360704\n"
    "buffer_load_dwordx4 v5, s[12:15], s43 offen offset:3072 lds\n"
    "v_mul_f32 v191, v191, v227\n"
    "v_cvt_pk_bf16_f32 v239, v190, v191\n"
    "s_waitcnt lgkmcnt(4)\n"
    "v_mfma_f32_32x32x16_bf16 v[160:175], v[204:207], v[156:159], v[160:175]\n"
    "ds_read_b64_tr_b16 v[200:201], v1 offset:35840\n"
    "ds_read_b64_tr_b16 v[202:203], v1 offset:36352\n"
    "ds_write_b128 v7, v[236:239] offset:4352\n"
    "s_waitcnt lgkmcnt(5)\n"
    "v_mfma_f32_32x32x16_bf16 v[176:191], v[192:195], v[16:19], v[208:223]\n"
    "ds_read_b64_tr_b16 v[204:205], v1 offset:52224\n"
    "ds_read_b64_tr_b16 v[206:207], v1 offset:52736\n"
    "s_waitcnt lgkmcnt(5)\n"
    "v_mfma_f32_32x32x16_bf16 v[176:191], v[196:199], v[20:23], v[176:191]\n"
    "s_waitcnt vmcnt(2)\n"
    "s_barrier\n"
    "s_waitcnt lgkmcnt(3)\n"
    "v_mfma_f32_32x32x16_bf16 v[176:191], v[200:203], v[24:27], v[176:191]\n"
    "ds_read_b64_tr_b16 v[192:193], v1 offset:4096\n"
    "ds_read_b64_tr_b16 v[194:195], v1 offset:4608\n"
    "ds_read_b64_tr_b16 v[196:197], v1 offset:20480\n"
    "ds_read_b64_tr_b16 v[198:199], v1 offset:20992\n"
    "s_add_u32 m0, s42, 0\n"
    "s_mov_b32 s43, 2953600\n"
    "buffer_load_dwordx4 v5, s[12:15], s43 offen offset:0 lds\n"
    "v_mul_f32 v224, v160, v160\n"
    "v_fmaak_f32 v224, v13, v224, 0xc0135761\n"
    "v_mul_f32 v224, v160, v224\n"
    "v_exp_f32 v224, v224\n"
    "v_mul_f32 v225, v161, v161\n"
    "v_fmaak_f32 v225, v13, v225, 0xc0135761\n"
    "s_mov_b32 s43, 3542400\n"
    "buffer_load_dwordx4 v5, s[12:15], s43 offen offset:1024 lds\n"
    "v_add_f32 v224, 1.0, v224\n"
    "v_mul_f32 v225, v161, v225\n"
    "v_rcp_f32 v224, v224\n"
    "s_waitcnt lgkmcnt(4)\n"
    "v_mfma_f32_32x32x16_bf16 v[176:191], v[204:207], v[28:31], v[176:191]\n"
    "ds_read_b64_tr_b16 v[200:201], v1 offset:36864\n"
    "ds_read_b64_tr_b16 v[202:203], v1 offset:37376\n"
    "v_exp_f32 v225, v225\n"
    "v_mul_f32 v226, v162, v162\n"
    "v_fmaak_f32 v226, v13, v226, 0xc0135761\n"
    "v_mul_f32 v160, v160, v224\n"
    "s_waitcnt lgkmcnt(4)\n"
    "v_mfma_f32_32x32x16_bf16 v[176:191], v[192:195], v[32:35], v[176:191]\n"
    "ds_read_b64_tr_b16 v[204:205], v1 offset:53248\n"
    "ds_read_b64_tr_b16 v[206:207], v1 offset:53760\n"
    "v_add_f32 v225, 1.0, v225\n"
    "v_mul_f32 v226, v162, v226\n"
    "v_rcp_f32 v225, v225\n"
    "s_waitcnt lgkmcnt(4)\n"
    "v_mfma_f32_32x32x16_bf16 v[176:191], v[196:199], v[36:39], v[176:191]\n"
    "ds_read_b64_tr_b16 v[192:193], v1 offset:5120\n"
    "ds_read_b64_tr_b16 v[194:195], v1 offset:5632\n"
    "v_exp_f32 v226, v226\n"
    "v_mul_f32 v227, v163, v163\n"
    "v_fmaak_f32 v227, v13, v227, 0xc0135761\n"
    "v_mul_f32 v161, v161, v225\n"
    "s_waitcnt lgkmcnt(4)\n"
    "v_mfma_f32_32x32x16_bf16 v[176:191], v[200:203], v[40:43], v[176:191]\n"
    "ds_read_b64_tr_b16 v[196:197], v1 offset:21504\n"
    "ds_read_b64_tr_b16 v[198:199], v1 offset:22016\n"
    "v_add_f32 v226, 1.0, v226\n"
    "v_mul_f32 v227, v163, v227\n"
    "v_rcp_f32 v226, v226\n"
    "v_cvt_pk_bf16_f32 v232, v160, v161\n"
    "s_waitcnt lgkmcnt(4)\n"
    "v_mfma_f32_32x32x16_bf16 v[176:191], v[204:207], v[44:47], v[176:191]\n"
    "ds_read_b64_tr_b16 v[200:201], v1 offset:37888\n"
    "ds_read_b64_tr_b16 v[202:203], v1 offset:38400\n"
    "v_exp_f32 v227, v227\n"
    "v_mul_f32 v224, v164, v164\n"
    "v_fmaak_f32 v224, v13, v224, 0xc0135761\n"
    "v_mul_f32 v162, v162, v226\n"
    "s_waitcnt lgkmcnt(4)\n"
    "v_mfma_f32_32x32x16_bf16 v[176:191], v[192:195], v[48:51], v[176:191]\n"
    "ds_read_b64_tr_b16 v[204:205], v1 offset:54272\n"
    "ds_read_b64_tr_b16 v[206:207], v1 offset:54784\n"
    "v_add_f32 v227, 1.0, v227\n"
    "v_mul_f32 v224, v164, v224\n"
    "v_rcp_f32 v227, v227\n"
    "s_waitcnt lgkmcnt(4)\n"
    "v_mfma_f32_32x32x16_bf16 v[176:191], v[196:199], v[52:55], v[176:191]\n"
    "s_waitcnt vmcnt(2)\n"
    "s_barrier\n"
    "s_waitcnt lgkmcnt(2)\n"
    "v_mfma_f32_32x32x16_bf16 v[176:191], v[200:203], v[56:59], v[176:191]\n"
    "ds_read_b64_tr_b16 v[192:193], v1 offset:6144\n"
    "ds_read_b64_tr_b16 v[194:195], v1 offset:6656\n"
    "ds_read_b64_tr_b16 v[196:197], v1 offset:22528\n"
    "ds_read_b64_tr_b16 v[198:199], v1 offset:23040\n"
    "s_mov_b32 s43, 4131200\n"
    "buffer_load_dwordx4 v5, s[12:15], s43 offen offset:2048 lds\n"
    "v_exp_f32 v224, v224\n"
    "v_mul_f32 v225, v165, v165\n"
    "v_fmaak_f32 v225, v13, v225, 0xc0135761\n"
    "v_mul_f32 v163, v163, v227\n"
    "s_mov_b32 s43, 4720000\n"
    "buffer_load_dwordx4 v5, s[12:15], s43 offen offset:3072 lds\n"
    "s_waitcnt lgkmcnt(4)\n"
    "v_mfma_f32_32x32x16_bf16 v[176:191], v[204:207], v[60:63], v[176:191]\n"
    "ds_read_b64_tr_b16 v[200:201], v1 offset:38912\n"
    "ds_read_b64_tr_b16 v[202:203], v1 offset:39424\n"
    "v_add_f32 v224, 1.0, v224\n"
    "v_mul_f32 v225, v165, v225\n"
    "v_rcp_f32 v224, v224\n"
    "v_cvt_pk_bf16_f32 v233, v162, v163\n"
    "s_waitcnt lgkmcnt(4)\n"
    "v_mfma_f32_32x32x16_bf16 v[176:191], v[192:195], v[64:67], v[176:191]\n"
    "ds_read_b64_tr_b16 v[204:205], v1 offset:55296\n"
    "ds_read_b64_tr_b16 v[206:207], v1 offset:55808\n"
    "v_exp_f32 v225, v225\n"
    "v_mul_f32 v226, v166, v166\n"
    "v_fmaak_f32 v226, v13, v226, 0xc0135761\n"
    "v_mul_f32 v164, v164, v224\n"
    "s_waitcnt lgkmcnt(4)\n"
    "v_mfma_f32_32x32x16_bf16 v[176:191], v[196:199], v[68:71], v[176:191]\n"
    "ds_read_b64_tr_b16 v[192:193], v1 offset:7168\n"
    "ds_read_b64_tr_b16 v[194:195], v1 offset:7680\n"
    "v_add_f32 v225, 1.0, v225\n"
    "v_mul_f32 v226, v166, v226\n"
    "v_rcp_f32 v225, v225\n"
    "s_waitcnt lgkmcnt(4)\n"
    "v_mfma_f32_32x32x16_bf16 v[176:191], v[200:203], v[72:75], v[176:191]\n"
    "ds_read_b64_tr_b16 v[196:197], v1 offset:23552\n"
    "ds_read_b64_tr_b16 v[198:199], v1 offset:24064\n"
    "v_exp_f32 v226, v226\n"
    "v_mul_f32 v227, v167, v167\n"
    "v_fmaak_f32 v227, v13, v227, 0xc0135761\n"
    "v_mul_f32 v165, v165, v225\n"
    "s_waitcnt lgkmcnt(4)\n"
    "v_mfma_f32_32x32x16_bf16 v[176:191], v[204:207], v[76:79], v[176:191]\n"
    "ds_read_b64_tr_b16 v[200:201], v1 offset:39936\n"
    "ds_read_b64_tr_b16 v[202:203], v1 offset:40448\n"
    "v_add_f32 v226, 1.0, v226\n"
    "v_mul_f32 v227, v167, v227\n"
    "v_rcp_f32 v226, v226\n"
    "v_cvt_pk_bf16_f32 v234, v164, v165\n"
    "s_waitcnt lgkmcnt(4)\n"
    "v_mfma_f32_32x32x16_bf16 v[176:191], v[192:195], v[80:83], v[176:191]\n"
    "ds_read_b64_tr_b16 v[204:205], v1 offset:56320\n"
    "ds_read_b64_tr_b16 v[206:207], v1 offset:56832\n"
    "v_exp_f32 v227, v227\n"
    "v_mul_f32 v224, v168, v168\n"
    "v_fmaak_f32 v224, v13, v224, 0xc0135761\n"
    "v_mul_f32 v166, v166, v226\n"
    "s_waitcnt lgkmcnt(4)\n"
    "v_mfma_f32_32x32x16_bf16 v[176:191], v[196:199], v[84:87], v[176:191]\n"
    "s_waitcnt vmcnt(2)\n"
    "s_barrier\n"
    "s_waitcnt lgkmcnt(2)\n"
    "v_mfma_f32_32x32x16_bf16 v[176:191], v[200:203], v[88:91], v[176:191]\n"
    "ds_read_b64_tr_b16 v[192:193], v1 offset:0\n"
    "ds_read_b64_tr_b16 v[194:195], v1 offset:512\n"
    "ds_read_b64_tr_b16 v[196:197], v1 offset:16384\n"
    "ds_read_b64_tr_b16 v[198:199], v1 offset:16896\n"
    "s_add_u32 m0, s42, 4096\n"
    "s_mov_b32 s43, 4608\n"
    "buffer_load_dwordx4 v5, s[12:15], s43 offen offset:0 lds\n"
    "v_add_f32 v227, 1.0, v227\n"
    "v_mul_f32 v224, v168, v224\n"
    "v_rcp_f32 v227, v227\n"
    "s_mov_b32 s43, 593408\n"
    "buffer_load_dwordx4 v5, s[12:15], s43 offen offset:1024 lds\n"
    "v_exp_f32 v224, v224\n"
    "v_mul_f32 v225, v169, v169\n"
    "v_fmaak_f32 v225, v13, v225, 0xc0135761\n"
    "v_mul_f32 v167, v167, v227\n"
    "s_waitcnt lgkmcnt(4)\n"
    "v_mfma_f32_32x32x16_bf16 v[176:191], v[204:207], v[92:95], v[176:191]\n"
    "ds_read_b64_tr_b16 v[200:201], v1 offset:32768\n"
    "ds_read_b64_tr_b16 v[202:203], v1 offset:33280\n"
    "v_add_f32 v224, 1.0, v224\n"
    "v_mul_f32 v225, v169, v225\n"
    "v_rcp_f32 v224, v224\n"
    "v_cvt_pk_bf16_f32 v235, v166, v167\n"
    "s_waitcnt lgkmcnt(4)\n"
    "v_mfma_f32_32x32x16_bf16 v[176:191], v[192:195], v[96:99], v[176:191]\n"
    "ds_read_b64_tr_b16 v[204:205], v1 offset:49152\n"
    "ds_read_b64_tr_b16 v[206:207], v1 offset:49664\n"
    "v_exp_f32 v225, v225\n"
    "v_mul_f32 v226, v170, v170\n"
    "v_fmaak_f32 v226, v13, v226, 0xc0135761\n"
    "v_mul_f32 v168, v168, v224\n"
    "s_waitcnt lgkmcnt(4)\n"
    "v_mfma_f32_32x32x16_bf16 v[176:191], v[196:199], v[100:103], v[176:191]\n"
    "ds_read_b64_tr_b16 v[192:193], v1 offset:1024\n"
    "ds_read_b64_tr_b16 v[194:195], v1 offset:1536\n"
    "v_add_f32 v225, 1.0, v225\n"
    "v_mul_f32 v226, v170, v226\n"
    "v_rcp_f32 v225, v225\n"
    "ds_write_b128 v7, v[232:235] offset:8192\n"
    "s_waitcnt lgkmcnt(5)\n"
    "v_mfma_f32_32x32x16_bf16 v[176:191], v[200:203], v[104:107], v[176:191]\n"
    "ds_read_b64_tr_b16 v[196:197], v1 offset:17408\n"
    "ds_read_b64_tr_b16 v[198:199], v1 offset:17920\n"
    "v_exp_f32 v226, v226\n"
    "v_mul_f32 v227, v171, v171\n"
    "v_fmaak_f32 v227, v13, v227, 0xc0135761\n"
    "v_mul_f32 v169, v169, v225\n"
    "s_waitcnt lgkmcnt(5)\n"
    "v_mfma_f32_32x32x16_bf16 v[176:191], v[204:207], v[108:111], v[176:191]\n"
    "ds_read_b64_tr_b16 v[200:201], v1 offset:33792\n"
    "ds_read_b64_tr_b16 v[202:203], v1 offset:34304\n"
    "v_add_f32 v226, 1.0, v226\n"
    "v_mul_f32 v227, v171, v227\n"
    "v_rcp_f32 v226, v226\n"
    "v_cvt_pk_bf16_f32 v236, v168, v169\n"
    "s_waitcnt lgkmcnt(5)\n"
    "v_mfma_f32_32x32x16_bf16 v[176:191], v[192:195], v[112:115], v[176:191]\n"
    "ds_read_b64_tr_b16 v[204:205], v1 offset:50176\n"
    "ds_read_b64_tr_b16 v[206:207], v1 offset:50688\n"
    "v_exp_f32 v227, v227\n"
    "v_mul_f32 v224, v172, v172\n"
    "v_fmaak_f32 v224, v13, v224, 0xc0135761\n"
    "v_mul_f32 v170, v170, v226\n"
    "s_waitcnt lgkmcnt(4)\n"
    "v_mfma_f32_32x32x16_bf16 v[176:191], v[196:199], v[116:119], v[176:191]\n"
    "s_waitcnt vmcnt(2)\n"
    "s_barrier\n"
    "s_waitcnt lgkmcnt(2)\n"
    "v_mfma_f32_32x32x16_bf16 v[176:191], v[200:203], v[120:123], v[176:191]\n"
    "ds_read_b64_tr_b16 v[192:193], v1 offset:2048\n"
    "ds_read_b64_tr_b16 v[194:195], v1 offset:2560\n"
    "ds_read_b64_tr_b16 v[196:197], v1 offset:18432\n"
    "ds_read_b64_tr_b16 v[198:199], v1 offset:18944\n"
    "ds_read_b128 v[208:211], v2 offset:16384\n"
    "v_add_f32 v227, 1.0, v227\n"
    "v_mul_f32 v224, v172, v224\n"
    "v_rcp_f32 v227, v227\n"
    "s_mov_b32 s43, 1182208\n"
    "buffer_load_dwordx4 v5, s[12:15], s43 offen offset:2048 lds\n"
    "v_exp_f32 v224, v224\n"
    "v_mul_f32 v225, v173, v173\n"
    "v_fmaak_f32 v225, v13, v225, 0xc0135761\n"
    "v_mul_f32 v171, v171, v227\n"
    "s_waitcnt lgkmcnt(5)\n"
    "v_mfma_f32_32x32x16_bf16 v[176:191], v[204:207], v[124:127], v[176:191]\n"
    "ds_read_b64_tr_b16 v[200:201], v1 offset:34816\n"
    "ds_read_b64_tr_b16 v[202:203], v1 offset:35328\n"
    "ds_read_b128 v[212:215], v2 offset:16416\n"
    "v_add_f32 v224, 1.0, v224\n"
    "v_mul_f32 v225, v173, v225\n"
    "v_rcp_f32 v224, v224\n"
    "v_cvt_pk_bf16_f32 v237, v170, v171\n"
    "s_waitcnt lgkmcnt(6)\n"
    "v_mfma_f32_32x32x16_bf16 v[176:191], v[192:195], v[128:131], v[176:191]\n"
    "ds_read_b64_tr_b16 v[204:205], v1 offset:51200\n"
    "ds_read_b64_tr_b16 v[206:207], v1 offset:51712\n"
    "ds_read_b128 v[216:219], v2 offset:16448\n"
    "v_exp_f32 v225, v225\n"
    "v_mul_f32 v226, v174, v174\n"
    "v_fmaak_f32 v226, v13, v226, 0xc0135761\n"
    "v_mul_f32 v172, v172, v224\n"
    "s_waitcnt lgkmcnt(7)\n"
    "v_mfma_f32_32x32x16_bf16 v[176:191], v[196:199], v[132:135], v[176:191]\n"
    "ds_read_b64_tr_b16 v[192:193], v1 offset:3072\n"
    "ds_read_b64_tr_b16 v[194:195], v1 offset:3584\n"
    "ds_read_b128 v[220:223], v2 offset:16480\n"
    "v_add_f32 v225, 1.0, v225\n"
    "v_mul_f32 v226, v174, v226\n"
    "v_rcp_f32 v225, v225\n"
    "s_waitcnt lgkmcnt(7)\n"
    "v_mfma_f32_32x32x16_bf16 v[176:191], v[200:203], v[136:139], v[176:191]\n"
    "ds_read_b64_tr_b16 v[196:197], v1 offset:19456\n"
    "ds_read_b64_tr_b16 v[198:199], v1 offset:19968\n"
    "v_exp_f32 v226, v226\n"
    "v_mul_f32 v227, v175, v175\n"
    "v_fmaak_f32 v227, v13, v227, 0xc0135761\n"
    "v_mul_f32 v173, v173, v225\n"
    "s_waitcnt lgkmcnt(6)\n"
    "v_mfma_f32_32x32x16_bf16 v[176:191], v[204:207], v[140:143], v[176:191]\n"
    "ds_read_b64_tr_b16 v[200:201], v1 offset:35840\n"
    "ds_read_b64_tr_b16 v[202:203], v1 offset:36352\n"
    "v_add_f32 v226, 1.0, v226\n"
    "v_mul_f32 v227, v175, v227\n"
    "v_rcp_f32 v226, v226\n"
    "v_cvt_pk_bf16_f32 v238, v172, v173\n"
    "s_waitcnt lgkmcnt(5)\n"
    "v_mfma_f32_32x32x16_bf16 v[176:191], v[192:195], v[144:147], v[176:191]\n"
    "ds_read_b64_tr_b16 v[204:205], v1 offset:52224\n"
    "ds_read_b64_tr_b16 v[206:207], v1 offset:52736\n"
    "v_exp_f32 v227, v227\n"
    "v_mul_f32 v174, v174, v226\n"
    "s_mov_b32 s43, 1771008\n"
    "buffer_load_dwordx4 v5, s[12:15], s43 offen offset:3072 lds\n"
    "s_waitcnt lgkmcnt(4)\n"
    "v_mfma_f32_32x32x16_bf16 v[176:191], v[196:199], v[148:151], v[176:191]\n"
    "s_waitcnt vmcnt(2)\n"
    "s_barrier\n"
    "s_waitcnt lgkmcnt(2)\n"
    "v_mfma_f32_32x32x16_bf16 v[176:191], v[200:203], v[152:155], v[176:191]\n"
    "ds_read_b64_tr_b16 v[192:193], v1 offset:4096\n"
    "ds_read_b64_tr_b16 v[194:195], v1 offset:4608\n"
    "ds_read_b64_tr_b16 v[196:197], v1 offset:20480\n"
    "ds_read_b64_tr_b16 v[198:199], v1 offset:20992\n"
    "v_add_f32 v227, 1.0, v227\n"
    "v_rcp_f32 v227, v227\n"
    "s_add_u32 m0, s42, 0\n"
    "s_mov_b32 s43, 2363904\n"
    "buffer_load_dwordx4 v5, s[12:15], s43 offen offset:0 lds\n"
    "v_mul_f32 v175, v175, v227\n"
    "v_cvt_pk_bf16_f32 v239, v174, v175\n"
    "s_waitcnt lgkmcnt(4)\n"
    "v_mfma_f32_32x32x16_bf16 v[176:191], v[204:207], v[156:159], v[176:191]\n"
    "ds_read_b64_tr_b16 v[200:201], v1 offset:36864\n"
    "ds_read_b64_tr_b16 v[202:203], v1 offset:37376\n"
    "s_mov_b32 s43, 2952704\n"
    "buffer_load_dwordx4 v5, s[12:15], s43 offen offset:1024 lds\n"
    "ds_write_b128 v7, v[236:239] offset:8448\n"
    "s_waitcnt lgkmcnt(5)\n"
    "v_mfma_f32_32x32x16_bf16 v[160:175], v[192:195], v[16:19], v[208:223]\n"
    "ds_read_b64_tr_b16 v[204:205], v1 offset:53248\n"
    "ds_read_b64_tr_b16 v[206:207], v1 offset:53760\n"
    "s_waitcnt lgkmcnt(5)\n"
    "v_mfma_f32_32x32x16_bf16 v[160:175], v[196:199], v[20:23], v[160:175]\n"
    "ds_read_b64_tr_b16 v[192:193], v1 offset:5120\n"
    "ds_read_b64_tr_b16 v[194:195], v1 offset:5632\n"
    "v_mul_f32 v224, v176, v176\n"
    "v_fmaak_f32 v224, v13, v224, 0xc0135761\n"
    "v_mul_f32 v224, v176, v224\n"
    "v_exp_f32 v224, v224\n"
    "v_mul_f32 v225, v177, v177\n"
    "v_fmaak_f32 v225, v13, v225, 0xc0135761\n"
    "s_waitcnt lgkmcnt(5)\n"
    "v_mfma_f32_32x32x16_bf16 v[160:175], v[200:203], v[24:27], v[160:175]\n"
    "ds_read_b64_tr_b16 v[196:197], v1 offset:21504\n"
    "ds_read_b64_tr_b16 v[198:199], v1 offset:22016\n"
    "v_add_f32 v224, 1.0, v224\n"
    "v_mul_f32 v225, v177, v225\n"
    "v_rcp_f32 v224, v224\n"
    "s_waitcnt lgkmcnt(4)\n"
    "v_mfma_f32_32x32x16_bf16 v[160:175], v[204:207], v[28:31], v[160:175]\n"
    "ds_read_b64_tr_b16 v[200:201], v1 offset:37888\n"
    "ds_read_b64_tr_b16 v[202:203], v1 offset:38400\n"
    "v_exp_f32 v225, v225\n"
    "v_mul_f32 v226, v178, v178\n"
    "v_fmaak_f32 v226, v13, v226, 0xc0135761\n"
    "v_mul_f32 v176, v176, v224\n"
    "s_waitcnt lgkmcnt(4)\n"
    "v_mfma_f32_32x32x16_bf16 v[160:175], v[192:195], v[32:35], v[160:175]\n"
    "ds_read_b64_tr_b16 v[204:205], v1 offset:54272\n"
    "ds_read_b64_tr_b16 v[206:207], v1 offset:54784\n"
    "v_add_f32 v225, 1.0, v225\n"
    "v_mul_f32 v226, v178, v226\n"
    "v_rcp_f32 v225, v225\n"
    "s_waitcnt lgkmcnt(4)\n"
    "v_mfma_f32_32x32x16_bf16 v[160:175], v[196:199], v[36:39], v[160:175]\n"
    "s_waitcnt vmcnt(2)\n"
    "s_barrier\n"
    "s_waitcnt lgkmcnt(2)\n"
    "v_mfma_f32_32x32x16_bf16 v[160:175], v[200:203], v[40:43], v[160:175]\n"
    "ds_read_b64_tr_b16 v[192:193], v1 offset:6144\n"
    "ds_read_b64_tr_b16 v[194:195], v1 offset:6656\n"
    "ds_read_b64_tr_b16 v[196:197], v1 offset:22528\n"
    "ds_read_b64_tr_b16 v[198:199], v1 offset:23040\n"
    "s_mov_b32 s43, 3541504\n"
    "buffer_load_dwordx4 v5, s[12:15], s43 offen offset:2048 lds\n"
    "v_exp_f32 v226, v226\n"
    "v_mul_f32 v227, v179, v179\n"
    "v_fmaak_f32 v227, v13, v227, 0xc0135761\n"
    "v_mul_f32 v177, v177, v225\n"
    "s_mov_b32 s43, 4130304\n"
    "buffer_load_dwordx4 v5, s[12:15], s43 offen offset:3072 lds\n"
    "s_waitcnt lgkmcnt(4)\n"
    "v_mfma_f32_32x32x16_bf16 v[160:175], v[204:207], v[44:47], v[160:175]\n"
    "ds_read_b64_tr_b16 v[200:201], v1 offset:38912\n"
    "ds_read_b64_tr_b16 v[202:203], v1 offset:39424\n"
    "v_add_f32 v226, 1.0, v226\n"
    "v_mul_f32 v227, v179, v227\n"
    "v_rcp_f32 v226, v226\n"
    "v_cvt_pk_bf16_f32 v232, v176, v177\n"
    "s_waitcnt lgkmcnt(4)\n"
    "v_mfma_f32_32x32x16_bf16 v[160:175], v[192:195], v[48:51], v[160:175]\n"
    "ds_read_b64_tr_b16 v[204:205], v1 offset:55296\n"
    "ds_read_b64_tr_b16 v[206:207], v1 offset:55808\n"
    "v_exp_f32 v227, v227\n"
    "v_mul_f32 v224, v180, v180\n"
    "v_fmaak_f32 v224, v13, v224, 0xc0135761\n"
    "v_mul_f32 v178, v178, v226\n"
    "s_waitcnt lgkmcnt(4)\n"
    "v_mfma_f32_32x32x16_bf16 v[160:175], v[196:199], v[52:55], v[160:175]\n"
    "ds_read_b64_tr_b16 v[192:193], v1 offset:7168\n"
    "ds_read_b64_tr_b16 v[194:195], v1 offset:7680\n"
    "v_add_f32 v227, 1.0, v227\n"
    "v_mul_f32 v224, v180, v224\n"
    "v_rcp_f32 v227, v227\n"
    "s_waitcnt lgkmcnt(4)\n"
    "v_mfma_f32_32x32x16_bf16 v[160:175], v[200:203], v[56:59], v[160:175]\n"
    "ds_read_b64_tr_b16 v[196:197], v1 offset:23552\n"
    "ds_read_b64_tr_b16 v[198:199], v1 offset:24064\n"
    "v_exp_f32 v224, v224\n"
    "v_mul_f32 v225, v181, v181\n"
    "v_fmaak_f32 v225, v13, v225, 0xc0135761\n"
    "v_mul_f32 v179, v179, v227\n"
    "s_waitcnt lgkmcnt(4)\n"
    "v_mfma_f32_32x32x16_bf16 v[160:175], v[204:207], v[60:63], v[160:175]\n"
    "ds_read_b64_tr_b16 v[200:201], v1 offset:39936\n"
    "ds_read_b64_tr_b16 v[202:203], v1 offset:40448\n"
    "v_add_f32 v224, 1.0, v224\n"
    "v_mul_f32 v225, v181, v225\n"
    "v_rcp_f32 v224, v224\n"
    "v_cvt_pk_bf16_f32 v233, v178, v179\n"
    "s_waitcnt lgkmcnt(4)\n"
    "v_mfma_f32_32x32x16_bf16 v[160:175], v[192:195], v[64:67], v[160:175]\n"
    "ds_read_b64_tr_b16 v[204:205], v1 offset:56320\n"
    "ds_read_b64_tr_b16 v[206:207], v1 offset:56832\n"
    "v_exp_f32 v225, v225\n"
    "v_mul_f32 v226, v182, v182\n"
    "v_fmaak_f32 v226, v13, v226, 0xc0135761\n"
    "v_mul_f32 v180, v180, v224\n"
    "v_add_f32 v225, 1.0, v225\n"
    "v_mul_f32 v226, v182, v226\n"
    "v_rcp_f32 v225, v225\n"
    "s_waitcnt lgkmcnt(4)\n"
    "v_mfma_f32_32x32x16_bf16 v[160:175], v[196:199], v[68:71], v[160:175]\n"
    "s_waitcnt vmcnt(2)\n"
    "s_barrier\n"
    "s_waitcnt lgkmcnt(2)\n"
    "v_mfma_f32_32x32x16_bf16 v[160:175], v[200:203], v[72:75], v[160:175]\n"
    "ds_read_b64_tr_b16 v[192:193], v1 offset:0\n"
    "ds_read_b64_tr_b16 v[194:195], v1 offset:512\n"
    "ds_read_b64_tr_b16 v[196:197], v1 offset:16384\n"
    "ds_read_b64_tr_b16 v[198:199], v1 offset:16896\n"
    "s_add_u32 m0, s42, 4096\n"
    "s_mov_b32 s43, 4723200\n"
    "buffer_load_dwordx4 v5, s[12:15], s43 offen offset:0 lds\n"
    "v_exp_f32 v226, v226\n"
    "v_mul_f32 v227, v183, v183\n"
    "v_fmaak_f32 v227, v13, v227, 0xc0135761\n"
    "v_mul_f32 v181, v181, v225\n"
    "s_mov_b32 s43, 3712\n"
    "buffer_load_dwordx4 v5, s[12:15], s43 offen offset:1024 lds\n"
    "s_waitcnt lgkmcnt(4)\n"
    "v_mfma_f32_32x32x16_bf16 v[160:175], v[204:207], v[76:79], v[160:175]\n"
    "ds_read_b64_tr_b16 v[200:201], v1 offset:32768\n"
    "ds_read_b64_tr_b16 v[202:203], v1 offset:33280\n"
    "v_add_f32 v226, 1.0, v226\n"
    "v_mul_f32 v227, v183, v227\n"
    "v_rcp_f32 v226, v226\n"
    "v_cvt_pk_bf16_f32 v234, v180, v181\n"
    "s_waitcnt lgkmcnt(4)\n"
    "v_mfma_f32_32x32x16_bf16 v[160:175], v[192:195], v[80:83], v[160:175]\n"
    "ds_read_b64_tr_b16 v[204:205], v1 offset:49152\n"
    "ds_read_b64_tr_b16 v[206:207], v1 offset:49664\n"
    "v_exp_f32 v227, v227\n"
    "v_mul_f32 v224, v184, v184\n"
    "v_fmaak_f32 v224, v13, v224, 0xc0135761\n"
    "v_mul_f32 v182, v182, v226\n"
    "s_waitcnt lgkmcnt(4)\n"
    "v_mfma_f32_32x32x16_bf16 v[160:175], v[196:199], v[84:87], v[160:175]\n"
    "ds_read_b64_tr_b16 v[192:193], v1 offset:1024\n"
    "ds_read_b64_tr_b16 v[194:195], v1 offset:1536\n"
    "v_add_f32 v227, 1.0, v227\n"
    "v_mul_f32 v224, v184, v224\n"
    "v_rcp_f32 v227, v227\n"
    "s_waitcnt lgkmcnt(4)\n"
    "v_mfma_f32_32x32x16_bf16 v[160:175], v[200:203], v[88:91], v[160:175]\n"
    "ds_read_b64_tr_b16 v[196:197], v1 offset:17408\n"
    "ds_read_b64_tr_b16 v[198:199], v1 offset:17920\n"
    "v_exp_f32 v224, v224\n"
    "v_mul_f32 v225, v185, v185\n"
    "v_fmaak_f32 v225, v13, v225, 0xc0135761\n"
    "v_mul_f32 v183, v183, v227\n"
    "s_waitcnt lgkmcnt(4)\n"
    "v_mfma_f32_32x32x16_bf16 v[160:175], v[204:207], v[92:95], v[160:175]\n"
    "ds_read_b64_tr_b16 v[200:201], v1 offset:33792\n"
    "ds_read_b64_tr_b16 v[202:203], v1 offset:34304\n"
    "v_add_f32 v224, 1.0, v224\n"
    "v_mul_f32 v225, v185, v225\n"
    "v_rcp_f32 v224, v224\n"
    "v_cvt_pk_bf16_f32 v235, v182, v183\n"
    "s_waitcnt lgkmcnt(4)\n"
    "v_mfma_f32_32x32x16_bf16 v[160:175], v[192:195], v[96:99], v[160:175]\n"
    "ds_read_b64_tr_b16 v[204:205], v1 offset:50176\n"
    "ds_read_b64_tr_b16 v[206:207], v1 offset:50688\n"
    "v_exp_f32 v225, v225\n"
    "v_mul_f32 v226, v186, v186\n"
    "v_fmaak_f32 v226, v13, v226, 0xc0135761\n"
    "v_mul_f32 v184, v184, v224\n"
    "s_waitcnt lgkmcnt(4)\n"
    "v_mfma_f32_32x32x16_bf16 v[160:175], v[196:199], v[100:103], v[160:175]\n"
    "s_waitcnt vmcnt(2)\n"
    "s_barrier\n"
    "s_waitcnt lgkmcnt(2)\n"
    "v_mfma_f32_32x32x16_bf16 v[160:175], v[200:203], v[104:107], v[160:175]\n"
    "ds_read_b64_tr_b16 v[192:193], v1 offset:2048\n"
    "ds_read_b64_tr_b16 v[194:195], v1 offset:2560\n"
    "ds_read_b64_tr_b16 v[196:197], v1 offset:18432\n"
    "ds_read_b64_tr_b16 v[198:199], v1 offset:18944\n"
    "s_mov_b32 s43, 592512\n"
    "buffer_load_dwordx4 v5, s[12:15], s43 offen offset:2048 lds\n"
    "v_add_f32 v225, 1.0, v225\n"
    "v_mul_f32 v226, v186, v226\n"
    "v_rcp_f32 v225, v225\n"
    "ds_write_b128 v7, v[232:235] offset:12288\n"
    "s_mov_b32 s43, 1181312\n"
    "buffer_load_dwordx4 v5, s[12:15], s43 offen offset:3072 lds\n"
    "v_exp_f32 v226, v226\n"
    "v_mul_f32 v227, v187, v187\n"
    "v_fmaak_f32 v227, v13, v227, 0xc0135761\n"
    "v_mul_f32 v185, v185, v225\n"
    "s_waitcnt lgkmcnt(5)\n"
    "v_mfma_f32_32x32x16_bf16 v[160:175], v[204:207], v[108:111], v[160:175]\n"
    "ds_read_b64_tr_b16 v[200:201], v1 offset:34816\n"
    "ds_read_b64_tr_b16 v[202:203], v1 offset:35328\n"
    "v_add_f32 v226, 1.0, v226\n"
    "v_mul_f32 v227, v187, v227\n"
    "v_rcp_f32 v226, v226\n"
    "v_cvt_pk_bf16_f32 v236, v184, v185\n"
    "s_waitcnt lgkmcnt(5)\n"
    "v_mfma_f32_32x32x16_bf16 v[160:175], v[192:195], v[112:115], v[160:175]\n"
    "ds_read_b64_tr_b16 v[204:205], v1 offset:51200\n"
    "ds_read_b64_tr_b16 v[206:207], v1 offset:51712\n"
    "v_exp_f32 v227, v227\n"
    "v_mul_f32 v224, v188, v188\n"
    "v_fmaak_f32 v224, v13, v224, 0xc0135761\n"
    "v_mul_f32 v186, v186, v226\n"
    "s_waitcnt lgkmcnt(5)\n"
    "v_mfma_f32_32x32x16_bf16 v[160:175], v[196:199], v[116:119], v[160:175]\n"
    "ds_read_b64_tr_b16 v[192:193], v1 offset:3072\n"
    "ds_read_b64_tr_b16 v[194:195], v1 offset:3584\n"
    "v_add_f32 v227, 1.0, v227\n"
    "v_mul_f32 v224, v188, v224\n"
    "v_rcp_f32 v227, v227\n"
    "s_waitcnt lgkmcnt(4)\n"
    "v_mfma_f32_32x32x16_bf16 v[160:175], v[200:203], v[120:123], v[160:175]\n"
    "ds_read_b64_tr_b16 v[196:197], v1 offset:19456\n"
    "ds_read_b64_tr_b16 v[198:199], v1 offset:19968\n"
    "ds_read_b128 v[208:211], v2 offset:20480\n"
    "v_exp_f32 v224, v224\n"
    "v_mul_f32 v225, v189, v189\n"
    "v_fmaak_f32 v225, v13, v225, 0xc0135761\n"
    "v_mul_f32 v187, v187, v227\n"
    "s_waitcnt lgkmcnt(5)\n"
    "v_mfma_f32_32x32x16_bf16 v[160:175], v[204:207], v[124:127], v[160:175]\n"
    "ds_read_b64_tr_b16 v[200:201], v1 offset:35840\n"
    "ds_read_b64_tr_b16 v[202:203], v1 offset:36352\n"
    "ds_read_b128 v[212:215], v2 offset:20512\n"
    "v_add_f32 v224, 1.0, v224\n"
    "v_mul_f32 v225, v189, v225\n"
    "v_rcp_f32 v224, v224\n"
    "v_cvt_pk_bf16_f32 v237, v186, v187\n"
    "s_waitcnt lgkmcnt(6)\n"
    "v_mfma_f32_32x32x16_bf16 v[160:175], v[192:195], v[128:131], v[160:175]\n"
    "ds_read_b64_tr_b16 v[204:205], v1 offset:52224\n"
    "ds_read_b64_tr_b16 v[206:207], v1 offset:52736\n"
    "ds_read_b128 v[216:219], v2 offset:20544\n"
    "v_exp_f32 v225, v225\n"
    "v_mul_f32 v226, v190, v190\n"
    "v_fmaak_f32 v226, v13, v226, 0xc0135761\n"
    "v_mul_f32 v188, v188, v224\n"
    "s_waitcnt lgkmcnt(7)\n"
    "v_mfma_f32_32x32x16_bf16 v[160:175], v[196:199], v[132:135], v[160:175]\n"
    "s_waitcnt vmcnt(2)\n"
    "s_barrier\n"
    "s_waitcnt lgkmcnt(4)\n"
    "v_mfma_f32_32x32x16_bf16 v[160:175], v[200:203], v[136:139], v[160:175]\n"
    "ds_read_b64_tr_b16 v[192:193], v1 offset:4096\n"
    "ds_read_b64_tr_b16 v[194:195], v1 offset:4608\n"
    "ds_read_b128 v[220:223], v2 offset:20576\n"
    "ds_read_b64_tr_b16 v[196:197], v1 offset:20480\n"
    "ds_read_b64_tr_b16 v[198:199], v1 offset:20992\n"
    "v_add_f32 v225, 1.0, v225\n"
    "v_mul_f32 v226, v190, v226\n"
    "v_rcp_f32 v225, v225\n"
    "v_exp_f32 v226, v226\n"
    "v_mul_f32 v227, v191, v191\n"
    "v_fmaak_f32 v227, v13, v227, 0xc0135761\n"
    "v_mul_f32 v189, v189, v225\n"
    "s_waitcnt lgkmcnt(6)\n"
    "v_mfma_f32_32x32x16_bf16 v[160:175], v[204:207], v[140:143], v[160:175]\n"
    "ds_read_b64_tr_b16 v[200:201], v1 offset:36864\n"
    "ds_read_b64_tr_b16 v[202:203], v1 offset:37376\n"
    "v_add_f32 v226, 1.0, v226\n"
    "v_mul_f32 v227, v191, v227\n"
    "v_rcp_f32 v226, v226\n"
    "v_cvt_pk_bf16_f32 v238, v188, v189\n"
    "s_waitcnt lgkmcnt(5)\n"
    "v_mfma_f32_32x32x16_bf16 v[160:175], v[192:195], v[144:147], v[160:175]\n"
    "ds_read_b64_tr_b16 v[204:205], v1 offset:53248\n"
    "ds_read_b64_tr_b16 v[206:207], v1 offset:53760\n"
    "v_exp_f32 v227, v227\n"
    "v_mul_f32 v190, v190, v226\n"
    "s_waitcnt lgkmcnt(4)\n"
    "v_mfma_f32_32x32x16_bf16 v[160:175], v[196:199], v[148:151], v[160:175]\n"
    "ds_read_b64_tr_b16 v[192:193], v1 offset:5120\n"
    "ds_read_b64_tr_b16 v[194:195], v1 offset:5632\n"
    "v_add_f32 v227, 1.0, v227\n"
    "v_rcp_f32 v227, v227\n"
    "s_waitcnt lgkmcnt(4)\n"
    "v_mfma_f32_32x32x16_bf16 v[160:175], v[200:203], v[152:155], v[160:175]\n"
    "ds_read_b64_tr_b16 v[196:197], v1 offset:21504\n"
    "ds_read_b64_tr_b16 v[198:199], v1 offset:22016\n"
    "s_add_u32 m0, s42, 0\n"
    "s_mov_b32 s43, 1774208\n"
    "buffer_load_dwordx4 v5, s[12:15], s43 offen offset:0 lds\n"
    "v_mul_f32 v191, v191, v227\n"
    "v_cvt_pk_bf16_f32 v239, v190, v191\n"
    "s_waitcnt lgkmcnt(4)\n"
    "v_mfma_f32_32x32x16_bf16 v[160:175], v[204:207], v[156:159], v[160:175]\n"
    "ds_read_b64_tr_b16 v[200:201], v1 offset:37888\n"
    "ds_read_b64_tr_b16 v[202:203], v1 offset:38400\n"
    "s_mov_b32 s43, 2363008\n"
    "buffer_load_dwordx4 v5, s[12:15], s43 offen offset:1024 lds\n"
    "ds_write_b128 v7, v[236:239] offset:12544\n"
    "s_waitcnt lgkmcnt(5)\n"
    "v_mfma_f32_32x32x16_bf16 v[176:191], v[192:195], v[16:19], v[208:223]\n"
    "ds_read_b64_tr_b16 v[204:205], v1 offset:54272\n"
    "ds_read_b64_tr_b16 v[206:207], v1 offset:54784\n"
    "s_waitcnt lgkmcnt(5)\n"
    "v_mfma_f32_32x32x16_bf16 v[176:191], v[196:199], v[20:23], v[176:191]\n"
    "s_waitcnt vmcnt(2)\n"
    "s_barrier\n"
    "s_waitcnt lgkmcnt(3)\n"
    "v_mfma_f32_32x32x16_bf16 v[176:191], v[200:203], v[24:27], v[176:191]\n"
    "ds_read_b64_tr_b16 v[192:193], v1 offset:6144\n"
    "ds_read_b64_tr_b16 v[194:195], v1 offset:6656\n"
    "ds_read_b64_tr_b16 v[196:197], v1 offset:22528\n"
    "ds_read_b64_tr_b16 v[198:199], v1 offset:23040\n"
    "s_mov_b32 s43, 2951808\n"
    "buffer_load_dwordx4 v5, s[12:15], s43 offen offset:2048 lds\n"
    "v_mul_f32 v224, v160, v160\n"
    "v_fmaak_f32 v224, v13, v224, 0xc0135761\n"
    "v_mul_f32 v224, v160, v224\n"
    "s_mov_b32 s43, 3540608\n"
    "buffer_load_dwordx4 v5, s[12:15], s43 offen offset:3072 lds\n"
    "v_exp_f32 v224, v224\n"
    "v_mul_f32 v225, v161, v161\n"
    "v_fmaak_f32 v225, v13, v225, 0xc0135761\n"
    "s_waitcnt lgkmcnt(4)\n"
    "v_mfma_f32_32x32x16_bf16 v[176:191], v[204:207], v[28:31], v[176:191]\n"
    "ds_read_b64_tr_b16 v[200:201], v1 offset:38912\n"
    "ds_read_b64_tr_b16 v[202:203], v1 offset:39424\n"
    "v_add_f32 v224, 1.0, v224\n"
    "v_mul_f32 v225, v161, v225\n"
    "v_rcp_f32 v224, v224\n"
    "s_waitcnt lgkmcnt(4)\n"
    "v_mfma_f32_32x32x16_bf16 v[176:191], v[192:195], v[32:35], v[176:191]\n"
    "ds_read_b64_tr_b16 v[204:205], v1 offset:55296\n"
    "ds_read_b64_tr_b16 v[206:207], v1 offset:55808\n"
    "v_exp_f32 v225, v225\n"
    "v_mul_f32 v226, v162, v162\n"
    "v_fmaak_f32 v226, v13, v226, 0xc0135761\n"
    "v_mul_f32 v160, v160, v224\n"
    "s_waitcnt lgkmcnt(4)\n"
    "v_mfma_f32_32x32x16_bf16 v[176:191], v[196:199], v[36:39], v[176:191]\n"
    "ds_read_b64_tr_b16 v[192:193], v1 offset:7168\n"
    "ds_read_b64_tr_b16 v[194:195], v1 offset:7680\n"
    "v_add_f32 v225, 1.0, v225\n"
    "v_mul_f32 v226, v162, v226\n"
    "v_rcp_f32 v225, v225\n"
    "s_waitcnt lgkmcnt(4)\n"
    "v_mfma_f32_32x32x16_bf16 v[176:191], v[200:203], v[40:43], v[176:191]\n"
    "ds_read_b64_tr_b16 v[196:197], v1 offset:23552\n"
    "ds_read_b64_tr_b16 v[198:199], v1 offset:24064\n"
    "v_exp_f32 v226, v226\n"
    "v_mul_f32 v227, v163, v163\n"
    "v_fmaak_f32 v227, v13, v227, 0xc0135761\n"
    "v_mul_f32 v161, v161, v225\n"
    "s_waitcnt lgkmcnt(4)\n"
    "v_mfma_f32_32x32x16_bf16 v[176:191], v[204:207], v[44:47], v[176:191]\n"
    "ds_read_b64_tr_b16 v[200:201], v1 offset:39936\n"
    "ds_read_b64_tr_b16 v[202:203], v1 offset:40448\n"
    "v_add_f32 v226, 1.0, v226\n"
    "v_mul_f32 v227, v163, v227\n"
    "v_rcp_f32 v226, v226\n"
    "v_cvt_pk_bf16_f32 v232, v160, v161\n"
    "s_waitcnt lgkmcnt(4)\n"
    "v_mfma_f32_32x32x16_bf16 v[176:191], v[192:195], v[48:51], v[176:191]\n"
    "ds_read_b64_tr_b16 v[204:205], v1 offset:56320\n"
    "ds_read_b64_tr_b16 v[206:207], v1 offset:56832\n"
    "v_exp_f32 v227, v227\n"
    "v_mul_f32 v224, v164, v164\n"
    "v_fmaak_f32 v224, v13, v224, 0xc0135761\n"
    "v_mul_f32 v162, v162, v226\n"
    "v_add_f32 v227, 1.0, v227\n"
    "v_mul_f32 v224, v164, v224\n"
    "v_rcp_f32 v227, v227\n"
    "s_waitcnt lgkmcnt(4)\n"
    "v_mfma_f32_32x32x16_bf16 v[176:191], v[196:199], v[52:55], v[176:191]\n"
    "s_waitcnt vmcnt(2)\n"
    "s_barrier\n"
    "s_waitcnt lgkmcnt(2)\n"
    "v_mfma_f32_32x32x16_bf16 v[176:191], v[200:203], v[56:59], v[176:191]\n"
    "ds_read_b64_tr_b16 v[192:193], v1 offset:0\n"
    "ds_read_b64_tr_b16 v[194:195], v1 offset:512\n"
    "ds_read_b64_tr_b16 v[196:197], v1 offset:16384\n"
    "ds_read_b64_tr_b16 v[198:199], v1 offset:16896\n"
    "s_add_u32 m0, s42, 4096\n"
    "s_mov_b32 s43, 4133504\n"
    "buffer_load_dwordx4 v5, s[12:15], s43 offen offset:0 lds\n"
    "v_exp_f32 v224, v224\n"
    "v_mul_f32 v225, v165, v165\n"
    "v_fmaak_f32 v225, v13, v225, 0xc0135761\n"
    "v_mul_f32 v163, v163, v227\n"
    "s_mov_b32 s43, 4722304\n"
    "buffer_load_dwordx4 v5, s[12:15], s43 offen offset:1024 lds\n"
    "s_waitcnt lgkmcnt(4)\n"
    "v_mfma_f32_32x32x16_bf16 v[176:191], v[204:207], v[60:63], v[176:191]\n"
    "ds_read_b64_tr_b16 v[200:201], v1 offset:32768\n"
    "ds_read_b64_tr_b16 v[202:203], v1 offset:33280\n"
    "v_add_f32 v224, 1.0, v224\n"
    "v_mul_f32 v225, v165, v225\n"
    "v_rcp_f32 v224, v224\n"
    "v_cvt_pk_bf16_f32 v233, v162, v163\n"
    "s_waitcnt lgkmcnt(4)\n"
    "v_mfma_f32_32x32x16_bf16 v[176:191], v[192:195], v[64:67], v[176:191]\n"
    "ds_read_b64_tr_b16 v[204:205], v1 offset:49152\n"
    "ds_read_b64_tr_b16 v[206:207], v1 offset:49664\n"
    "v_exp_f32 v225, v225\n"
    "v_mul_f32 v226, v166, v166\n"
    "v_fmaak_f32 v226, v13, v226, 0xc0135761\n"
    "v_mul_f32 v164, v164, v224\n"
    "s_waitcnt lgkmcnt(4)\n"
    "v_mfma_f32_32x32x16_bf16 v[176:191], v[196:199], v[68:71], v[176:191]\n"
    "ds_read_b64_tr_b16 v[192:193], v1 offset:1024\n"
    "ds_read_b64_tr_b16 v[194:195], v1 offset:1536\n"
    "v_add_f32 v225, 1.0, v225\n"
    "v_mul_f32 v226, v166, v226\n"
    "v_rcp_f32 v225, v225\n"
    "s_waitcnt lgkmcnt(4)\n"
    "v_mfma_f32_32x32x16_bf16 v[176:191], v[200:203], v[72:75], v[176:191]\n"
    "ds_read_b64_tr_b16 v[196:197], v1 offset:17408\n"
    "ds_read_b64_tr_b16 v[198:199], v1 offset:17920\n"
    "v_exp_f32 v226, v226\n"
    "v_mul_f32 v227, v167, v167\n"
    "v_fmaak_f32 v227, v13, v227, 0xc0135761\n"
    "v_mul_f32 v165, v165, v225\n"
    "s_waitcnt lgkmcnt(4)\n"
    "v_mfma_f32_32x32x16_bf16 v[176:191], v[204:207], v[76:79], v[176:191]\n"
    "ds_read_b64_tr_b16 v[200:201], v1 offset:33792\n"
    "ds_read_b64_tr_b16 v[202:203], v1 offset:34304\n"
    "v_add_f32 v226, 1.0, v226\n"
    "v_mul_f32 v227, v167, v227\n"
    "v_rcp_f32 v226, v226\n"
    "v_cvt_pk_bf16_f32 v234, v164, v165\n"
    "s_waitcnt lgkmcnt(4)\n"
    "v_mfma_f32_32x32x16_bf16 v[176:191], v[192:195], v[80:83], v[176:191]\n"
    "ds_read_b64_tr_b16 v[204:205], v1 offset:50176\n"
    "ds_read_b64_tr_b16 v[206:207], v1 offset:50688\n"
    "v_exp_f32 v227, v227\n"
    "v_mul_f32 v224, v168, v168\n"
    "v_fmaak_f32 v224, v13, v224, 0xc0135761\n"
    "v_mul_f32 v166, v166, v226\n"
    "s_waitcnt lgkmcnt(4)\n"
    "v_mfma_f32_32x32x16_bf16 v[176:191], v[196:199], v[84:87], v[176:191]\n"
    "s_waitcnt vmcnt(2)\n"
    "s_barrier\n"
    "s_waitcnt lgkmcnt(2)\n"
    "v_mfma_f32_32x32x16_bf16 v[176:191], v[200:203], v[88:91], v[176:191]\n"
    "ds_read_b64_tr_b16 v[192:193], v1 offset:2048\n"
    "ds_read_b64_tr_b16 v[194:195], v1 offset:2560\n"
    "ds_read_b64_tr_b16 v[196:197], v1 offset:18432\n"
    "ds_read_b64_tr_b16 v[198:199], v1 offset:18944\n"
    "s_mov_b32 s43, 2048\n"
    "buffer_load_dwordx4 v6, s[16:19], s43 offen offset:2048 lds\n"
    "v_add_f32 v227, 1.0, v227\n"
    "v_mul_f32 v224, v168, v224\n"
    "v_rcp_f32 v227, v227\n"
    "s_mov_b32 s43, 74752\n"
    "buffer_load_dwordx4 v6, s[16:19], s43 offen offset:3072 lds\n"
    "v_exp_f32 v224, v224\n"
    "v_mul_f32 v225, v169, v169\n"
    "v_fmaak_f32 v225, v13, v225, 0xc0135761\n"
    "v_mul_f32 v167, v167, v227\n"
    "s_waitcnt lgkmcnt(4)\n"
    "v_mfma_f32_32x32x16_bf16 v[176:191], v[204:207], v[92:95], v[176:191]\n"
    "ds_read_b64_tr_b16 v[200:201], v1 offset:34816\n"
    "ds_read_b64_tr_b16 v[202:203], v1 offset:35328\n"
    "v_add_f32 v224, 1.0, v224\n"
    "v_mul_f32 v225, v169, v225\n"
    "v_rcp_f32 v224, v224\n"
    "v_cvt_pk_bf16_f32 v235, v166, v167\n"
    "s_waitcnt lgkmcnt(4)\n"
    "v_mfma_f32_32x32x16_bf16 v[176:191], v[192:195], v[96:99], v[176:191]\n"
    "ds_read_b64_tr_b16 v[204:205], v1 offset:51200\n"
    "ds_read_b64_tr_b16 v[206:207], v1 offset:51712\n"
    "v_exp_f32 v225, v225\n"
    "v_mul_f32 v226, v170, v170\n"
    "v_fmaak_f32 v226, v13, v226, 0xc0135761\n"
    "v_mul_f32 v168, v168, v224\n"
    "s_waitcnt lgkmcnt(4)\n"
    "v_mfma_f32_32x32x16_bf16 v[176:191], v[196:199], v[100:103], v[176:191]\n"
    "ds_read_b64_tr_b16 v[192:193], v1 offset:3072\n"
    "ds_read_b64_tr_b16 v[194:195], v1 offset:3584\n"
    "v_add_f32 v225, 1.0, v225\n"
    "v_mul_f32 v226, v170, v226\n"
    "v_rcp_f32 v225, v225\n"
    "ds_write_b128 v7, v[232:235] offset:16384\n"
    "s_waitcnt lgkmcnt(5)\n"
    "v_mfma_f32_32x32x16_bf16 v[176:191], v[200:203], v[104:107], v[176:191]\n"
    "ds_read_b64_tr_b16 v[196:197], v1 offset:19456\n"
    "ds_read_b64_tr_b16 v[198:199], v1 offset:19968\n"
    "v_exp_f32 v226, v226\n"
    "v_mul_f32 v227, v171, v171\n"
    "v_fmaak_f32 v227, v13, v227, 0xc0135761\n"
    "v_mul_f32 v169, v169, v225\n"
    "s_waitcnt lgkmcnt(5)\n"
    "v_mfma_f32_32x32x16_bf16 v[176:191], v[204:207], v[108:111], v[176:191]\n"
    "ds_read_b64_tr_b16 v[200:201], v1 offset:35840\n"
    "ds_read_b64_tr_b16 v[202:203], v1 offset:36352\n"
    "v_add_f32 v226, 1.0, v226\n"
    "v_mul_f32 v227, v171, v227\n"
    "v_rcp_f32 v226, v226\n"
    "v_cvt_pk_bf16_f32 v236, v168, v169\n"
    "s_waitcnt lgkmcnt(5)\n"
    "v_mfma_f32_32x32x16_bf16 v[176:191], v[192:195], v[112:115], v[176:191]\n"
    "ds_read_b64_tr_b16 v[204:205], v1 offset:52224\n"
    "ds_read_b64_tr_b16 v[206:207], v1 offset:52736\n"
    "v_exp_f32 v227, v227\n"
    "v_mul_f32 v224, v172, v172\n"
    "v_fmaak_f32 v224, v13, v224, 0xc0135761\n"
    "v_mul_f32 v170, v170, v226\n"
    "s_waitcnt lgkmcnt(4)\n"
    "v_mfma_f32_32x32x16_bf16 v[176:191], v[196:199], v[116:119], v[176:191]\n"
    "s_waitcnt vmcnt(2)\n"
    "s_barrier\n"
    "s_waitcnt lgkmcnt(2)\n"
    "v_mfma_f32_32x32x16_bf16 v[176:191], v[200:203], v[120:123], v[176:191]\n"
    "ds_read_b64_tr_b16 v[192:193], v1 offset:4096\n"
    "ds_read_b64_tr_b16 v[194:195], v1 offset:4608\n"
    "ds_read_b64_tr_b16 v[196:197], v1 offset:20480\n"
    "ds_read_b64_tr_b16 v[198:199], v1 offset:20992\n"
    "v_add_f32 v227, 1.0, v227\n"
    "v_mul_f32 v224, v172, v224\n"
    "v_rcp_f32 v227, v227\n"
    "s_add_u32 m0, s42, 0\n"
    "s_mov_b32 s43, 151552\n"
    "buffer_load_dwordx4 v6, s[16:19], s43 offen offset:0 lds\n"
    "v_exp_f32 v224, v224\n"
    "v_mul_f32 v225, v173, v173\n"
    "v_fmaak_f32 v225, v13, v225, 0xc0135761\n"
    "v_mul_f32 v171, v171, v227\n"
    "s_waitcnt lgkmcnt(4)\n"
    "v_mfma_f32_32x32x16_bf16 v[176:191], v[204:207], v[124:127], v[176:191]\n"
    "ds_read_b64_tr_b16 v[200:201], v1 offset:36864\n"
    "ds_read_b64_tr_b16 v[202:203], v1 offset:37376\n"
    "v_add_f32 v224, 1.0, v224\n"
    "v_mul_f32 v225, v173, v225\n"
    "v_rcp_f32 v224, v224\n"
    "v_cvt_pk_bf16_f32 v237, v170, v171\n"
    "s_waitcnt lgkmcnt(4)\n"
    "v_mfma_f32_32x32x16_bf16 v[176:191], v[192:195], v[128:131], v[176:191]\n"
    "ds_read_b64_tr_b16 v[204:205], v1 offset:53248\n"
    "ds_read_b64_tr_b16 v[206:207], v1 offset:53760\n"
    "v_exp_f32 v225, v225\n"
    "v_mul_f32 v226, v174, v174\n"
    "v_fmaak_f32 v226, v13, v226, 0xc0135761\n"
    "v_mul_f32 v172, v172, v224\n"
    "s_waitcnt lgkmcnt(4)\n"
    "v_mfma_f32_32x32x16_bf16 v[176:191], v[196:199], v[132:135], v[176:191]\n"
    "ds_read_b64_tr_b16 v[192:193], v1 offset:5120\n"
    "ds_read_b64_tr_b16 v[194:195], v1 offset:5632\n"
    "v_add_f32 v225, 1.0, v225\n"
    "v_mul_f32 v226, v174, v226\n"
    "v_rcp_f32 v225, v225\n"
    "s_waitcnt lgkmcnt(4)\n"
    "v_mfma_f32_32x32x16_bf16 v[176:191], v[200:203], v[136:139], v[176:191]\n"
    "ds_read_b64_tr_b16 v[196:197], v1 offset:21504\n"
    "ds_read_b64_tr_b16 v[198:199], v1 offset:22016\n"
    "v_exp_f32 v226, v226\n"
    "v_mul_f32 v227, v175, v175\n"
    "v_fmaak_f32 v227, v13, v227, 0xc0135761\n"
    "v_mul_f32 v173, v173, v225\n"
    "s_waitcnt lgkmcnt(4)\n"
    "v_mfma_f32_32x32x16_bf16 v[176:191], v[204:207], v[140:143], v[176:191]\n"
    "ds_read_b64_tr_b16 v[200:201], v1 offset:37888\n"
    "ds_read_b64_tr_b16 v[202:203], v1 offset:38400\n"
    "v_add_f32 v226, 1.0, v226\n"
    "v_mul_f32 v227, v175, v227\n"
    "v_rcp_f32 v226, v226\n"
    "v_cvt_pk_bf16_f32 v238, v172, v173\n"
    "s_waitcnt lgkmcnt(4)\n"
    "v_mfma_f32_32x32x16_bf16 v[176:191], v[192:195], v[144:147], v[176:191]\n"
    "ds_read_b64_tr_b16 v[204:205], v1 offset:54272\n"
    "ds_read_b64_tr_b16 v[206:207], v1 offset:54784\n"
    "v_exp_f32 v227, v227\n"
    "v_mul_f32 v174, v174, v226\n"
    "s_waitcnt lgkmcnt(4)\n"
    "v_mfma_f32_32x32x16_bf16 v[176:191], v[196:199], v[148:151], v[176:191]\n"
    "s_mov_b32 s43, 224256\n"
    "buffer_load_dwordx4 v6, s[16:19], s43 offen offset:1024 lds\n"
    "v_add_f32 v227, 1.0, v227\n"
    "v_rcp_f32 v227, v227\n"
    "s_nop 0\n"
    "v_mul_f32 v175, v175, v227\n"
    "v_cvt_pk_bf16_f32 v239, v174, v175\n"
    "ds_write_b128 v7, v[236:239] offset:16640\n"
    "s_waitcnt lgkmcnt(3)\n"
    "v_mfma_f32_32x32x16_bf16 v[176:191], v[200:203], v[152:155], v[176:191]\n"
    "s_waitcnt vmcnt(2) lgkmcnt(0)\n"
    "s_barrier\n"
    "v_mfma_f32_32x32x16_bf16 v[176:191], v[204:207], v[156:159], v[176:191]\n"
    "ds_read_b64_tr_b16 v[240:241], v3 offset:6144\n"
    "ds_read_b64_tr_b16 v[242:243], v3 offset:14336\n"
    "ds_read_b64_tr_b16 v[244:245], v3 offset:38912\n"
    "ds_read_b64_tr_b16 v[246:247], v3 offset:47104\n"
    "ds_read_b128 v[48:51], v9 offset:4096\n"
    "ds_read_b128 v[52:55], v9 offset:5120\n"
    "ds_read_b128 v[56:59], v9 offset:28672\n"
    "ds_read_b128 v[60:63], v9 offset:29696\n"
    "ds_read_b128 v[16:19], v9 offset:0\n"
    "ds_read_b128 v[20:23], v9 offset:1024\n"
    "ds_read_b128 v[24:27], v9 offset:24576\n"
    "s_waitcnt lgkmcnt(9)\n"
    "s_waitcnt lgkmcnt(2)\n"
    "v_mfma_f32_16x16x32_bf16 v[208:211], v[240:243], v[16:19], 0\n"
    "ds_read_b64_tr_b16 v[248:249], v3 offset:7168\n"
    "ds_read_b128 v[64:67], v9 offset:6144\n"
    "ds_read_b128 v[68:71], v9 offset:7168\n"
    "ds_read_b128 v[72:75], v9 offset:30720\n"
    "s_waitcnt lgkmcnt(5)\n"
    "v_mfma_f32_16x16x32_bf16 v[212:215], v[240:243], v[20:23], 0\n"
    "ds_read_b64_tr_b16 v[250:251], v3 offset:15360\n"
    "ds_read_b128 v[76:79], v9 offset:31744\n"
    "ds_read_b128 v[28:31], v9 offset:25600\n"
    "ds_read_b128 v[32:35], v9 offset:2048\n"
    "s_waitcnt lgkmcnt(8)\n"
    "v_mfma_f32_16x16x32_bf16 v[216:219], v[240:243], v[24:27], 0\n"
    "ds_read_b128 v[36:39], v9 offset:3072\n"
    "ds_read_b128 v[40:43], v9 offset:26624\n"
    "ds_read_b128 v[44:47], v9 offset:27648\n"
    "s_waitcnt lgkmcnt(4)\n"
    "v_mfma_f32_16x16x32_bf16 v[220:223], v[240:243], v[28:31], 0\n"
    "s_mov_b32 s43, 296960\n"
    "buffer_load_dwordx4 v6, s[16:19], s43 offen offset:2048 lds\n"
    "s_waitcnt lgkmcnt(3)\n"
    "v_mfma_f32_16x16x32_bf16 v[208:211], v[244:247], v[32:35], v[208:211]\n"
    "ds_read_b64_tr_b16 v[240:241], v3 offset:39936\n"
    "ds_read_b128 v[80:83], v9 offset:8192\n"
    "ds_read_b128 v[84:87], v9 offset:9216\n"
    "ds_read_b128 v[88:91], v9 offset:32768\n"
    "ds_read_b128 v[92:95], v9 offset:33792\n"
    "s_waitcnt lgkmcnt(7)\n"
    "v_mfma_f32_16x16x32_bf16 v[212:215], v[244:247], v[36:39], v[212:215]\n"
    "ds_read_b64_tr_b16 v[242:243], v3 offset:48128\n"
    "s_mov_b32 s43, 369664\n"
    "buffer_load_dwordx4 v6, s[16:19], s43 offen offset:3072 lds\n"
    "s_waitcnt lgkmcnt(7)\n"
    "v_mfma_f32_16x16x32_bf16 v[216:219], v[244:247], v[40:43], v[216:219]\n"
    "v_mul_f32 v224, v176, v176\n"
    "v_fmaak_f32 v224, v13, v224, 0xc0135761\n"
    "v_mul_f32 v224, v176, v224\n"
    "s_waitcnt lgkmcnt(6)\n"
    "v_mfma_f32_16x16x32_bf16 v[220:223], v[244:247], v[44:47], v[220:223]\n"
    "v_exp_f32 v224, v224\n"
    "v_mul_f32 v225, v177, v177\n"
    "v_fmaak_f32 v225, v13, v225, 0xc0135761\n"
    "v_mfma_f32_16x16x32_bf16 v[208:211], v[248:251], v[48:51], v[208:211]\n"
    "s_waitcnt vmcnt(2)\n"
    "s_barrier\n"
    "v_mfma_f32_16x16x32_bf16 v[212:215], v[248:251], v[52:55], v[212:215]\n"
    "ds_read_b64_tr_b16 v[244:245], v3 offset:0\n"
    "ds_read_b128 v[96:99], v9 offset:10240\n"
    "ds_read_b128 v[100:103], v9 offset:11264\n"
    "ds_read_b128 v[104:107], v9 offset:34816\n"
    "ds_read_b128 v[108:111], v9 offset:35840\n"
    "s_add_u32 m0, s42, 4096\n"
    "s_mov_b32 s43, 4224\n"
    "buffer_load_dwordx4 v6, s[16:19], s43 offen offset:0 lds\n"
    "v_mfma_f32_16x16x32_bf16 v[216:219], v[248:251], v[56:59], v[216:219]\n"
    "ds_read_b64_tr_b16 v[246:247], v3 offset:8192\n"
    "s_mov_b32 s43, 76928\n"
    "buffer_load_dwordx4 v6, s[16:19], s43 offen offset:1024 lds\n"
    "v_add_f32 v224, 1.0, v224\n"
    "v_mul_f32 v225, v177, v225\n"
    "v_rcp_f32 v224, v224\n"
    "v_mfma_f32_16x16x32_bf16 v[220:223], v[248:251], v[60:63], v[220:223]\n"
    "v_exp_f32 v225, v225\n"
    "v_mul_f32 v226, v178, v178\n"
    "v_fmaak_f32 v226, v13, v226, 0xc0135761\n"
    "v_mul_f32 v176, v176, v224\n"
    "s_waitcnt lgkmcnt(6)\n"
    "v_mfma_f32_16x16x32_bf16 v[208:211], v[240:243], v[64:67], v[208:211]\n"
    "ds_read_b64_tr_b16 v[248:249], v3 offset:32768\n"
    "ds_read_b128 v[112:115], v9 offset:12288\n"
    "ds_read_b128 v[116:119], v9 offset:13312\n"
    "ds_read_b128 v[120:123], v9 offset:36864\n"
    "ds_read_b128 v[124:127], v9 offset:37888\n"
    "v_add_f32 v225, 1.0, v225\n"
    "v_mul_f32 v226, v178, v226\n"
    "v_rcp_f32 v225, v225\n"
    "v_mfma_f32_16x16x32_bf16 v[212:215], v[240:243], v[68:71], v[212:215]\n"
    "ds_read_b64_tr_b16 v[250:251], v3 offset:40960\n"
    "v_exp_f32 v226, v226\n"
    "v_mul_f32 v227, v179, v179\n"
    "v_fmaak_f32 v227, v13, v227, 0xc0135761\n"
    "v_mul_f32 v177, v177, v225\n"
    "v_mfma_f32_16x16x32_bf16 v[216:219], v[240:243], v[72:75], v[216:219]\n"
    "v_add_f32 v226, 1.0, v226\n"
    "v_mul_f32 v227, v179, v227\n"
    "v_rcp_f32 v226, v226\n"
    "v_cvt_pk_bf16_f32 v232, v176, v177\n"
    "v_mfma_f32_16x16x32_bf16 v[220:223], v[240:243], v[76:79], v[220:223]\n"
    "v_exp_f32 v227, v227\n"
    "v_mul_f32 v224, v180, v180\n"
    "v_fmaak_f32 v224, v13, v224, 0xc0135761\n"
    "v_mul_f32 v178, v178, v226\n"
    "v_add_f32 v227, 1.0, v227\n"
    "v_mul_f32 v224, v180, v224\n"
    "v_rcp_f32 v227, v227\n"
    "s_waitcnt lgkmcnt(6)\n"
    "v_mfma_f32_16x16x32_bf16 v[208:211], v[244:247], v[80:83], v[208:211]\n"
    "ds_read_b64_tr_b16 v[240:241], v3 offset:1024\n"
    "ds_read_b128 v[128:131], v9 offset:14336\n"
    "ds_read_b128 v[132:135], v9 offset:15360\n"
    "ds_read_b128 v[136:139], v9 offset:38912\n"
    "ds_read_b128 v[140:143], v9 offset:39936\n"
    "v_mfma_f32_16x16x32_bf16 v[212:215], v[244:247], v[84:87], v[212:215]\n"
    "ds_read_b64_tr_b16 v[242:243], v3 offset:9216\n"
    "v_exp_f32 v224, v224\n"
    "v_mul_f32 v225, v181, v181\n"
    "v_fmaak_f32 v225, v13, v225, 0xc0135761\n"
    "v_mul_f32 v179, v179, v227\n"
    "v_mfma_f32_16x16x32_bf16 v[216:219], v[244:247], v[88:91], v[216:219]\n"
    "v_add_f32 v224, 1.0, v224\n"
    "v_mul_f32 v225, v181, v225\n"
    "v_rcp_f32 v224, v224\n"
    "v_cvt_pk_bf16_f32 v233, v178, v179\n"
    "v_mfma_f32_16x16x32_bf16 v[220:223], v[244:247], v[92:95], v[220:223]\n"
    "v_exp_f32 v225, v225\n"
    "v_mul_f32 v226, v182, v182\n"
    "v_fmaak_f32 v226, v13, v226, 0xc0135761\n"
    "v_mul_f32 v180, v180, v224\n"
    "v_add_f32 v225, 1.0, v225\n"
    "v_mul_f32 v226, v182, v226\n"
    "v_rcp_f32 v225, v225\n"
    "s_waitcnt lgkmcnt(6)\n"
    "v_mfma_f32_16x16x32_bf16 v[208:211], v[248:251], v[96:99], v[208:211]\n"
    "ds_read_b64_tr_b16 v[244:245], v3 offset:33792\n"
    "ds_read_b128 v[144:147], v9 offset:16384\n"
    "ds_read_b128 v[148:151], v9 offset:17408\n"
    "ds_read_b128 v[152:155], v9 offset:40960\n"
    "ds_read_b128 v[156:159], v9 offset:41984\n"
    "v_mfma_f32_16x16x32_bf16 v[212:215], v[248:251], v[100:103], v[212:215]\n"
    "ds_read_b64_tr_b16 v[246:247], v3 offset:41984\n"
    "v_exp_f32 v226, v226\n"
    "v_mul_f32 v227, v183, v183\n"
    "v_fmaak_f32 v227, v13, v227, 0xc0135761\n"
    "v_mul_f32 v181, v181, v225\n"
    "v_mfma_f32_16x16x32_bf16 v[216:219], v[248:251], v[104:107], v[216:219]\n"
    "v_add_f32 v226, 1.0, v226\n"
    "v_mul_f32 v227, v183, v227\n"
    "v_rcp_f32 v226, v226\n"
    "v_cvt_pk_bf16_f32 v234, v180, v181\n"
    "v_exp_f32 v227, v227\n"
    "v_mul_f32 v224, v184, v184\n"
    "v_fmaak_f32 v224, v13, v224, 0xc0135761\n"
    "v_mul_f32 v182, v182, v226\n"
    "v_mfma_f32_16x16x32_bf16 v[220:223], v[248:251], v[108:111], v[220:223]\n"
    "v_add_f32 v227, 1.0, v227\n"
    "v_mul_f32 v224, v184, v224\n"
    "v_rcp_f32 v227, v227\n"
    "s_waitcnt lgkmcnt(6)\n"
    "v_mfma_f32_16x16x32_bf16 v[208:211], v[240:243], v[112:115], v[208:211]\n"
    "s_waitcnt vmcnt(2)\n"
    "s_barrier\n"
    "v_mfma_f32_16x16x32_bf16 v[212:215], v[240:243], v[116:119], v[212:215]\n"
    "ds_read_b64_tr_b16 v[248:249], v3 offset:2048\n"
    "ds_read_b128 v[160:163], v9 offset:18432\n"
    "ds_read_b128 v[164:167], v9 offset:19456\n"
    "ds_read_b128 v[168:171], v9 offset:43008\n"
    "ds_read_b128 v[172:175], v9 offset:44032\n"
    "s_mov_b32 s43, 149632\n"
    "buffer_load_dwordx4 v6, s[16:19], s43 offen offset:2048 lds\n"
    "v_mfma_f32_16x16x32_bf16 v[216:219], v[240:243], v[120:123], v[216:219]\n"
    "ds_read_b64_tr_b16 v[250:251], v3 offset:10240\n"
    "s_mov_b32 s43, 222336\n"
    "buffer_load_dwordx4 v6, s[16:19], s43 offen offset:3072 lds\n"
    "v_exp_f32 v224, v224\n"
    "v_mul_f32 v225, v185, v185\n"
    "v_fmaak_f32 v225, v13, v225, 0xc0135761\n"
    "v_mul_f32 v183, v183, v227\n"
    "v_mfma_f32_16x16x32_bf16 v[220:223], v[240:243], v[124:127], v[220:223]\n"
    "v_add_f32 v224, 1.0, v224\n"
    "v_mul_f32 v225, v185, v225\n"
    "v_rcp_f32 v224, v224\n"
    "v_cvt_pk_bf16_f32 v235, v182, v183\n"
    "s_waitcnt lgkmcnt(6)\n"
    "v_mfma_f32_16x16x32_bf16 v[208:211], v[244:247], v[128:131], v[208:211]\n"
    "ds_read_b64_tr_b16 v[240:241], v3 offset:34816\n"
    "v_exp_f32 v225, v225\n"
    "v_mul_f32 v226, v186, v186\n"
    "v_fmaak_f32 v226, v13, v226, 0xc0135761\n"
    "v_mul_f32 v184, v184, v224\n"
    "v_mfma_f32_16x16x32_bf16 v[212:215], v[244:247], v[132:135], v[212:215]\n"
    "ds_read_b64_tr_b16 v[242:243], v3 offset:43008\n"
    "v_add_f32 v225, 1.0, v225\n"
    "v_mul_f32 v226, v186, v226\n"
    "v_rcp_f32 v225, v225\n"
    "ds_write_b128 v7, v[232:235] offset:20480\n"
    "v_mfma_f32_16x16x32_bf16 v[216:219], v[244:247], v[136:139], v[216:219]\n"
    "v_exp_f32 v226, v226\n"
    "v_mul_f32 v227, v187, v187\n"
    "v_fmaak_f32 v227, v13, v227, 0xc0135761\n"
    "v_mul_f32 v185, v185, v225\n"
    "v_mfma_f32_16x16x32_bf16 v[220:223], v[244:247], v[140:143], v[220:223]\n"
    "v_add_f32 v226, 1.0, v226\n"
    "v_mul_f32 v227, v187, v227\n"
    "v_rcp_f32 v226, v226\n"
    "v_cvt_pk_bf16_f32 v236, v184, v185\n"
    "v_exp_f32 v227, v227\n"
    "v_mul_f32 v224, v188, v188\n"
    "v_fmaak_f32 v224, v13, v224, 0xc0135761\n"
    "v_mul_f32 v186, v186, v226\n"
    "v_add_f32 v227, 1.0, v227\n"
    "v_mul_f32 v224, v188, v224\n"
    "v_rcp_f32 v227, v227\n"
    "v_exp_f32 v224, v224\n"
    "v_mul_f32 v225, v189, v189\n"
    "v_fmaak_f32 v225, v13, v225, 0xc0135761\n"
    "v_mul_f32 v187, v187, v227\n"
    "v_add_f32 v224, 1.0, v224\n"
    "v_mul_f32 v225, v189, v225\n"
    "v_rcp_f32 v224, v224\n"
    "v_cvt_pk_bf16_f32 v237, v186, v187\n"
    "v_exp_f32 v225, v225\n"
    "v_mul_f32 v226, v190, v190\n"
    "v_fmaak_f32 v226, v13, v226, 0xc0135761\n"
    "v_mul_f32 v188, v188, v224\n"
    "v_add_f32 v225, 1.0, v225\n"
    "v_mul_f32 v226, v190, v226\n"
    "v_rcp_f32 v225, v225\n"
    "v_exp_f32 v226, v226\n"
    "v_mul_f32 v227, v191, v191\n"
    "v_fmaak_f32 v227, v13, v227, 0xc0135761\n"
    "v_mul_f32 v189, v189, v225\n"
    "v_add_f32 v226, 1.0, v226\n"
    "v_mul_f32 v227, v191, v227\n"
    "v_rcp_f32 v226, v226\n"
    "v_cvt_pk_bf16_f32 v238, v188, v189\n"
    "v_exp_f32 v227, v227\n"
    "v_mul_f32 v190, v190, v226\n"
    "v_add_f32 v227, 1.0, v227\n"
    "v_rcp_f32 v227, v227\n"
    "s_nop 0\n"
    "v_mul_f32 v191, v191, v227\n"
    "v_cvt_pk_bf16_f32 v239, v190, v191\n"
    "ds_write_b128 v7, v[236:239] offset:20736\n"
    "s_waitcnt lgkmcnt(0)\n"
    "s_barrier\n"
    "v_mfma_f32_16x16x32_bf16 v[208:211], v[248:251], v[144:147], v[208:211]\n"
    "ds_read_b64_tr_b16 v[244:245], v3 offset:3072\n"
    "v_mfma_f32_16x16x32_bf16 v[212:215], v[248:251], v[148:151], v[212:215]\n"
    "ds_read_b64_tr_b16 v[246:247], v3 offset:11264\n"
    "ds_read_b128 v[176:179], v9 offset:20480\n"
    "v_mfma_f32_16x16x32_bf16 v[216:219], v[248:251], v[152:155], v[216:219]\n"
    "ds_read_b128 v[180:183], v9 offset:21504\n"
    "v_mfma_f32_16x16x32_bf16 v[220:223], v[248:251], v[156:159], v[220:223]\n"
    "ds_read_b128 v[184:187], v9 offset:45056\n"
    "v_mfma_f32_16x16x32_bf16 v[208:211], v[240:243], v[160:163], v[208:211]\n"
    "ds_read_b64_tr_b16 v[248:249], v3 offset:35840\n"
    "ds_read_b128 v[188:191], v9 offset:46080\n"
    "v_mfma_f32_16x16x32_bf16 v[212:215], v[240:243], v[164:167], v[212:215]\n"
    "ds_read_b64_tr_b16 v[250:251], v3 offset:44032\n"
    "ds_read_b128 v[192:195], v9 offset:22528\n"
    "v_mfma_f32_16x16x32_bf16 v[216:219], v[240:243], v[168:171], v[216:219]\n"
    "ds_read_b128 v[196:199], v9 offset:23552\n"
    "v_mfma_f32_16x16x32_bf16 v[220:223], v[240:243], v[172:175], v[220:223]\n"
    "ds_read_b128 v[200:203], v9 offset:47104\n"
    "s_waitcnt lgkmcnt(9)\n"
    "s_waitcnt lgkmcnt(8)\n"
    "v_mfma_f32_16x16x32_bf16 v[208:211], v[244:247], v[176:179], v[208:211]\n"
    "s_waitcnt vmcnt(2)\n"
    "s_barrier\n"
    "s_waitcnt lgkmcnt(7)\n"
    "v_mfma_f32_16x16x32_bf16 v[212:215], v[244:247], v[180:183], v[212:215]\n"
    "ds_read_b64_tr_b16 v[240:241], v3 offset:4096\n"
    "ds_read_b128 v[204:207], v9 offset:48128\n"
    "s_waitcnt lgkmcnt(8)\n"
    "v_mfma_f32_16x16x32_bf16 v[216:219], v[244:247], v[184:187], v[216:219]\n"
    "ds_read_b64_tr_b16 v[242:243], v3 offset:12288\n"
    "s_waitcnt lgkmcnt(7)\n"
    "v_mfma_f32_16x16x32_bf16 v[220:223], v[244:247], v[188:191], v[220:223]\n"
    "s_add_u32 m0, s42, 0\n"
    "s_mov_b32 s43, 299136\n"
    "buffer_load_dwordx4 v6, s[16:19], s43 offen offset:0 lds\n"
    "s_waitcnt lgkmcnt(6)\n"
    "s_waitcnt lgkmcnt(5)\n"
    "v_mfma_f32_16x16x32_bf16 v[208:211], v[248:251], v[192:195], v[208:211]\n"
    "ds_read_b64_tr_b16 v[244:245], v3 offset:36864\n"
    "s_waitcnt lgkmcnt(5)\n"
    "v_mfma_f32_16x16x32_bf16 v[212:215], v[248:251], v[196:199], v[212:215]\n"
    "ds_read_b64_tr_b16 v[246:247], v3 offset:45056\n"
    "s_mov_b32 s43, 371840\n"
    "buffer_load_dwordx4 v6, s[16:19], s43 offen offset:1024 lds\n"
    "s_waitcnt lgkmcnt(5)\n"
    "v_mfma_f32_16x16x32_bf16 v[216:219], v[248:251], v[200:203], v[216:219]\n"
    "s_waitcnt lgkmcnt(3)\n"
    "v_mfma_f32_16x16x32_bf16 v[220:223], v[248:251], v[204:207], v[220:223]\n"
    "s_waitcnt lgkmcnt(2)\n"
    "v_mfma_f32_16x16x32_bf16 v[224:227], v[240:243], v[16:19], 0\n"
    "ds_read_b64_tr_b16 v[248:249], v3 offset:5120\n"
    "v_mfma_f32_16x16x32_bf16 v[228:231], v[240:243], v[20:23], 0\n"
    "ds_read_b64_tr_b16 v[250:251], v3 offset:13312\n"
    "v_mfma_f32_16x16x32_bf16 v[232:235], v[240:243], v[24:27], 0\n"
    "v_mfma_f32_16x16x32_bf16 v[236:239], v[240:243], v[28:31], 0\n"
    "s_waitcnt lgkmcnt(2)\n"
    "v_mfma_f32_16x16x32_bf16 v[224:227], v[244:247], v[32:35], v[224:227]\n"
    "ds_read_b64_tr_b16 v[240:241], v3 offset:37888\n"
    "v_cvt_pk_bf16_f32 v208, v208, v209\n"
    "v_cvt_pk_bf16_f32 v209, v210, v211\n"
    "v_cvt_pk_bf16_f32 v210, v212, v213\n"
    "v_mfma_f32_16x16x32_bf16 v[228:231], v[244:247], v[36:39], v[228:231]\n"
    "ds_read_b64_tr_b16 v[242:243], v3 offset:46080\n"
    "v_cvt_pk_bf16_f32 v211, v214, v215\n"
    "v_mfma_f32_16x16x32_bf16 v[232:235], v[244:247], v[40:43], v[232:235]\n"
    "v_cvt_pk_bf16_f32 v212, v216, v217\n"
    "v_mfma_f32_16x16x32_bf16 v[236:239], v[244:247], v[44:47], v[236:239]\n"
    "v_cvt_pk_bf16_f32 v213, v218, v219\n"
    "s_waitcnt lgkmcnt(2)\n"
    "v_mfma_f32_16x16x32_bf16 v[224:227], v[248:251], v[48:51], v[224:227]\n"
    "s_waitcnt vmcnt(2)\n"
    "s_barrier\n"
    "v_mfma_f32_16x16x32_bf16 v[228:231], v[248:251], v[52:55], v[228:231]\n"
    "ds_read_b64_tr_b16 v[244:245], v3 offset:6144\n"
    "v_cvt_pk_bf16_f32 v214, v220, v221\n"
    "v_mfma_f32_16x16x32_bf16 v[232:235], v[248:251], v[56:59], v[232:235]\n"
    "ds_read_b64_tr_b16 v[246:247], v3 offset:14336\n"
    "v_cvt_pk_bf16_f32 v215, v222, v223\n"
    "v_mfma_f32_16x16x32_bf16 v[236:239], v[248:251], v[60:63], v[236:239]\n"
    "s_waitcnt lgkmcnt(2)\n"
    "v_mfma_f32_16x16x32_bf16 v[224:227], v[240:243], v[64:67], v[224:227]\n"
    "ds_read_b64_tr_b16 v[248:249], v3 offset:38912\n"
    "s_add_u32 m0, s47, 0\n"
    "s_mov_b32 s43, 4352\n"
    "buffer_load_dwordx4 v6, s[16:19], s43 offen offset:0 lds\n"
    "v_mfma_f32_16x16x32_bf16 v[228:231], v[240:243], v[68:71], v[228:231]\n"
    "ds_read_b64_tr_b16 v[250:251], v3 offset:47104\n"
    "v_mfma_f32_16x16x32_bf16 v[232:235], v[240:243], v[72:75], v[232:235]\n"
    "s_mov_b32 s43, 77056\n"
    "buffer_load_dwordx4 v6, s[16:19], s43 offen offset:1024 lds\n"
    "v_mfma_f32_16x16x32_bf16 v[236:239], v[240:243], v[76:79], v[236:239]\n"
    "s_waitcnt lgkmcnt(2)\n"
    "v_mfma_f32_16x16x32_bf16 v[224:227], v[244:247], v[80:83], v[224:227]\n"
    "ds_read_b64_tr_b16 v[240:241], v3 offset:7168\n"
    "s_mov_b32 s43, 149760\n"
    "buffer_load_dwordx4 v6, s[16:19], s43 offen offset:2048 lds\n"
    "v_mfma_f32_16x16x32_bf16 v[228:231], v[244:247], v[84:87], v[228:231]\n"
    "ds_read_b64_tr_b16 v[242:243], v3 offset:15360\n"
    "v_mfma_f32_16x16x32_bf16 v[232:235], v[244:247], v[88:91], v[232:235]\n"
    "s_mov_b32 s43, 222464\n"
    "buffer_load_dwordx4 v6, s[16:19], s43 offen offset:3072 lds\n"
    "v_mfma_f32_16x16x32_bf16 v[236:239], v[244:247], v[92:95], v[236:239]\n"
    "s_waitcnt lgkmcnt(2)\n"
    "v_mfma_f32_16x16x32_bf16 v[224:227], v[248:251], v[96:99], v[224:227]\n"
    "ds_read_b64_tr_b16 v[244:245], v3 offset:39936\n"
    "s_add_u32 m0, s47, 4096\n"
    "s_mov_b32 s43, 299264\n"
    "buffer_load_dwordx4 v6, s[16:19], s43 offen offset:0 lds\n"
    "v_mfma_f32_16x16x32_bf16 v[228:231], v[248:251], v[100:103], v[228:231]\n"
    "ds_read_b64_tr_b16 v[246:247], v3 offset:48128\n"
    "v_mfma_f32_16x16x32_bf16 v[232:235], v[248:251], v[104:107], v[232:235]\n"
    "s_mov_b32 s43, 371968\n"
    "buffer_load_dwordx4 v6, s[16:19], s43 offen offset:1024 lds\n"
    "v_mfma_f32_16x16x32_bf16 v[236:239], v[248:251], v[108:111], v[236:239]\n"
    "s_waitcnt lgkmcnt(2)\n"
    "v_mfma_f32_16x16x32_bf16 v[224:227], v[240:243], v[112:115], v[224:227]\n"
    "s_waitcnt vmcnt(6)\n"
    "s_barrier\n"
    "v_mfma_f32_16x16x32_bf16 v[228:231], v[240:243], v[116:119], v[228:231]\n"
    "ds_read_b64_tr_b16 v[248:249], v3 offset:0\n"
    "s_mov_b32 s38, 0\n"
    "buffer_store_dwordx4 v[208:211], v12, s[24:27], s38 offen sc1\n"
    "v_mfma_f32_16x16x32_bf16 v[232:235], v[240:243], v[120:123], v[232:235]\n"
    "ds_read_b64_tr_b16 v[250:251], v3 offset:8192\n"
    "v_mfma_f32_16x16x32_bf16 v[236:239], v[240:243], v[124:127], v[236:239]\n"
    "s_mov_b32 s38, 36864\n"
    "buffer_store_dwordx4 v[212:215], v12, s[24:27], s38 offen sc1\n"
    "s_waitcnt lgkmcnt(2)\n"
    "v_mfma_f32_16x16x32_bf16 v[224:227], v[244:247], v[128:131], v[224:227]\n"
    "ds_read_b64_tr_b16 v[240:241], v3 offset:32768\n"
    "v_mfma_f32_16x16x32_bf16 v[228:231], v[244:247], v[132:135], v[228:231]\n"
    "ds_read_b64_tr_b16 v[242:243], v3 offset:40960\n"
    "v_mfma_f32_16x16x32_bf16 v[232:235], v[244:247], v[136:139], v[232:235]\n"
    "v_mfma_f32_16x16x32_bf16 v[236:239], v[244:247], v[140:143], v[236:239]\n"
    "s_waitcnt lgkmcnt(2)\n"
    "v_mfma_f32_16x16x32_bf16 v[224:227], v[248:251], v[144:147], v[224:227]\n"
    "ds_read_b64_tr_b16 v[244:245], v3 offset:1024\n"
    "v_mfma_f32_16x16x32_bf16 v[228:231], v[248:251], v[148:151], v[228:231]\n"
    "ds_read_b64_tr_b16 v[246:247], v3 offset:9216\n"
    "v_mfma_f32_16x16x32_bf16 v[232:235], v[248:251], v[152:155], v[232:235]\n"
    "v_mfma_f32_16x16x32_bf16 v[236:239], v[248:251], v[156:159], v[236:239]\n"
    "s_waitcnt lgkmcnt(2)\n"
    "v_mfma_f32_16x16x32_bf16 v[224:227], v[240:243], v[160:163], v[224:227]\n"
    "ds_read_b64_tr_b16 v[248:249], v3 offset:33792\n"
    "v_mfma_f32_16x16x32_bf16 v[228:231], v[240:243], v[164:167], v[228:231]\n"
    "ds_read_b64_tr_b16 v[250:251], v3 offset:41984\n"
    "v_mfma_f32_16x16x32_bf16 v[232:235], v[240:243], v[168:171], v[232:235]\n"
    "v_mfma_f32_16x16x32_bf16 v[236:239], v[240:243], v[172:175], v[236:239]\n"
    "s_waitcnt lgkmcnt(2)\n"
    "v_mfma_f32_16x16x32_bf16 v[224:227], v[244:247], v[176:179], v[224:227]\n"
    "s_waitcnt vmcnt(2)\n"
    "s_barrier\n"
    "v_mfma_f32_16x16x32_bf16 v[228:231], v[244:247], v[180:183], v[228:231]\n"
    "ds_read_b64_tr_b16 v[240:241], v10 offset:0\n"
    "s_mov_b32 s43, 2432\n"
    "buffer_load_dwordx4 v6, s[16:19], s43 offen offset:2048 lds\n"
    "v_mfma_f32_16x16x32_bf16 v[232:235], v[244:247], v[184:187], v[232:235]\n"
    "ds_read_b64_tr_b16 v[242:243], v10 offset:10240\n"
    "s_mov_b32 s43, 75136\n"
    "buffer_load_dwordx4 v6, s[16:19], s43 offen offset:3072 lds\n"
    "v_mfma_f32_16x16x32_bf16 v[236:239], v[244:247], v[188:191], v[236:239]\n"
    "s_waitcnt lgkmcnt(2)\n"
    "v_mfma_f32_16x16x32_bf16 v[224:227], v[248:251], v[192:195], v[224:227]\n"
    "ds_read_b64_tr_b16 v[244:245], v10 offset:40960\n"
    "s_add_u32 m0, s47, 8192\n"
    "s_mov_b32 s43, 151936\n"
    "buffer_load_dwordx4 v6, s[16:19], s43 offen offset:0 lds\n"
    "v_mfma_f32_16x16x32_bf16 v[228:231], v[248:251], v[196:199], v[228:231]\n"
    "ds_read_b64_tr_b16 v[246:247], v10 offset:51200\n"
    "v_mfma_f32_16x16x32_bf16 v[232:235], v[248:251], v[200:203], v[232:235]\n"
    "s_mov_b32 s43, 224640\n"
    "buffer_load_dwordx4 v6, s[16:19], s43 offen offset:1024 lds\n"
    "v_mfma_f32_16x16x32_bf16 v[236:239], v[248:251], v[204:207], v[236:239]\n"
    "s_waitcnt lgkmcnt(2)\n"
    "v_mfma_f32_16x16x32_bf16 v[208:211], v[240:243], v[16:19], 0\n"
    "ds_read_b64_tr_b16 v[248:249], v10 offset:1024\n"
    "s_add_u32 m0, s42, 0\n"
    "s_mov_b32 s43, 297344\n"
    "buffer_load_dwordx4 v6, s[16:19], s43 offen offset:2048 lds\n"
    "v_mfma_f32_16x16x32_bf16 v[212:215], v[240:243], v[20:23], 0\n"
    "ds_read_b64_tr_b16 v[250:251], v10 offset:11264\n"
    "v_mfma_f32_16x16x32_bf16 v[216:219], v[240:243], v[24:27], 0\n"
    "s_mov_b32 s43, 370048\n"
    "buffer_load_dwordx4 v6, s[16:19], s43 offen offset:3072 lds\n"
    "v_mfma_f32_16x16x32_bf16 v[220:223], v[240:243], v[28:31], 0\n"
    "s_waitcnt lgkmcnt(2)\n"
    "v_mfma_f32_16x16x32_bf16 v[208:211], v[244:247], v[32:35], v[208:211]\n"
    "ds_read_b64_tr_b16 v[240:241], v10 offset:41984\n"
    "v_cvt_pk_bf16_f32 v224, v224, v225\n"
    "v_cvt_pk_bf16_f32 v225, v226, v227\n"
    "v_mfma_f32_16x16x32_bf16 v[212:215], v[244:247], v[36:39], v[212:215]\n"
    "ds_read_b64_tr_b16 v[242:243], v10 offset:52224\n"
    "v_cvt_pk_bf16_f32 v226, v228, v229\n"
    "v_mfma_f32_16x16x32_bf16 v[216:219], v[244:247], v[40:43], v[216:219]\n"
    "v_cvt_pk_bf16_f32 v227, v230, v231\n"
    "v_mfma_f32_16x16x32_bf16 v[220:223], v[244:247], v[44:47], v[220:223]\n"
    "v_cvt_pk_bf16_f32 v228, v232, v233\n"
    "s_waitcnt lgkmcnt(2)\n"
    "v_mfma_f32_16x16x32_bf16 v[208:211], v[248:251], v[48:51], v[208:211]\n"
    "ds_read_b64_tr_b16 v[244:245], v10 offset:2048\n"
    "v_cvt_pk_bf16_f32 v229, v234, v235\n"
    "v_mfma_f32_16x16x32_bf16 v[212:215], v[248:251], v[52:55], v[212:215]\n"
    "ds_read_b64_tr_b16 v[246:247], v10 offset:12288\n"
    "v_cvt_pk_bf16_f32 v230, v236, v237\n"
    "v_mfma_f32_16x16x32_bf16 v[216:219], v[248:251], v[56:59], v[216:219]\n"
    "v_cvt_pk_bf16_f32 v231, v238, v239\n"
    "v_mfma_f32_16x16x32_bf16 v[220:223], v[248:251], v[60:63], v[220:223]\n"
    "s_waitcnt lgkmcnt(2)\n"
    "v_mfma_f32_16x16x32_bf16 v[208:211], v[240:243], v[64:67], v[208:211]\n"
    "ds_read_b64_tr_b16 v[248:249], v10 offset:43008\n"
    "s_mov_b32 s38, 4096\n"
    "buffer_store_dwordx4 v[224:227], v12, s[24:27], s38 offen sc1\n"
    "v_mfma_f32_16x16x32_bf16 v[212:215], v[240:243], v[68:71], v[212:215]\n"
    "ds_read_b64_tr_b16 v[250:251], v10 offset:53248\n"
    "v_mfma_f32_16x16x32_bf16 v[216:219], v[240:243], v[72:75], v[216:219]\n"
    "s_mov_b32 s38, 40960\n"
    "buffer_store_dwordx4 v[228:231], v12, s[24:27], s38 offen sc1\n"
    "v_mfma_f32_16x16x32_bf16 v[220:223], v[240:243], v[76:79], v[220:223]\n"
    "s_waitcnt lgkmcnt(2)\n"
    "v_mfma_f32_16x16x32_bf16 v[208:211], v[244:247], v[80:83], v[208:211]\n"
    "ds_read_b64_tr_b16 v[240:241], v10 offset:3072\n"
    "v_mfma_f32_16x16x32_bf16 v[212:215], v[244:247], v[84:87], v[212:215]\n"
    "ds_read_b64_tr_b16 v[242:243], v10 offset:13312\n"
    "v_mfma_f32_16x16x32_bf16 v[216:219], v[244:247], v[88:91], v[216:219]\n"
    "v_mfma_f32_16x16x32_bf16 v[220:223], v[244:247], v[92:95], v[220:223]\n"
    "s_waitcnt lgkmcnt(2)\n"
    "v_mfma_f32_16x16x32_bf16 v[208:211], v[248:251], v[96:99], v[208:211]\n"
    "ds_read_b64_tr_b16 v[244:245], v10 offset:44032\n"
    "v_mfma_f32_16x16x32_bf16 v[212:215], v[248:251], v[100:103], v[212:215]\n"
    "ds_read_b64_tr_b16 v[246:247], v10 offset:54272\n"
    "v_mfma_f32_16x16x32_bf16 v[216:219], v[248:251], v[104:107], v[216:219]\n"
    "v_mfma_f32_16x16x32_bf16 v[220:223], v[248:251], v[108:111], v[220:223]\n"
    "s_waitcnt lgkmcnt(2)\n"
    "v_mfma_f32_16x16x32_bf16 v[208:211], v[240:243], v[112:115], v[208:211]\n"
    "ds_read_b64_tr_b16 v[248:249], v10 offset:4096\n"
    "v_mfma_f32_16x16x32_bf16 v[212:215], v[240:243], v[116:119], v[212:215]\n"
    "ds_read_b64_tr_b16 v[250:251], v10 offset:14336\n"
    "v_mfma_f32_16x16x32_bf16 v[216:219], v[240:243], v[120:123], v[216:219]\n"
    "v_mfma_f32_16x16x32_bf16 v[220:223], v[240:243], v[124:127], v[220:223]\n"
    "s_waitcnt lgkmcnt(2)\n"
    "v_mfma_f32_16x16x32_bf16 v[208:211], v[244:247], v[128:131], v[208:211]\n"
    "ds_read_b64_tr_b16 v[240:241], v10 offset:45056\n"
    "v_mfma_f32_16x16x32_bf16 v[212:215], v[244:247], v[132:135], v[212:215]\n"
    "ds_read_b64_tr_b16 v[242:243], v10 offset:55296\n"
    "v_mfma_f32_16x16x32_bf16 v[216:219], v[244:247], v[136:139], v[216:219]\n"
    "v_mfma_f32_16x16x32_bf16 v[220:223], v[244:247], v[140:143], v[220:223]\n"
    "s_waitcnt lgkmcnt(2)\n"
    "v_mfma_f32_16x16x32_bf16 v[208:211], v[248:251], v[144:147], v[208:211]\n"
    "ds_read_b64_tr_b16 v[244:245], v10 offset:5120\n"
    "v_mfma_f32_16x16x32_bf16 v[212:215], v[248:251], v[148:151], v[212:215]\n"
    "ds_read_b64_tr_b16 v[246:247], v10 offset:15360\n"
    "v_mfma_f32_16x16x32_bf16 v[216:219], v[248:251], v[152:155], v[216:219]\n"
    "v_mfma_f32_16x16x32_bf16 v[220:223], v[248:251], v[156:159], v[220:223]\n"
    "s_waitcnt lgkmcnt(2)\n"
    "v_mfma_f32_16x16x32_bf16 v[208:211], v[240:243], v[160:163], v[208:211]\n"
    "ds_read_b64_tr_b16 v[248:249], v10 offset:46080\n"
    "v_mfma_f32_16x16x32_bf16 v[212:215], v[240:243], v[164:167], v[212:215]\n"
    "ds_read_b64_tr_b16 v[250:251], v10 offset:56320\n"
    "v_mfma_f32_16x16x32_bf16 v[216:219], v[240:243], v[168:171], v[216:219]\n"
    "v_mfma_f32_16x16x32_bf16 v[220:223], v[240:243], v[172:175], v[220:223]\n"
    "s_waitcnt lgkmcnt(2)\n"
    "v_mfma_f32_16x16x32_bf16 v[208:211], v[244:247], v[176:179], v[208:211]\n"
    "s_waitcnt vmcnt(2)\n"
    "s_barrier\n"
    "v_mfma_f32_16x16x32_bf16 v[212:215], v[244:247], v[180:183], v[212:215]\n"
    "ds_read_b64_tr_b16 v[240:241], v10 offset:6144\n"
    "s_add_u32 m0, s42, 4096\n"
    "s_mov_b32 s43, 4608\n"
    "buffer_load_dwordx4 v6, s[16:19], s43 offen offset:0 lds\n"
    "v_mfma_f32_16x16x32_bf16 v[216:219], v[244:247], v[184:187], v[216:219]\n"
    "ds_read_b64_tr_b16 v[242:243], v10 offset:16384\n"
    "s_mov_b32 s43, 77312\n"
    "buffer_load_dwordx4 v6, s[16:19], s43 offen offset:1024 lds\n"
    "v_mfma_f32_16x16x32_bf16 v[220:223], v[244:247], v[188:191], v[220:223]\n"
    "s_waitcnt lgkmcnt(2)\n"
    "v_mfma_f32_16x16x32_bf16 v[208:211], v[248:251], v[192:195], v[208:211]\n"
    "ds_read_b64_tr_b16 v[244:245], v10 offset:47104\n"
    "s_mov_b32 s43, 150016\n"
    "buffer_load_dwordx4 v6, s[16:19], s43 offen offset:2048 lds\n"
    "v_mfma_f32_16x16x32_bf16 v[212:215], v[248:251], v[196:199], v[212:215]\n"
    "ds_read_b64_tr_b16 v[246:247], v10 offset:57344\n"
    "v_mfma_f32_16x16x32_bf16 v[216:219], v[248:251], v[200:203], v[216:219]\n"
    "s_mov_b32 s43, 222720\n"
    "buffer_load_dwordx4 v6, s[16:19], s43 offen offset:3072 lds\n"
    "v_mfma_f32_16x16x32_bf16 v[220:223], v[248:251], v[204:207], v[220:223]\n"
    "s_waitcnt lgkmcnt(2)\n"
    "v_mfma_f32_16x16x32_bf16 v[224:227], v[240:243], v[16:19], 0\n"
    "ds_read_b64_tr_b16 v[248:249], v10 offset:7168\n"
    "s_add_u32 m0, s42, 0\n"
    "s_mov_b32 s43, 299520\n"
    "buffer_load_dwordx4 v6, s[16:19], s43 offen offset:0 lds\n"
    "v_mfma_f32_16x16x32_bf16 v[228:231], v[240:243], v[20:23], 0\n"
    "ds_read_b64_tr_b16 v[250:251], v10 offset:17408\n"
    "v_mfma_f32_16x16x32_bf16 v[232:235], v[240:243], v[24:27], 0\n"
    "s_mov_b32 s43, 372224\n"
    "buffer_load_dwordx4 v6, s[16:19], s43 offen offset:1024 lds\n"
    "v_mfma_f32_16x16x32_bf16 v[236:239], v[240:243], v[28:31], 0\n"
    "s_waitcnt lgkmcnt(2)\n"
    "v_mfma_f32_16x16x32_bf16 v[224:227], v[244:247], v[32:35], v[224:227]\n"
    "ds_read_b64_tr_b16 v[240:241], v10 offset:48128\n"
    "v_cvt_pk_bf16_f32 v208, v208, v209\n"
    "v_cvt_pk_bf16_f32 v209, v210, v211\n"
    "v_mfma_f32_16x16x32_bf16 v[228:231], v[244:247], v[36:39], v[228:231]\n"
    "ds_read_b64_tr_b16 v[242:243], v10 offset:58368\n"
    "v_cvt_pk_bf16_f32 v210, v212, v213\n"
    "v_mfma_f32_16x16x32_bf16 v[232:235], v[244:247], v[40:43], v[232:235]\n"
    "v_cvt_pk_bf16_f32 v211, v214, v215\n"
    "v_mfma_f32_16x16x32_bf16 v[236:239], v[244:247], v[44:47], v[236:239]\n"
    "v_cvt_pk_bf16_f32 v212, v216, v217\n"
    "s_waitcnt lgkmcnt(2)\n"
    "v_mfma_f32_16x16x32_bf16 v[224:227], v[248:251], v[48:51], v[224:227]\n"
    "ds_read_b64_tr_b16 v[244:245], v10 offset:8192\n"
    "v_cvt_pk_bf16_f32 v213, v218, v219\n"
    "v_mfma_f32_16x16x32_bf16 v[228:231], v[248:251], v[52:55], v[228:231]\n"
    "ds_read_b64_tr_b16 v[246:247], v10 offset:18432\n"
    "v_cvt_pk_bf16_f32 v214, v220, v221\n"
    "v_mfma_f32_16x16x32_bf16 v[232:235], v[248:251], v[56:59], v[232:235]\n"
    "v_cvt_pk_bf16_f32 v215, v222, v223\n"
    "v_mfma_f32_16x16x32_bf16 v[236:239], v[248:251], v[60:63], v[236:239]\n"
    "s_waitcnt lgkmcnt(2)\n"
    "v_mfma_f32_16x16x32_bf16 v[224:227], v[240:243], v[64:67], v[224:227]\n"
    "ds_read_b64_tr_b16 v[248:249], v10 offset:49152\n"
    "s_add_u32 s38, s53, 0\n"
    "buffer_store_dwordx2 v[252:253], v254, s[48:51], s38 offen sc1\n"
    "v_mfma_f32_16x16x32_bf16 v[228:231], v[240:243], v[68:71], v[228:231]\n"
    "ds_read_b64_tr_b16 v[250:251], v10 offset:59392\n"
    "v_mfma_f32_16x16x32_bf16 v[232:235], v[240:243], v[72:75], v[232:235]\n"
    "s_mov_b32 s38, 8192\n"
    "buffer_store_dwordx4 v[208:211], v12, s[24:27], s38 offen sc1\n"
    "v_mfma_f32_16x16x32_bf16 v[236:239], v[240:243], v[76:79], v[236:239]\n"
    "s_waitcnt lgkmcnt(2)\n"
    "v_mfma_f32_16x16x32_bf16 v[224:227], v[244:247], v[80:83], v[224:227]\n"
    "ds_read_b64_tr_b16 v[240:241], v10 offset:9216\n"
    "s_mov_b32 s38, 45056\n"
    "buffer_store_dwordx4 v[212:215], v12, s[24:27], s38 offen sc1\n"
    "v_mfma_f32_16x16x32_bf16 v[228:231], v[244:247], v[84:87], v[228:231]\n"
    "ds_read_b64_tr_b16 v[242:243], v10 offset:19456\n"
    "v_mfma_f32_16x16x32_bf16 v[232:235], v[244:247], v[88:91], v[232:235]\n"
    "v_mfma_f32_16x16x32_bf16 v[236:239], v[244:247], v[92:95], v[236:239]\n"
    "s_waitcnt lgkmcnt(2)\n"
    "v_mfma_f32_16x16x32_bf16 v[224:227], v[248:251], v[96:99], v[224:227]\n"
    "ds_read_b64_tr_b16 v[244:245], v10 offset:50176\n"
    "v_mfma_f32_16x16x32_bf16 v[228:231], v[248:251], v[100:103], v[228:231]\n"
    "ds_read_b64_tr_b16 v[246:247], v10 offset:60416\n"
    "v_mfma_f32_16x16x32_bf16 v[232:235], v[248:251], v[104:107], v[232:235]\n"
    "v_mfma_f32_16x16x32_bf16 v[236:239], v[248:251], v[108:111], v[236:239]\n"
    "s_waitcnt lgkmcnt(2)\n"
    "v_mfma_f32_16x16x32_bf16 v[224:227], v[240:243], v[112:115], v[224:227]\n"
    "ds_read_b64_tr_b16 v[248:249], v3 offset:2048\n"
    "v_mfma_f32_16x16x32_bf16 v[228:231], v[240:243], v[116:119], v[228:231]\n"
    "ds_read_b64_tr_b16 v[250:251], v3 offset:10240\n"
    "v_mfma_f32_16x16x32_bf16 v[232:235], v[240:243], v[120:123], v[232:235]\n"
    "v_mfma_f32_16x16x32_bf16 v[236:239], v[240:243], v[124:127], v[236:239]\n"
    "s_waitcnt lgkmcnt(2)\n"
    "v_mfma_f32_16x16x32_bf16 v[224:227], v[244:247], v[128:131], v[224:227]\n"
    "ds_read_b64_tr_b16 v[240:241], v3 offset:34816\n"
    "v_mfma_f32_16x16x32_bf16 v[228:231], v[244:247], v[132:135], v[228:231]\n"
    "ds_read_b64_tr_b16 v[242:243], v3 offset:43008\n"
    "v_mfma_f32_16x16x32_bf16 v[232:235], v[244:247], v[136:139], v[232:235]\n"
    "v_mfma_f32_16x16x32_bf16 v[236:239], v[244:247], v[140:143], v[236:239]\n"
    "s_waitcnt lgkmcnt(2)\n"
    "v_mfma_f32_16x16x32_bf16 v[224:227], v[248:251], v[144:147], v[224:227]\n"
    "ds_read_b64_tr_b16 v[244:245], v3 offset:3072\n"
    "v_mfma_f32_16x16x32_bf16 v[228:231], v[248:251], v[148:151], v[228:231]\n"
    "ds_read_b64_tr_b16 v[246:247], v3 offset:11264\n"
    "v_mfma_f32_16x16x32_bf16 v[232:235], v[248:251], v[152:155], v[232:235]\n"
    "v_mfma_f32_16x16x32_bf16 v[236:239], v[248:251], v[156:159], v[236:239]\n"
    "s_waitcnt lgkmcnt(2)\n"
    "v_mfma_f32_16x16x32_bf16 v[224:227], v[240:243], v[160:163], v[224:227]\n"
    "ds_read_b64_tr_b16 v[248:249], v3 offset:35840\n"
    "v_mfma_f32_16x16x32_bf16 v[228:231], v[240:243], v[164:167], v[228:231]\n"
    "ds_read_b64_tr_b16 v[250:251], v3 offset:44032\n"
    "v_mfma_f32_16x16x32_bf16 v[232:235], v[240:243], v[168:171], v[232:235]\n"
    "v_mfma_f32_16x16x32_bf16 v[236:239], v[240:243], v[172:175], v[236:239]\n"
    "s_waitcnt lgkmcnt(2)\n"
    "v_mfma_f32_16x16x32_bf16 v[224:227], v[244:247], v[176:179], v[224:227]\n"
    "s_waitcnt vmcnt(3)\n"
    "s_barrier\n"
    "v_mfma_f32_16x16x32_bf16 v[228:231], v[244:247], v[180:183], v[228:231]\n"
    "ds_read_b64_tr_b16 v[240:241], v3 offset:4096\n"
    "s_add_u32 m0, s47, 0\n"
    "s_mov_b32 s43, 4736\n"
    "buffer_load_dwordx4 v6, s[16:19], s43 offen offset:0 lds\n"
    "v_mfma_f32_16x16x32_bf16 v[232:235], v[244:247], v[184:187], v[232:235]\n"
    "ds_read_b64_tr_b16 v[242:243], v3 offset:12288\n"
    "s_mov_b32 s43, 77440\n"
    "buffer_load_dwordx4 v6, s[16:19], s43 offen offset:1024 lds\n"
    "v_mfma_f32_16x16x32_bf16 v[236:239], v[244:247], v[188:191], v[236:239]\n"
    "s_waitcnt lgkmcnt(2)\n"
    "v_mfma_f32_16x16x32_bf16 v[224:227], v[248:251], v[192:195], v[224:227]\n"
    "ds_read_b64_tr_b16 v[244:245], v3 offset:36864\n"
    "s_mov_b32 s43, 150144\n"
    "buffer_load_dwordx4 v6, s[16:19], s43 offen offset:2048 lds\n"
    "v_mfma_f32_16x16x32_bf16 v[228:231], v[248:251], v[196:199], v[228:231]\n"
    "ds_read_b64_tr_b16 v[246:247], v3 offset:45056\n"
    "v_mfma_f32_16x16x32_bf16 v[232:235], v[248:251], v[200:203], v[232:235]\n"
    "s_mov_b32 s43, 222848\n"
    "buffer_load_dwordx4 v6, s[16:19], s43 offen offset:3072 lds\n"
    "v_mfma_f32_16x16x32_bf16 v[236:239], v[248:251], v[204:207], v[236:239]\n"
    "s_waitcnt lgkmcnt(2)\n"
    "v_mfma_f32_16x16x32_bf16 v[208:211], v[240:243], v[16:19], 0\n"
    "ds_read_b64_tr_b16 v[248:249], v3 offset:5120\n"
    "s_add_u32 m0, s47, 4096\n"
    "s_mov_b32 s43, 299648\n"
    "buffer_load_dwordx4 v6, s[16:19], s43 offen offset:0 lds\n"
    "v_mfma_f32_16x16x32_bf16 v[212:215], v[240:243], v[20:23], 0\n"
    "ds_read_b64_tr_b16 v[250:251], v3 offset:13312\n"
    "v_mfma_f32_16x16x32_bf16 v[216:219], v[240:243], v[24:27], 0\n"
    "s_mov_b32 s43, 372352\n"
    "buffer_load_dwordx4 v6, s[16:19], s43 offen offset:1024 lds\n"
    "v_mfma_f32_16x16x32_bf16 v[220:223], v[240:243], v[28:31], 0\n"
    "s_waitcnt lgkmcnt(2)\n"
    "v_mfma_f32_16x16x32_bf16 v[208:211], v[244:247], v[32:35], v[208:211]\n"
    "ds_read_b64_tr_b16 v[240:241], v3 offset:37888\n"
    "v_cvt_pk_bf16_f32 v224, v224, v225\n"
    "v_cvt_pk_bf16_f32 v225, v226, v227\n"
    "v_mfma_f32_16x16x32_bf16 v[212:215], v[244:247], v[36:39], v[212:215]\n"
    "ds_read_b64_tr_b16 v[242:243], v3 offset:46080\n"
    "v_cvt_pk_bf16_f32 v226, v228, v229\n"
    "v_mfma_f32_16x16x32_bf16 v[216:219], v[244:247], v[40:43], v[216:219]\n"
    "v_cvt_pk_bf16_f32 v227, v230, v231\n"
    "v_mfma_f32_16x16x32_bf16 v[220:223], v[244:247], v[44:47], v[220:223]\n"
    "v_cvt_pk_bf16_f32 v228, v232, v233\n"
    "s_waitcnt lgkmcnt(2)\n"
    "v_mfma_f32_16x16x32_bf16 v[208:211], v[248:251], v[48:51], v[208:211]\n"
    "ds_read_b64_tr_b16 v[244:245], v3 offset:6144\n"
    "v_cvt_pk_bf16_f32 v229, v234, v235\n"
    "v_mfma_f32_16x16x32_bf16 v[212:215], v[248:251], v[52:55], v[212:215]\n"
    "ds_read_b64_tr_b16 v[246:247], v3 offset:14336\n"
    "v_cvt_pk_bf16_f32 v230, v236, v237\n"
    "v_mfma_f32_16x16x32_bf16 v[216:219], v[248:251], v[56:59], v[216:219]\n"
    "v_cvt_pk_bf16_f32 v231, v238, v239\n"
    "v_mfma_f32_16x16x32_bf16 v[220:223], v[248:251], v[60:63], v[220:223]\n"
    "s_waitcnt lgkmcnt(2)\n"
    "v_mfma_f32_16x16x32_bf16 v[208:211], v[240:243], v[64:67], v[208:211]\n"
    "ds_read_b64_tr_b16 v[248:249], v3 offset:38912\n"
    "s_add_u32 s38, s53, 1536\n"
    "buffer_store_dwordx2 v[252:253], v254, s[48:51], s38 offen sc1\n"
    "v_mfma_f32_16x16x32_bf16 v[212:215], v[240:243], v[68:71], v[212:215]\n"
    "ds_read_b64_tr_b16 v[250:251], v3 offset:47104\n"
    "v_mfma_f32_16x16x32_bf16 v[216:219], v[240:243], v[72:75], v[216:219]\n"
    "s_mov_b32 s38, 12288\n"
    "buffer_store_dwordx4 v[224:227], v12, s[24:27], s38 offen sc1\n"
    "v_mfma_f32_16x16x32_bf16 v[220:223], v[240:243], v[76:79], v[220:223]\n"
    "s_waitcnt lgkmcnt(2)\n"
    "v_mfma_f32_16x16x32_bf16 v[208:211], v[244:247], v[80:83], v[208:211]\n"
    "ds_read_b64_tr_b16 v[240:241], v3 offset:7168\n"
    "s_mov_b32 s38, 49152\n"
    "buffer_store_dwordx4 v[228:231], v12, s[24:27], s38 offen sc1\n"
    "v_mfma_f32_16x16x32_bf16 v[212:215], v[244:247], v[84:87], v[212:215]\n"
    "ds_read_b64_tr_b16 v[242:243], v3 offset:15360\n"
    "v_mfma_f32_16x16x32_bf16 v[216:219], v[244:247], v[88:91], v[216:219]\n"
    "v_mfma_f32_16x16x32_bf16 v[220:223], v[244:247], v[92:95], v[220:223]\n"
    "s_waitcnt lgkmcnt(2)\n"
    "v_mfma_f32_16x16x32_bf16 v[208:211], v[248:251], v[96:99], v[208:211]\n"
    "ds_read_b64_tr_b16 v[244:245], v3 offset:39936\n"
    "v_mfma_f32_16x16x32_bf16 v[212:215], v[248:251], v[100:103], v[212:215]\n"
    "ds_read_b64_tr_b16 v[246:247], v3 offset:48128\n"
    "v_mfma_f32_16x16x32_bf16 v[216:219], v[248:251], v[104:107], v[216:219]\n"
    "v_mfma_f32_16x16x32_bf16 v[220:223], v[248:251], v[108:111], v[220:223]\n"
    "s_waitcnt lgkmcnt(2)\n"
    "v_mfma_f32_16x16x32_bf16 v[208:211], v[240:243], v[112:115], v[208:211]\n"
    "ds_read_b64_tr_b16 v[248:249], v3 offset:0\n"
    "v_mfma_f32_16x16x32_bf16 v[212:215], v[240:243], v[116:119], v[212:215]\n"
    "ds_read_b64_tr_b16 v[250:251], v3 offset:8192\n"
    "v_mfma_f32_16x16x32_bf16 v[216:219], v[240:243], v[120:123], v[216:219]\n"
    "v_mfma_f32_16x16x32_bf16 v[220:223], v[240:243], v[124:127], v[220:223]\n"
    "s_waitcnt lgkmcnt(2)\n"
    "v_mfma_f32_16x16x32_bf16 v[208:211], v[244:247], v[128:131], v[208:211]\n"
    "ds_read_b64_tr_b16 v[240:241], v3 offset:32768\n"
    "v_mfma_f32_16x16x32_bf16 v[212:215], v[244:247], v[132:135], v[212:215]\n"
    "ds_read_b64_tr_b16 v[242:243], v3 offset:40960\n"
    "v_mfma_f32_16x16x32_bf16 v[216:219], v[244:247], v[136:139], v[216:219]\n"
    "v_mfma_f32_16x16x32_bf16 v[220:223], v[244:247], v[140:143], v[220:223]\n"
    "s_waitcnt lgkmcnt(2)\n"
    "v_mfma_f32_16x16x32_bf16 v[208:211], v[248:251], v[144:147], v[208:211]\n"
    "ds_read_b64_tr_b16 v[244:245], v3 offset:1024\n"
    "v_mfma_f32_16x16x32_bf16 v[212:215], v[248:251], v[148:151], v[212:215]\n"
    "ds_read_b64_tr_b16 v[246:247], v3 offset:9216\n"
    "v_mfma_f32_16x16x32_bf16 v[216:219], v[248:251], v[152:155], v[216:219]\n"
    "v_mfma_f32_16x16x32_bf16 v[220:223], v[248:251], v[156:159], v[220:223]\n"
    "s_waitcnt lgkmcnt(2)\n"
    "v_mfma_f32_16x16x32_bf16 v[208:211], v[240:243], v[160:163], v[208:211]\n"
    "ds_read_b64_tr_b16 v[248:249], v3 offset:33792\n"
    "v_mfma_f32_16x16x32_bf16 v[212:215], v[240:243], v[164:167], v[212:215]\n"
    "ds_read_b64_tr_b16 v[250:251], v3 offset:41984\n"
    "v_mfma_f32_16x16x32_bf16 v[216:219], v[240:243], v[168:171], v[216:219]\n"
    "v_mfma_f32_16x16x32_bf16 v[220:223], v[240:243], v[172:175], v[220:223]\n"
    "s_waitcnt lgkmcnt(2)\n"
    "v_mfma_f32_16x16x32_bf16 v[208:211], v[244:247], v[176:179], v[208:211]\n"
    "s_waitcnt vmcnt(3)\n"
    "s_barrier\n"
    "v_mfma_f32_16x16x32_bf16 v[212:215], v[244:247], v[180:183], v[212:215]\n"
    "ds_read_b64_tr_b16 v[240:241], v10 offset:0\n"
    "s_mov_b32 s43, 2816\n"
    "buffer_load_dwordx4 v6, s[16:19], s43 offen offset:2048 lds\n"
    "v_mfma_f32_16x16x32_bf16 v[216:219], v[244:247], v[184:187], v[216:219]\n"
    "ds_read_b64_tr_b16 v[242:243], v10 offset:10240\n"
    "s_mov_b32 s43, 75520\n"
    "buffer_load_dwordx4 v6, s[16:19], s43 offen offset:3072 lds\n"
    "v_mfma_f32_16x16x32_bf16 v[220:223], v[244:247], v[188:191], v[220:223]\n"
    "s_waitcnt lgkmcnt(2)\n"
    "v_mfma_f32_16x16x32_bf16 v[208:211], v[248:251], v[192:195], v[208:211]\n"
    "ds_read_b64_tr_b16 v[244:245], v10 offset:40960\n"
    "s_add_u32 m0, s47, 8192\n"
    "s_mov_b32 s43, 152320\n"
    "buffer_load_dwordx4 v6, s[16:19], s43 offen offset:0 lds\n"
    "v_mfma_f32_16x16x32_bf16 v[212:215], v[248:251], v[196:199], v[212:215]\n"
    "ds_read_b64_tr_b16 v[246:247], v10 offset:51200\n"
    "v_mfma_f32_16x16x32_bf16 v[216:219], v[248:251], v[200:203], v[216:219]\n"
    "s_mov_b32 s43, 225024\n"
    "buffer_load_dwordx4 v6, s[16:19], s43 offen offset:1024 lds\n"
    "v_mfma_f32_16x16x32_bf16 v[220:223], v[248:251], v[204:207], v[220:223]\n"
    "s_waitcnt lgkmcnt(2)\n"
    "v_mfma_f32_16x16x32_bf16 v[224:227], v[240:243], v[16:19], 0\n"
    "ds_read_b64_tr_b16 v[248:249], v10 offset:1024\n"
    "s_add_u32 m0, s42, 0\n"
    "s_mov_b32 s43, 297728\n"
    "buffer_load_dwordx4 v6, s[16:19], s43 offen offset:2048 lds\n"
    "v_mfma_f32_16x16x32_bf16 v[228:231], v[240:243], v[20:23], 0\n"
    "ds_read_b64_tr_b16 v[250:251], v10 offset:11264\n"
    "v_mfma_f32_16x16x32_bf16 v[232:235], v[240:243], v[24:27], 0\n"
    "s_mov_b32 s43, 370432\n"
    "buffer_load_dwordx4 v6, s[16:19], s43 offen offset:3072 lds\n"
    "v_mfma_f32_16x16x32_bf16 v[236:239], v[240:243], v[28:31], 0\n"
    "s_waitcnt lgkmcnt(2)\n"
    "v_mfma_f32_16x16x32_bf16 v[224:227], v[244:247], v[32:35], v[224:227]\n"
    "ds_read_b64_tr_b16 v[240:241], v10 offset:41984\n"
    "v_cvt_pk_bf16_f32 v208, v208, v209\n"
    "v_cvt_pk_bf16_f32 v209, v210, v211\n"
    "v_mfma_f32_16x16x32_bf16 v[228:231], v[244:247], v[36:39], v[228:231]\n"
    "ds_read_b64_tr_b16 v[242:243], v10 offset:52224\n"
    "v_cvt_pk_bf16_f32 v210, v212, v213\n"
    "v_mfma_f32_16x16x32_bf16 v[232:235], v[244:247], v[40:43], v[232:235]\n"
    "v_cvt_pk_bf16_f32 v211, v214, v215\n"
    "v_mfma_f32_16x16x32_bf16 v[236:239], v[244:247], v[44:47], v[236:239]\n"
    "v_cvt_pk_bf16_f32 v212, v216, v217\n"
    "s_waitcnt lgkmcnt(2)\n"
    "v_mfma_f32_16x16x32_bf16 v[224:227], v[248:251], v[48:51], v[224:227]\n"
    "ds_read_b64_tr_b16 v[244:245], v10 offset:2048\n"
    "v_cvt_pk_bf16_f32 v213, v218, v219\n"
    "v_mfma_f32_16x16x32_bf16 v[228:231], v[248:251], v[52:55], v[228:231]\n"
    "ds_read_b64_tr_b16 v[246:247], v10 offset:12288\n"
    "v_cvt_pk_bf16_f32 v214, v220, v221\n"
    "v_mfma_f32_16x16x32_bf16 v[232:235], v[248:251], v[56:59], v[232:235]\n"
    "v_cvt_pk_bf16_f32 v215, v222, v223\n"
    "v_mfma_f32_16x16x32_bf16 v[236:239], v[248:251], v[60:63], v[236:239]\n"
    "s_waitcnt lgkmcnt(2)\n"
    "v_mfma_f32_16x16x32_bf16 v[224:227], v[240:243], v[64:67], v[224:227]\n"
    "ds_read_b64_tr_b16 v[248:249], v10 offset:43008\n"
    "s_add_u32 s38, s53, 3072\n"
    "buffer_store_dwordx2 v[252:253], v254, s[48:51], s38 offen sc1\n"
    "v_mfma_f32_16x16x32_bf16 v[228:231], v[240:243], v[68:71], v[228:231]\n"
    "ds_read_b64_tr_b16 v[250:251], v10 offset:53248\n"
    "v_mfma_f32_16x16x32_bf16 v[232:235], v[240:243], v[72:75], v[232:235]\n"
    "s_mov_b32 s38, 16384\n"
    "buffer_store_dwordx4 v[208:211], v12, s[24:27], s38 offen sc1\n"
    "v_mfma_f32_16x16x32_bf16 v[236:239], v[240:243], v[76:79], v[236:239]\n"
    "s_waitcnt lgkmcnt(2)\n"
    "v_mfma_f32_16x16x32_bf16 v[224:227], v[244:247], v[80:83], v[224:227]\n"
    "ds_read_b64_tr_b16 v[240:241], v10 offset:3072\n"
    "s_mov_b32 s38, 53248\n"
    "buffer_store_dwordx4 v[212:215], v12, s[24:27], s38 offen sc1\n"
    "v_mfma_f32_16x16x32_bf16 v[228:231], v[244:247], v[84:87], v[228:231]\n"
    "ds_read_b64_tr_b16 v[242:243], v10 offset:13312\n"
    "v_mfma_f32_16x16x32_bf16 v[232:235], v[244:247], v[88:91], v[232:235]\n"
    "v_mfma_f32_16x16x32_bf16 v[236:239], v[244:247], v[92:95], v[236:239]\n"
    "s_waitcnt lgkmcnt(2)\n"
    "v_mfma_f32_16x16x32_bf16 v[224:227], v[248:251], v[96:99], v[224:227]\n"
    "ds_read_b64_tr_b16 v[244:245], v10 offset:44032\n"
    "v_mfma_f32_16x16x32_bf16 v[228:231], v[248:251], v[100:103], v[228:231]\n"
    "ds_read_b64_tr_b16 v[246:247], v10 offset:54272\n"
    "v_mfma_f32_16x16x32_bf16 v[232:235], v[248:251], v[104:107], v[232:235]\n"
    "v_mfma_f32_16x16x32_bf16 v[236:239], v[248:251], v[108:111], v[236:239]\n"
    "s_waitcnt lgkmcnt(2)\n"
    "v_mfma_f32_16x16x32_bf16 v[224:227], v[240:243], v[112:115], v[224:227]\n"
    "ds_read_b64_tr_b16 v[248:249], v10 offset:4096\n"
    "v_mfma_f32_16x16x32_bf16 v[228:231], v[240:243], v[116:119], v[228:231]\n"
    "ds_read_b64_tr_b16 v[250:251], v10 offset:14336\n"
    "v_mfma_f32_16x16x32_bf16 v[232:235], v[240:243], v[120:123], v[232:235]\n"
    "v_mfma_f32_16x16x32_bf16 v[236:239], v[240:243], v[124:127], v[236:239]\n"
    "s_waitcnt lgkmcnt(2)\n"
    "v_mfma_f32_16x16x32_bf16 v[224:227], v[244:247], v[128:131], v[224:227]\n"
    "ds_read_b64_tr_b16 v[240:241], v10 offset:45056\n"
    "v_mfma_f32_16x16x32_bf16 v[228:231], v[244:247], v[132:135], v[228:231]\n"
    "ds_read_b64_tr_b16 v[242:243], v10 offset:55296\n"
    "v_mfma_f32_16x16x32_bf16 v[232:235], v[244:247], v[136:139], v[232:235]\n"
    "v_mfma_f32_16x16x32_bf16 v[236:239], v[244:247], v[140:143], v[236:239]\n"
    "s_waitcnt lgkmcnt(2)\n"
    "v_mfma_f32_16x16x32_bf16 v[224:227], v[248:251], v[144:147], v[224:227]\n"
    "ds_read_b64_tr_b16 v[244:245], v10 offset:5120\n"
    "v_mfma_f32_16x16x32_bf16 v[228:231], v[248:251], v[148:151], v[228:231]\n"
    "ds_read_b64_tr_b16 v[246:247], v10 offset:15360\n"
    "v_mfma_f32_16x16x32_bf16 v[232:235], v[248:251], v[152:155], v[232:235]\n"
    "v_mfma_f32_16x16x32_bf16 v[236:239], v[248:251], v[156:159], v[236:239]\n"
    "s_waitcnt lgkmcnt(2)\n"
    "v_mfma_f32_16x16x32_bf16 v[224:227], v[240:243], v[160:163], v[224:227]\n"
    "ds_read_b64_tr_b16 v[248:249], v10 offset:46080\n"
    "v_mfma_f32_16x16x32_bf16 v[228:231], v[240:243], v[164:167], v[228:231]\n"
    "ds_read_b64_tr_b16 v[250:251], v10 offset:56320\n"
    "v_mfma_f32_16x16x32_bf16 v[232:235], v[240:243], v[168:171], v[232:235]\n"
    "v_mfma_f32_16x16x32_bf16 v[236:239], v[240:243], v[172:175], v[236:239]\n"
    "s_waitcnt lgkmcnt(2)\n"
    "v_mfma_f32_16x16x32_bf16 v[224:227], v[244:247], v[176:179], v[224:227]\n"
    "s_waitcnt vmcnt(3)\n"
    "s_barrier\n"
    "v_mfma_f32_16x16x32_bf16 v[228:231], v[244:247], v[180:183], v[228:231]\n"
    "ds_read_b64_tr_b16 v[240:241], v10 offset:6144\n"
    "s_add_u32 m0, s42, 4096\n"
    "s_mov_b32 s43, 4992\n"
    "buffer_load_dwordx4 v6, s[16:19], s43 offen offset:0 lds\n"
    "v_mfma_f32_16x16x32_bf16 v[232:235], v[244:247], v[184:187], v[232:235]\n"
    "ds_read_b64_tr_b16 v[242:243], v10 offset:16384\n"
    "s_mov_b32 s43, 77696\n"
    "buffer_load_dwordx4 v6, s[16:19], s43 offen offset:1024 lds\n"
    "v_mfma_f32_16x16x32_bf16 v[236:239], v[244:247], v[188:191], v[236:239]\n"
    "s_waitcnt lgkmcnt(2)\n"
    "v_mfma_f32_16x16x32_bf16 v[224:227], v[248:251], v[192:195], v[224:227]\n"
    "ds_read_b64_tr_b16 v[244:245], v10 offset:47104\n"
    "s_mov_b32 s43, 150400\n"
    "buffer_load_dwordx4 v6, s[16:19], s43 offen offset:2048 lds\n"
    "v_mfma_f32_16x16x32_bf16 v[228:231], v[248:251], v[196:199], v[228:231]\n"
    "ds_read_b64_tr_b16 v[246:247], v10 offset:57344\n"
    "v_mfma_f32_16x16x32_bf16 v[232:235], v[248:251], v[200:203], v[232:235]\n"
    "s_mov_b32 s43, 223104\n"
    "buffer_load_dwordx4 v6, s[16:19], s43 offen offset:3072 lds\n"
    "v_mfma_f32_16x16x32_bf16 v[236:239], v[248:251], v[204:207], v[236:239]\n"
    "s_waitcnt lgkmcnt(2)\n"
    "v_mfma_f32_16x16x32_bf16 v[208:211], v[240:243], v[16:19], 0\n"
    "ds_read_b64_tr_b16 v[248:249], v10 offset:7168\n"
    "s_add_u32 m0, s42, 0\n"
    "s_mov_b32 s43, 299904\n"
    "buffer_load_dwordx4 v6, s[16:19], s43 offen offset:0 lds\n"
    "v_mfma_f32_16x16x32_bf16 v[212:215], v[240:243], v[20:23], 0\n"
    "ds_read_b64_tr_b16 v[250:251], v10 offset:17408\n"
    "v_mfma_f32_16x16x32_bf16 v[216:219], v[240:243], v[24:27], 0\n"
    "s_mov_b32 s43, 372608\n"
    "buffer_load_dwordx4 v6, s[16:19], s43 offen offset:1024 lds\n"
    "v_mfma_f32_16x16x32_bf16 v[220:223], v[240:243], v[28:31], 0\n"
    "s_waitcnt lgkmcnt(2)\n"
    "v_mfma_f32_16x16x32_bf16 v[208:211], v[244:247], v[32:35], v[208:211]\n"
    "ds_read_b64_tr_b16 v[240:241], v10 offset:48128\n"
    "v_cvt_pk_bf16_f32 v224, v224, v225\n"
    "v_cvt_pk_bf16_f32 v225, v226, v227\n"
    "v_mfma_f32_16x16x32_bf16 v[212:215], v[244:247], v[36:39], v[212:215]\n"
    "ds_read_b64_tr_b16 v[242:243], v10 offset:58368\n"
    "v_cvt_pk_bf16_f32 v226, v228, v229\n"
    "v_mfma_f32_16x16x32_bf16 v[216:219], v[244:247], v[40:43], v[216:219]\n"
    "v_cvt_pk_bf16_f32 v227, v230, v231\n"
    "v_mfma_f32_16x16x32_bf16 v[220:223], v[244:247], v[44:47], v[220:223]\n"
    "v_cvt_pk_bf16_f32 v228, v232, v233\n"
    "s_waitcnt lgkmcnt(2)\n"
    "v_mfma_f32_16x16x32_bf16 v[208:211], v[248:251], v[48:51], v[208:211]\n"
    "ds_read_b64_tr_b16 v[244:245], v10 offset:8192\n"
    "v_cvt_pk_bf16_f32 v229, v234, v235\n"
    "v_mfma_f32_16x16x32_bf16 v[212:215], v[248:251], v[52:55], v[212:215]\n"
    "ds_read_b64_tr_b16 v[246:247], v10 offset:18432\n"
    "v_cvt_pk_bf16_f32 v230, v236, v237\n"
    "v_mfma_f32_16x16x32_bf16 v[216:219], v[248:251], v[56:59], v[216:219]\n"
    "v_cvt_pk_bf16_f32 v231, v238, v239\n"
    "v_mfma_f32_16x16x32_bf16 v[220:223], v[248:251], v[60:63], v[220:223]\n"
    "s_waitcnt lgkmcnt(2)\n"
    "v_mfma_f32_16x16x32_bf16 v[208:211], v[240:243], v[64:67], v[208:211]\n"
    "ds_read_b64_tr_b16 v[248:249], v10 offset:49152\n"
    "s_add_u32 s38, s53, 4608\n"
    "buffer_store_dwordx2 v[252:253], v254, s[48:51], s38 offen sc1\n"
    "v_mfma_f32_16x16x32_bf16 v[212:215], v[240:243], v[68:71], v[212:215]\n"
    "ds_read_b64_tr_b16 v[250:251], v10 offset:59392\n"
    "v_mfma_f32_16x16x32_bf16 v[216:219], v[240:243], v[72:75], v[216:219]\n"
    "s_mov_b32 s38, 20480\n"
    "buffer_store_dwordx4 v[224:227], v12, s[24:27], s38 offen sc1\n"
    "v_mfma_f32_16x16x32_bf16 v[220:223], v[240:243], v[76:79], v[220:223]\n"
    "s_waitcnt lgkmcnt(2)\n"
    "v_mfma_f32_16x16x32_bf16 v[208:211], v[244:247], v[80:83], v[208:211]\n"
    "ds_read_b64_tr_b16 v[240:241], v10 offset:9216\n"
    "s_mov_b32 s38, 57344\n"
    "buffer_store_dwordx4 v[228:231], v12, s[24:27], s38 offen sc1\n"
    "v_mfma_f32_16x16x32_bf16 v[212:215], v[244:247], v[84:87], v[212:215]\n"
    "ds_read_b64_tr_b16 v[242:243], v10 offset:19456\n"
    "v_mfma_f32_16x16x32_bf16 v[216:219], v[244:247], v[88:91], v[216:219]\n"
    "v_mfma_f32_16x16x32_bf16 v[220:223], v[244:247], v[92:95], v[220:223]\n"
    "s_waitcnt lgkmcnt(2)\n"
    "v_mfma_f32_16x16x32_bf16 v[208:211], v[248:251], v[96:99], v[208:211]\n"
    "ds_read_b64_tr_b16 v[244:245], v10 offset:50176\n"
    "v_mfma_f32_16x16x32_bf16 v[212:215], v[248:251], v[100:103], v[212:215]\n"
    "ds_read_b64_tr_b16 v[246:247], v10 offset:60416\n"
    "v_mfma_f32_16x16x32_bf16 v[216:219], v[248:251], v[104:107], v[216:219]\n"
    "v_mfma_f32_16x16x32_bf16 v[220:223], v[248:251], v[108:111], v[220:223]\n"
    "s_waitcnt lgkmcnt(2)\n"
    "v_mfma_f32_16x16x32_bf16 v[208:211], v[240:243], v[112:115], v[208:211]\n"
    "ds_read_b64_tr_b16 v[248:249], v3 offset:2048\n"
    "v_mfma_f32_16x16x32_bf16 v[212:215], v[240:243], v[116:119], v[212:215]\n"
    "ds_read_b64_tr_b16 v[250:251], v3 offset:10240\n"
    "v_mfma_f32_16x16x32_bf16 v[216:219], v[240:243], v[120:123], v[216:219]\n"
    "v_mfma_f32_16x16x32_bf16 v[220:223], v[240:243], v[124:127], v[220:223]\n"
    "s_waitcnt lgkmcnt(2)\n"
    "v_mfma_f32_16x16x32_bf16 v[208:211], v[244:247], v[128:131], v[208:211]\n"
    "ds_read_b64_tr_b16 v[240:241], v3 offset:34816\n"
    "v_mfma_f32_16x16x32_bf16 v[212:215], v[244:247], v[132:135], v[212:215]\n"
    "ds_read_b64_tr_b16 v[242:243], v3 offset:43008\n"
    "v_mfma_f32_16x16x32_bf16 v[216:219], v[244:247], v[136:139], v[216:219]\n"
    "v_mfma_f32_16x16x32_bf16 v[220:223], v[244:247], v[140:143], v[220:223]\n"
    "s_waitcnt lgkmcnt(2)\n"
    "v_mfma_f32_16x16x32_bf16 v[208:211], v[248:251], v[144:147], v[208:211]\n"
    "ds_read_b64_tr_b16 v[244:245], v3 offset:3072\n"
    "v_mfma_f32_16x16x32_bf16 v[212:215], v[248:251], v[148:151], v[212:215]\n"
    "ds_read_b64_tr_b16 v[246:247], v3 offset:11264\n"
    "v_mfma_f32_16x16x32_bf16 v[216:219], v[248:251], v[152:155], v[216:219]\n"
    "v_mfma_f32_16x16x32_bf16 v[220:223], v[248:251], v[156:159], v[220:223]\n"
    "s_waitcnt lgkmcnt(2)\n"
    "v_mfma_f32_16x16x32_bf16 v[208:211], v[240:243], v[160:163], v[208:211]\n"
    "ds_read_b64_tr_b16 v[248:249], v3 offset:35840\n"
    "v_mfma_f32_16x16x32_bf16 v[212:215], v[240:243], v[164:167], v[212:215]\n"
    "ds_read_b64_tr_b16 v[250:251], v3 offset:44032\n"
    "v_mfma_f32_16x16x32_bf16 v[216:219], v[240:243], v[168:171], v[216:219]\n"
    "v_mfma_f32_16x16x32_bf16 v[220:223], v[240:243], v[172:175], v[220:223]\n"
    "s_waitcnt lgkmcnt(2)\n"
    "v_mfma_f32_16x16x32_bf16 v[208:211], v[244:247], v[176:179], v[208:211]\n"
    "s_waitcnt vmcnt(3)\n"
    "s_barrier\n"
    "v_mfma_f32_16x16x32_bf16 v[212:215], v[244:247], v[180:183], v[212:215]\n"
    "ds_read_b64_tr_b16 v[240:241], v3 offset:4096\n"
    "s_add_u32 m0, s47, 0\n"
    "s_mov_b32 s43, 5120\n"
    "buffer_load_dwordx4 v6, s[16:19], s43 offen offset:0 lds\n"
    "v_mfma_f32_16x16x32_bf16 v[216:219], v[244:247], v[184:187], v[216:219]\n"
    "ds_read_b64_tr_b16 v[242:243], v3 offset:12288\n"
    "s_mov_b32 s43, 77824\n"
    "buffer_load_dwordx4 v6, s[16:19], s43 offen offset:1024 lds\n"
    "v_mfma_f32_16x16x32_bf16 v[220:223], v[244:247], v[188:191], v[220:223]\n"
    "s_waitcnt lgkmcnt(2)\n"
    "v_mfma_f32_16x16x32_bf16 v[208:211], v[248:251], v[192:195], v[208:211]\n"
    "ds_read_b64_tr_b16 v[244:245], v3 offset:36864\n"
    "s_mov_b32 s43, 150528\n"
    "buffer_load_dwordx4 v6, s[16:19], s43 offen offset:2048 lds\n"
    "v_mfma_f32_16x16x32_bf16 v[212:215], v[248:251], v[196:199], v[212:215]\n"
    "ds_read_b64_tr_b16 v[246:247], v3 offset:45056\n"
    "v_mfma_f32_16x16x32_bf16 v[216:219], v[248:251], v[200:203], v[216:219]\n"
    "s_mov_b32 s43, 223232\n"
    "buffer_load_dwordx4 v6, s[16:19], s43 offen offset:3072 lds\n"
    "v_mfma_f32_16x16x32_bf16 v[220:223], v[248:251], v[204:207], v[220:223]\n"
    "s_waitcnt lgkmcnt(2)\n"
    "v_mfma_f32_16x16x32_bf16 v[224:227], v[240:243], v[16:19], 0\n"
    "ds_read_b64_tr_b16 v[248:249], v3 offset:5120\n"
    "s_add_u32 m0, s47, 4096\n"
    "s_mov_b32 s43, 300032\n"
    "buffer_load_dwordx4 v6, s[16:19], s43 offen offset:0 lds\n"
    "v_mfma_f32_16x16x32_bf16 v[228:231], v[240:243], v[20:23], 0\n"
    "ds_read_b64_tr_b16 v[250:251], v3 offset:13312\n"
    "v_mfma_f32_16x16x32_bf16 v[232:235], v[240:243], v[24:27], 0\n"
    "s_mov_b32 s43, 372736\n"
    "buffer_load_dwordx4 v6, s[16:19], s43 offen offset:1024 lds\n"
    "v_mfma_f32_16x16x32_bf16 v[236:239], v[240:243], v[28:31], 0\n"
    "s_waitcnt lgkmcnt(2)\n"
    "v_mfma_f32_16x16x32_bf16 v[224:227], v[244:247], v[32:35], v[224:227]\n"
    "ds_read_b64_tr_b16 v[240:241], v3 offset:37888\n"
    "v_cvt_pk_bf16_f32 v208, v208, v209\n"
    "v_cvt_pk_bf16_f32 v209, v210, v211\n"
    "v_mfma_f32_16x16x32_bf16 v[228:231], v[244:247], v[36:39], v[228:231]\n"
    "ds_read_b64_tr_b16 v[242:243], v3 offset:46080\n"
    "v_cvt_pk_bf16_f32 v210, v212, v213\n"
    "v_mfma_f32_16x16x32_bf16 v[232:235], v[244:247], v[40:43], v[232:235]\n"
    "v_cvt_pk_bf16_f32 v211, v214, v215\n"
    "v_mfma_f32_16x16x32_bf16 v[236:239], v[244:247], v[44:47], v[236:239]\n"
    "v_cvt_pk_bf16_f32 v212, v216, v217\n"
    "s_waitcnt lgkmcnt(2)\n"
    "v_mfma_f32_16x16x32_bf16 v[224:227], v[248:251], v[48:51], v[224:227]\n"
    "ds_read_b64_tr_b16 v[244:245], v3 offset:6144\n"
    "v_cvt_pk_bf16_f32 v213, v218, v219\n"
    "v_mfma_f32_16x16x32_bf16 v[228:231], v[248:251], v[52:55], v[228:231]\n"
    "ds_read_b64_tr_b16 v[246:247], v3 offset:14336\n"
    "v_cvt_pk_bf16_f32 v214, v220, v221\n"
    "v_mfma_f32_16x16x32_bf16 v[232:235], v[248:251], v[56:59], v[232:235]\n"
    "v_cvt_pk_bf16_f32 v215, v222, v223\n"
    "v_mfma_f32_16x16x32_bf16 v[236:239], v[248:251], v[60:63], v[236:239]\n"
    "s_waitcnt lgkmcnt(2)\n"
    "v_mfma_f32_16x16x32_bf16 v[224:227], v[240:243], v[64:67], v[224:227]\n"
    "ds_read_b64_tr_b16 v[248:249], v3 offset:38912\n"
    "s_add_u32 s38, s53, 6144\n"
    "buffer_store_dwordx2 v[252:253], v254, s[48:51], s38 offen sc1\n"
    "v_mfma_f32_16x16x32_bf16 v[228:231], v[240:243], v[68:71], v[228:231]\n"
    "ds_read_b64_tr_b16 v[250:251], v3 offset:47104\n"
    "v_mfma_f32_16x16x32_bf16 v[232:235], v[240:243], v[72:75], v[232:235]\n"
    "s_mov_b32 s38, 24576\n"
    "buffer_store_dwordx4 v[208:211], v12, s[24:27], s38 offen sc1\n"
    "v_mfma_f32_16x16x32_bf16 v[236:239], v[240:243], v[76:79], v[236:239]\n"
    "s_waitcnt lgkmcnt(2)\n"
    "v_mfma_f32_16x16x32_bf16 v[224:227], v[244:247], v[80:83], v[224:227]\n"
    "ds_read_b64_tr_b16 v[240:241], v3 offset:7168\n"
    "s_mov_b32 s38, 61440\n"
    "buffer_store_dwordx4 v[212:215], v12, s[24:27], s38 offen sc1\n"
    "v_mfma_f32_16x16x32_bf16 v[228:231], v[244:247], v[84:87], v[228:231]\n"
    "ds_read_b64_tr_b16 v[242:243], v3 offset:15360\n"
    "v_mfma_f32_16x16x32_bf16 v[232:235], v[244:247], v[88:91], v[232:235]\n"
    "v_mfma_f32_16x16x32_bf16 v[236:239], v[244:247], v[92:95], v[236:239]\n"
    "s_waitcnt lgkmcnt(2)\n"
    "v_mfma_f32_16x16x32_bf16 v[224:227], v[248:251], v[96:99], v[224:227]\n"
    "ds_read_b64_tr_b16 v[244:245], v3 offset:39936\n"
    "v_mfma_f32_16x16x32_bf16 v[228:231], v[248:251], v[100:103], v[228:231]\n"
    "ds_read_b64_tr_b16 v[246:247], v3 offset:48128\n"
    "v_mfma_f32_16x16x32_bf16 v[232:235], v[248:251], v[104:107], v[232:235]\n"
    "v_mfma_f32_16x16x32_bf16 v[236:239], v[248:251], v[108:111], v[236:239]\n"
    "s_waitcnt lgkmcnt(2)\n"
    "v_mfma_f32_16x16x32_bf16 v[224:227], v[240:243], v[112:115], v[224:227]\n"
    "ds_read_b64_tr_b16 v[248:249], v3 offset:0\n"
    "v_mfma_f32_16x16x32_bf16 v[228:231], v[240:243], v[116:119], v[228:231]\n"
    "ds_read_b64_tr_b16 v[250:251], v3 offset:8192\n"
    "v_mfma_f32_16x16x32_bf16 v[232:235], v[240:243], v[120:123], v[232:235]\n"
    "v_mfma_f32_16x16x32_bf16 v[236:239], v[240:243], v[124:127], v[236:239]\n"
    "s_waitcnt lgkmcnt(2)\n"
    "v_mfma_f32_16x16x32_bf16 v[224:227], v[244:247], v[128:131], v[224:227]\n"
    "ds_read_b64_tr_b16 v[240:241], v3 offset:32768\n"
    "v_mfma_f32_16x16x32_bf16 v[228:231], v[244:247], v[132:135], v[228:231]\n"
    "ds_read_b64_tr_b16 v[242:243], v3 offset:40960\n"
    "v_mfma_f32_16x16x32_bf16 v[232:235], v[244:247], v[136:139], v[232:235]\n"
    "v_mfma_f32_16x16x32_bf16 v[236:239], v[244:247], v[140:143], v[236:239]\n"
    "s_waitcnt lgkmcnt(2)\n"
    "v_mfma_f32_16x16x32_bf16 v[224:227], v[248:251], v[144:147], v[224:227]\n"
    "ds_read_b64_tr_b16 v[244:245], v3 offset:1024\n"
    "v_mfma_f32_16x16x32_bf16 v[228:231], v[248:251], v[148:151], v[228:231]\n"
    "ds_read_b64_tr_b16 v[246:247], v3 offset:9216\n"
    "v_mfma_f32_16x16x32_bf16 v[232:235], v[248:251], v[152:155], v[232:235]\n"
    "v_mfma_f32_16x16x32_bf16 v[236:239], v[248:251], v[156:159], v[236:239]\n"
    "s_waitcnt lgkmcnt(2)\n"
    "v_mfma_f32_16x16x32_bf16 v[224:227], v[240:243], v[160:163], v[224:227]\n"
    "ds_read_b64_tr_b16 v[248:249], v3 offset:33792\n"
    "v_mfma_f32_16x16x32_bf16 v[228:231], v[240:243], v[164:167], v[228:231]\n"
    "ds_read_b64_tr_b16 v[250:251], v3 offset:41984\n"
    "v_mfma_f32_16x16x32_bf16 v[232:235], v[240:243], v[168:171], v[232:235]\n"
    "v_mfma_f32_16x16x32_bf16 v[236:239], v[240:243], v[172:175], v[236:239]\n"
    "s_waitcnt lgkmcnt(2)\n"
    "v_mfma_f32_16x16x32_bf16 v[224:227], v[244:247], v[176:179], v[224:227]\n"
    "s_waitcnt vmcnt(3)\n"
    "s_barrier\n"
    "v_mfma_f32_16x16x32_bf16 v[228:231], v[244:247], v[180:183], v[228:231]\n"
    "ds_read_b64_tr_b16 v[240:241], v10 offset:0\n"
    "s_add_u32 s38, s53, 7680\n"
    "buffer_store_dwordx2 v[252:253], v254, s[48:51], s38 offen sc1\n"
    "v_mfma_f32_16x16x32_bf16 v[232:235], v[244:247], v[184:187], v[232:235]\n"
    "ds_read_b64_tr_b16 v[242:243], v10 offset:10240\n"
    "v_mfma_f32_16x16x32_bf16 v[236:239], v[244:247], v[188:191], v[236:239]\n"
    "s_waitcnt lgkmcnt(2)\n"
    "v_mfma_f32_16x16x32_bf16 v[224:227], v[248:251], v[192:195], v[224:227]\n"
    "ds_read_b64_tr_b16 v[244:245], v10 offset:40960\n"
    "v_mfma_f32_16x16x32_bf16 v[228:231], v[248:251], v[196:199], v[228:231]\n"
    "ds_read_b64_tr_b16 v[246:247], v10 offset:51200\n"
    "v_mfma_f32_16x16x32_bf16 v[232:235], v[248:251], v[200:203], v[232:235]\n"
    "v_mfma_f32_16x16x32_bf16 v[236:239], v[248:251], v[204:207], v[236:239]\n"
    "s_waitcnt lgkmcnt(2)\n"
    "v_mfma_f32_16x16x32_bf16 v[208:211], v[240:243], v[16:19], 0\n"
    "ds_read_b64_tr_b16 v[248:249], v10 offset:1024\n"
    "v_mfma_f32_16x16x32_bf16 v[212:215], v[240:243], v[20:23], 0\n"
    "ds_read_b64_tr_b16 v[250:251], v10 offset:11264\n"
    "v_mfma_f32_16x16x32_bf16 v[216:219], v[240:243], v[24:27], 0\n"
    "v_mfma_f32_16x16x32_bf16 v[220:223], v[240:243], v[28:31], 0\n"
    "s_waitcnt lgkmcnt(2)\n"
    "v_mfma_f32_16x16x32_bf16 v[208:211], v[244:247], v[32:35], v[208:211]\n"
    "ds_read_b64_tr_b16 v[240:241], v10 offset:41984\n"
    "v_cvt_pk_bf16_f32 v224, v224, v225\n"
    "v_cvt_pk_bf16_f32 v225, v226, v227\n"
    "v_cvt_pk_bf16_f32 v226, v228, v229\n"
    "v_mfma_f32_16x16x32_bf16 v[212:215], v[244:247], v[36:39], v[212:215]\n"
    "ds_read_b64_tr_b16 v[242:243], v10 offset:52224\n"
    "v_cvt_pk_bf16_f32 v227, v230, v231\n"
    "v_mfma_f32_16x16x32_bf16 v[216:219], v[244:247], v[40:43], v[216:219]\n"
    "v_cvt_pk_bf16_f32 v228, v232, v233\n"
    "v_mfma_f32_16x16x32_bf16 v[220:223], v[244:247], v[44:47], v[220:223]\n"
    "v_cvt_pk_bf16_f32 v229, v234, v235\n"
    "s_waitcnt lgkmcnt(2)\n"
    "v_mfma_f32_16x16x32_bf16 v[208:211], v[248:251], v[48:51], v[208:211]\n"
    "ds_read_b64_tr_b16 v[244:245], v10 offset:2048\n"
    "v_cvt_pk_bf16_f32 v230, v236, v237\n"
    "v_mfma_f32_16x16x32_bf16 v[212:215], v[248:251], v[52:55], v[212:215]\n"
    "ds_read_b64_tr_b16 v[246:247], v10 offset:12288\n"
    "v_cvt_pk_bf16_f32 v231, v238, v239\n"
    "v_mfma_f32_16x16x32_bf16 v[216:219], v[248:251], v[56:59], v[216:219]\n"
    "v_mfma_f32_16x16x32_bf16 v[220:223], v[248:251], v[60:63], v[220:223]\n"
    "s_waitcnt lgkmcnt(2)\n"
    "v_mfma_f32_16x16x32_bf16 v[208:211], v[240:243], v[64:67], v[208:211]\n"
    "ds_read_b64_tr_b16 v[248:249], v10 offset:43008\n"
    "s_mov_b32 s38, 28672\n"
    "buffer_store_dwordx4 v[224:227], v12, s[24:27], s38 offen sc1\n"
    "v_mfma_f32_16x16x32_bf16 v[212:215], v[240:243], v[68:71], v[212:215]\n"
    "ds_read_b64_tr_b16 v[250:251], v10 offset:53248\n"
    "s_mov_b32 s38, 65536\n"
    "buffer_store_dwordx4 v[228:231], v12, s[24:27], s38 offen sc1\n"
    "v_mfma_f32_16x16x32_bf16 v[216:219], v[240:243], v[72:75], v[216:219]\n"
    "v_mfma_f32_16x16x32_bf16 v[220:223], v[240:243], v[76:79], v[220:223]\n"
    "s_waitcnt lgkmcnt(2)\n"
    "v_mfma_f32_16x16x32_bf16 v[208:211], v[244:247], v[80:83], v[208:211]\n"
    "ds_read_b64_tr_b16 v[240:241], v10 offset:3072\n"
    "v_mfma_f32_16x16x32_bf16 v[212:215], v[244:247], v[84:87], v[212:215]\n"
    "ds_read_b64_tr_b16 v[242:243], v10 offset:13312\n"
    "v_mfma_f32_16x16x32_bf16 v[216:219], v[244:247], v[88:91], v[216:219]\n"
    "v_mfma_f32_16x16x32_bf16 v[220:223], v[244:247], v[92:95], v[220:223]\n"
    "s_waitcnt lgkmcnt(2)\n"
    "v_mfma_f32_16x16x32_bf16 v[208:211], v[248:251], v[96:99], v[208:211]\n"
    "ds_read_b64_tr_b16 v[244:245], v10 offset:44032\n"
    "v_mfma_f32_16x16x32_bf16 v[212:215], v[248:251], v[100:103], v[212:215]\n"
    "ds_read_b64_tr_b16 v[246:247], v10 offset:54272\n"
    "v_mfma_f32_16x16x32_bf16 v[216:219], v[248:251], v[104:107], v[216:219]\n"
    "v_mfma_f32_16x16x32_bf16 v[220:223], v[248:251], v[108:111], v[220:223]\n"
    "s_waitcnt lgkmcnt(2)\n"
    "v_mfma_f32_16x16x32_bf16 v[208:211], v[240:243], v[112:115], v[208:211]\n"
    "ds_read_b64_tr_b16 v[248:249], v10 offset:4096\n"
    "v_mfma_f32_16x16x32_bf16 v[212:215], v[240:243], v[116:119], v[212:215]\n"
    "ds_read_b64_tr_b16 v[250:251], v10 offset:14336\n"
    "v_mfma_f32_16x16x32_bf16 v[216:219], v[240:243], v[120:123], v[216:219]\n"
    "v_mfma_f32_16x16x32_bf16 v[220:223], v[240:243], v[124:127], v[220:223]\n"
    "s_waitcnt lgkmcnt(2)\n"
    "v_mfma_f32_16x16x32_bf16 v[208:211], v[244:247], v[128:131], v[208:211]\n"
    "ds_read_b64_tr_b16 v[240:241], v10 offset:45056\n"
    "v_mfma_f32_16x16x32_bf16 v[212:215], v[244:247], v[132:135], v[212:215]\n"
    "ds_read_b64_tr_b16 v[242:243], v10 offset:55296\n"
    "v_mfma_f32_16x16x32_bf16 v[216:219], v[244:247], v[136:139], v[216:219]\n"
    "v_mfma_f32_16x16x32_bf16 v[220:223], v[244:247], v[140:143], v[220:223]\n"
    "s_waitcnt lgkmcnt(2)\n"
    "v_mfma_f32_16x16x32_bf16 v[208:211], v[248:251], v[144:147], v[208:211]\n"
    "ds_read_b64_tr_b16 v[244:245], v10 offset:5120\n"
    "v_mfma_f32_16x16x32_bf16 v[212:215], v[248:251], v[148:151], v[212:215]\n"
    "ds_read_b64_tr_b16 v[246:247], v10 offset:15360\n"
    "v_mfma_f32_16x16x32_bf16 v[216:219], v[248:251], v[152:155], v[216:219]\n"
    "v_mfma_f32_16x16x32_bf16 v[220:223], v[248:251], v[156:159], v[220:223]\n"
    "s_waitcnt lgkmcnt(2)\n"
    "v_mfma_f32_16x16x32_bf16 v[208:211], v[240:243], v[160:163], v[208:211]\n"
    "ds_read_b64_tr_b16 v[248:249], v10 offset:46080\n"
    "v_mfma_f32_16x16x32_bf16 v[212:215], v[240:243], v[164:167], v[212:215]\n"
    "ds_read_b64_tr_b16 v[250:251], v10 offset:56320\n"
    "v_mfma_f32_16x16x32_bf16 v[216:219], v[240:243], v[168:171], v[216:219]\n"
    "v_mfma_f32_16x16x32_bf16 v[220:223], v[240:243], v[172:175], v[220:223]\n"
    "s_waitcnt lgkmcnt(2)\n"
    "v_mfma_f32_16x16x32_bf16 v[208:211], v[244:247], v[176:179], v[208:211]\n"
    "v_mfma_f32_16x16x32_bf16 v[212:215], v[244:247], v[180:183], v[212:215]\n"
    "v_mfma_f32_16x16x32_bf16 v[216:219], v[244:247], v[184:187], v[216:219]\n"
    "v_mfma_f32_16x16x32_bf16 v[220:223], v[244:247], v[188:191], v[220:223]\n"
    "s_waitcnt lgkmcnt(0)\n"
    "v_mfma_f32_16x16x32_bf16 v[208:211], v[248:251], v[192:195], v[208:211]\n"
    "v_mfma_f32_16x16x32_bf16 v[212:215], v[248:251], v[196:199], v[212:215]\n"
    "v_mfma_f32_16x16x32_bf16 v[216:219], v[248:251], v[200:203], v[216:219]\n"
    "v_mfma_f32_16x16x32_bf16 v[220:223], v[248:251], v[204:207], v[220:223]\n"
    "s_nop 4\n"
    "v_cvt_pk_bf16_f32 v208, v208, v209\n"
    "v_cvt_pk_bf16_f32 v209, v210, v211\n"
    "v_cvt_pk_bf16_f32 v210, v212, v213\n"
    "v_cvt_pk_bf16_f32 v211, v214, v215\n"
    "v_cvt_pk_bf16_f32 v212, v216, v217\n"
    "v_cvt_pk_bf16_f32 v213, v218, v219\n"
    "v_cvt_pk_bf16_f32 v214, v220, v221\n"
    "v_cvt_pk_bf16_f32 v215, v222, v223\n"
    "s_mov_b32 s38, 32768\n"
    "buffer_store_dwordx4 v[208:211], v12, s[24:27], s38 offen sc1\n"
    "s_mov_b32 s38, 69632\n"
    "buffer_store_dwordx4 v[212:215], v12, s[24:27], s38 offen sc1\n"
    "s_waitcnt vmcnt(0) lgkmcnt(0)\n"
    "s_barrier\n"
    "s_add_u32 s38, s53, 9216\n"
    "buffer_store_dwordx2 v[252:253], v254, s[48:51], s38 offen sc1\n"
    "s_add_u32 s38, s53, 10752\n"
    "buffer_store_dwordx2 v[252:253], v254, s[48:51], s38 offen sc1\n"
    "s_add_u32 s38, s53, 12288\n"
    "buffer_store_dwordx2 v[252:253], v254, s[48:51], s38 offen sc1\n"
    "v_readfirstlane_b32 s55, v252\n"
    "s_nop 3\n"
    "s_mov_b32 %0, s55\n"
        : "=&s"(tag)
        : "s"(px), "s"(pw1), "s"(pw2), "s"(pb1), "s"(pp), "s"(ps), "s"(stn), "s"(pc), "s"(oseen), "s"(opf), "{v0}"(threadIdx.x)
        : "memory", "vcc", "scc",
        "v1", "v2", "v3", "v4", "v5", "v6", "v7", "v8", "v9", "v10", "v11", "v12", "v13", "v14", "v15", "v16", "v17", "v18", "v19", "v20", "v21", "v22", "v23", "v24", "v25", "v26", "v27", "v28", "v29", "v30", "v31", "v32", "v33", "v34", "v35", "v36", "v37", "v38", "v39", "v40", "v41", "v42", "v43", "v44", "v45", "v46", "v47", "v48", "v49", "v50", "v51", "v52", "v53", "v54", "v55", "v56", "v57", "v58", "v59", "v60", "v61", "v62", "v63", "v64", "v65", "v66", "v67", "v68", "v69", "v70", "v71", "v72", "v73", "v74", "v75", "v76", "v77", "v78", "v79", "v80", "v81", "v82", "v83", "v84", "v85", "v86", "v87", "v88", "v89", "v90", "v91", "v92", "v93", "v94", "v95", "v96", "v97", "v98", "v99", "v100", "v101", "v102", "v103", "v104", "v105", "v106", "v107", "v108", "v109", "v110", "v111", "v112", "v113", "v114", "v115", "v116", "v117", "v118", "v119", "v120", "v121", "v122", "v123", "v124", "v125", "v126", "v127", "v128", "v129", "v130", "v131", "v132", "v133", "v134", "v135", "v136", "v137", "v138", "v139", "v140", "v141", "v142", "v143", "v144", "v145", "v146", "v147", "v148", "v149", "v150", "v151", "v152", "v153", "v154", "v155", "v156", "v157", "v158", "v159", "v160", "v161", "v162", "v163", "v164", "v165", "v166", "v167", "v168", "v169", "v170", "v171", "v172", "v173", "v174", "v175", "v176", "v177", "v178", "v179", "v180", "v181", "v182", "v183", "v184", "v185", "v186", "v187", "v188", "v189", "v190", "v191", "v192", "v193", "v194", "v195", "v196", "v197", "v198", "v199", "v200", "v201", "v202", "v203", "v204", "v205", "v206", "v207", "v208", "v209", "v210", "v211", "v212", "v213", "v214", "v215", "v216", "v217", "v218", "v219", "v220", "v221", "v222", "v223", "v224", "v225", "v226", "v227", "v228", "v229", "v230", "v231", "v232", "v233", "v234", "v235", "v236", "v237", "v238", "v239", "v240", "v241", "v242", "v243", "v244", "v245", "v246", "v247", "v248", "v249", "v250", "v251", "v252", "v253", "v254", "v255", "s8", "s9", "s10", "s11", "s12", "s13", "s14", "s15", "s16", "s17", "s18", "s19", "s20", "s21", "s22", "s23", "s24", "s25", "s26", "s27", "s28", "s29", "s30", "s31", "s36", "s37", "s38", "s39", "s40", "s41", "s42", "s43", "s44", "s45", "s46", "s47", "s48", "s49", "s50", "s51", "s52", "s53", "s54", "s55"
    );
    const int w = __builtin_amdgcn_readfirstlane(threadIdx.x >> 6);
    reduce_round8<1>(rb, sp & 3, 6 + (sp >> 2), tag, x, b2, ws, out, (LAS char*)smem + w * 2048, w, threadIdx.x & 63);
    STAMP(14);
  } else {
    conv_wg(8 * xcd + (j - 24), x, kp, vp, skp, gp, Dp, b2, w1, w2, out, (LAS char*)smem, ws);
  }
}

extern "C" void kernel_launch(void* const* d_in, const int* in_sizes, int n_in, void* d_out, int out_size, void* d_ws, size_t ws_size,
                              hipStream_t stream) {
  (void)hipFuncSetAttribute((const void*)kA, hipFuncAttributeMaxDynamicSharedMemorySize, 163840);
  kA<<<NMLP + NCONV, 512, 163840, stream>>>((const u16*)d_in[0], (const u16*)d_in[1], (const u16*)d_in[2], (const u16*)d_in[3], (const u16*)d_in[4],
                                            (const u16*)d_in[5], (const u16*)d_in[7], (const u16*)d_in[8], (const u16*)d_in[9], (const u16*)d_in[10],
                                            (u16*)d_out, (char*)d_ws + WS_SHIFT);
}
```
